```python
import torch

def _softmax(x, axis=-1):
    return torch.softmax(x, dim=axis)
def _gelu(x):
    return torch.nn.functional.gelu(x, approximate="tanh")
def _silu(x):
    return torch.nn.functional.silu(x)
def _sigmoid(x):
    return torch.sigmoid(x)
def _layernorm(x, axis=-1, eps=1e-5):
    m = x.mean(dim=axis, keepdim=True)
    v = x.var(dim=axis, keepdim=True, unbiased=False)
    return (x - m) / torch.sqrt(v + eps)
def _scatter_add(tgt, idx, src, axis=0):
    return tgt.clone().index_add_(axis, idx, src)
def _segment_sum(src, seg, n):
    out = torch.zeros((n,) + src.shape[1:], dtype=src.dtype)
    return out.index_add_(0, seg, src)
def _pad(x, pw):
    flat = tuple(v for pair in reversed(pw) for v in pair)
    return torch.nn.functional.pad(x, flat)

kernel_name = 'sequential_preLN__grouped_kv_mqa__dense_activation_relu__prompt'

def setup_inputs():

    _w = (lambda a: (lambda t: t.to(torch.bfloat16) if t.is_floating_point() else t)(torch.as_tensor(a)))
    def _A_setup():
        B, S, D, H = 1, 2048, 576, 32
        Dh = D // H
        return {
            "x": _w((lambda *s: torch.randn(*s, dtype=torch.bfloat16) * 0.05)(B, S, D)),
            "wq": _w((lambda *s: torch.randn(*s, dtype=torch.bfloat16) * 0.05)(D, H * Dh)),
            "wk": _w((lambda *s: torch.randn(*s, dtype=torch.bfloat16) * 0.05)(D, Dh)),
            "wv": _w((lambda *s: torch.randn(*s, dtype=torch.bfloat16) * 0.05)(D, Dh)),
            "wo": _w((lambda *s: torch.randn(*s, dtype=torch.bfloat16) * 0.05)(H * Dh, D)),
        }
    _A_in = _A_setup()

    def _B_setup():
        B, S, D, Dff = 1, 2048, 576, 4608
        return {
            "x": _w((lambda *s: torch.randn(*s, dtype=torch.bfloat16) * 0.05)(B, S, D)),
            "w1": _w((lambda *s: torch.randn(*s, dtype=torch.bfloat16) * 0.05)(D, Dff)),
            "w2": _w((lambda *s: torch.randn(*s, dtype=torch.bfloat16) * 0.05)(Dff, D)),
        }
    _B_in = _B_setup()

    return {
        "A_x": _A_in["x"],
        "A_wq": _A_in["wq"],
        "A_wk": _A_in["wk"],
        "A_wv": _A_in["wv"],
        "A_wo": _A_in["wo"],
        "B_x": _B_in["x"],
        "B_w1": _B_in["w1"],
        "B_w2": _B_in["w2"],
    }

def reference(A_x, A_wq, A_wk, A_wv, A_wo, B_x, B_w1, B_w2):
    def _A_ref(x, wq, wk, wv, wo):
        B, S, D = x.shape
        H = 32
        Dh = D // H
        q = (lambda x, *d: x.permute(*d))((x @ wq).reshape(B, S, H, Dh), 0, 2, 1, 3)
        k = x @ wk
        v = x @ wv
        scores = torch.einsum("bhqd,bkd->bhqk", q, k) * (Dh ** -0.5)
        p = _softmax(scores, axis=-1)
        o = torch.einsum("bhqk,bkd->bhqd", p, v)
        return (lambda x, *d: x.permute(*d))(o, 0, 2, 1, 3).reshape(B, S, H * Dh) @ wo

    def _B_ref(x, w1, w2):
        h = torch.einsum("bsd,df->bsf", x, w1)
        zero = torch.zeros_like(h)
        h = torch.maximum(h, zero)
        return torch.einsum("bsf,fd->bsd", h, w2)

    x = A_x
    h = x + _A_ref(_layernorm(x, axis=-1), A_wq, A_wk, A_wv, A_wo)
    h2 = h + _B_ref(_layernorm(h, axis=-1), B_w1, B_w2)
    return h2
```

```cpp
#include <hip/hip_runtime.h>
#include <hip/hip_bf16.h>

typedef unsigned short u16;
typedef unsigned u32;
typedef float f32x2 __attribute__((ext_vector_type(2)));
typedef float f32x4 __attribute__((ext_vector_type(4)));
typedef float f32x16 __attribute__((ext_vector_type(16)));
typedef __bf16 bf16x2 __attribute__((ext_vector_type(2)));
typedef __bf16 bf16x8 __attribute__((ext_vector_type(8)));
typedef short s16x4 __attribute__((ext_vector_type(4)));
typedef u32 u32x2 __attribute__((ext_vector_type(2)));
typedef u32 u32x4 __attribute__((ext_vector_type(4)));
typedef u32 u32x4a __attribute__((ext_vector_type(4), aligned(4)));
#define LAS __attribute__((address_space(3)))
#define DI __device__ __forceinline__

constexpr int S = 2048, D = 576, NH = 32, DH = 18, FF = 4608;
constexpr int MT = S / 32;
constexpr int KS = D / 16;
constexpr int KVT = 2368;
constexpr size_t QF0_OFF = 0;
constexpr size_t QF1_OFF = QF0_OFF + (size_t)NH * MT * 1024;
constexpr size_t KV_OFF = QF1_OFF + (size_t)NH * S * 4;
constexpr size_t KMAX_OFF = KV_OFF + (size_t)MT * KVT;
constexpr size_t OF_OFF = KMAX_OFF + 1024;
constexpr size_t HF_OFF = OF_OFF + (size_t)MT * KS * 1024;
constexpr size_t ST_OFF = HF_OFF + (size_t)MT * KS * 1024;
constexpr size_t WOF_OFF = ST_OFF + (size_t)MT * 18 * 256;
constexpr size_t WS_OFF = WOF_OFF + (size_t)18 * KS * 1024;
constexpr size_t CS_OFF = WS_OFF + (size_t)8 * 4 * 324 * 1024;
constexpr size_t PART_OFF = CS_OFF + (size_t)8 * 18 * 128;
constexpr size_t WS_END = PART_OFF + (size_t)8 * MT * KS * 1024;

DI u32 cvtpk(float lo, float hi) { f32x2 v = {lo, hi}; return __builtin_bit_cast(u32, __builtin_convertvector(v, bf16x2)); }
DI float bflo(u32 w) { return __builtin_bit_cast(float, w << 16); }
DI float bfhi(u32 w) { return __builtin_bit_cast(float, w & 0xffff0000u); }
DI float bfround(float x) { return bflo(cvtpk(x, 0.f)); }
DI float dot2(u32 a, u32 b, float c) { return __builtin_amdgcn_fdot2_f32_bf16(__builtin_bit_cast(bf16x2, a), __builtin_bit_cast(bf16x2, b), c, false); }
DI f32x16 mfma(u32x4 a, u32x4 b, f32x16 c) { return __builtin_amdgcn_mfma_f32_32x32x16_bf16(__builtin_bit_cast(bf16x8, a), __builtin_bit_cast(bf16x8, b), c, 0, 0, 0); }
DI u32x4 pack8(const f32x16& a, int s, float sc) {
  u32x4 r;
#pragma unroll
  for (int i = 0; i < 4; ++i) r[i] = cvtpk(a[8 * s + 2 * i] * sc, a[8 * s + 2 * i + 1] * sc);
  return r;
}
DI float xhalf(float v) { return __shfl_xor(v, 32, 64); }
DI u32x4 trread(const char* lds_base, u32 off, u32 step) {
  s16x4 a = __builtin_amdgcn_ds_read_tr16_b64_v4i16((LAS s16x4*)(lds_base + off));
  s16x4 b = __builtin_amdgcn_ds_read_tr16_b64_v4i16((LAS s16x4*)(lds_base + off + step));
  u32x2 x = __builtin_bit_cast(u32x2, a), y = __builtin_bit_cast(u32x2, b);
  return u32x4{x[0], x[1], y[0], y[1]};
}
DI f32x2 ln_scalars(float sum, float sumsq) {
  float mean = sum * (1.f / D);
  float var = fmaxf(sumsq * (1.f / D) - mean * mean, 0.f);
  float sd = bfround(__builtin_amdgcn_sqrtf(bfround(bfround(var) + 1e-5f)));
  return f32x2{bfround(mean), 1.f / sd};
}

constexpr int XROW = 1168, WROW = 192;
constexpr int K1_LDS = 64 * XROW + 288 * WROW;
__global__ __launch_bounds__(512) void k1_qkv(const u16* __restrict__ x, const u16* __restrict__ wq, const u16* __restrict__ wk,
                                              const u16* __restrict__ wv, char* __restrict__ ws) {
  extern __shared__ __attribute__((aligned(16))) char smem[];
  char* XN = smem; char* WL = smem + 64 * XROW;
  const int tid = threadIdx.x, lane = tid & 63, w = __builtin_amdgcn_readfirstlane(tid >> 6);
  const int b = blockIdx.x, mg = (b & 7) * 4 + (b >> 6), hg = (b >> 3) & 7;
  const int xr = 8 * w + (lane >> 3);
  const char* xp = (const char*)x + (size_t)(mg * 64 + xr) * (D * 2) + (lane & 7) * 16;
  u32x4 xv[9];
#pragma unroll
  for (int i = 0; i < 9; ++i) xv[i] = *(const u32x4*)(xp + i * 128);
  u32x4 wm[9]; u32 wl[5]; u32x4 we[3]; u32 wd[2];
  const char* wqb = (const char*)wq + hg * 144;
#pragma unroll
  for (int i = 0; i < 9; ++i) { int e = tid + 512 * i; wm[i] = *(const u32x4a*)(wqb + (e >> 3) * (D * 2) + ((e >> 1) & 3) * 36 + (e & 1) * 16); }
#pragma unroll
  for (int i = 0; i < 5; ++i) { int e = tid + 512 * i; e = e < 2304 ? e : 0; wl[i] = *(const u32*)(wqb + (e >> 2) * (D * 2) + (e & 3) * 36 + 32); }
  const char* wkv = (const char*)(hg < 2 ? wk : wv);
  if (hg < 4) {
#pragma unroll
    for (int i = 0; i < 3; ++i) { int e = tid + 512 * i; e = e < 1152 ? e : 0; we[i] = *(const u32x4a*)(wkv + (e >> 1) * 36 + (e & 1) * 16); }
#pragma unroll
    for (int i = 0; i < 2; ++i) { int e = tid + 512 * i; e = e < 576 ? e : 0; wd[i] = *(const u32*)(wkv + e * 36 + 32); }
  } else {
#pragma unroll
    for (int i = 0; i < 3; ++i) we[i] = u32x4{0, 0, 0, 0};
    wd[0] = wd[1] = 0;
  }
  float sum = 0.f, sq = 0.f;
#pragma unroll
  for (int i = 0; i < 9; ++i)
#pragma unroll
    for (int j = 0; j < 4; ++j) { sum = dot2(xv[i][j], 0x3f803f80u, sum); sq = dot2(xv[i][j], xv[i][j], sq); }
#pragma unroll
  for (int o = 1; o < 8; o <<= 1) { sum += __shfl_xor(sum, o, 64); sq += __shfl_xor(sq, o, 64); }
  const f32x2 ms = ln_scalars(sum, sq);
  const float nb = -ms[0] * ms[1];
#pragma unroll
  for (int i = 0; i < 9; ++i) {
    u32x4 o;
#pragma unroll
    for (int j = 0; j < 4; ++j) o[j] = cvtpk(fmaf(bflo(xv[i][j]), ms[1], nb), fmaf(bfhi(xv[i][j]), ms[1], nb));
    *(u32x4*)(XN + xr * XROW + (lane & 7) * 16 + i * 128) = o;
  }
  const int m = w & 1, T = w >> 1, h = lane >> 5, t32 = lane & 31;
  const u32 xoff = (32 * m + t32) * XROW + h * 16;
  const u32 woff = (8 * h + ((lane & 15) >> 2)) * WROW + (T < 2 ? 64 * T : 128) + ((lane >> 4) & 1) * 32 + (lane & 3) * 8;
  const bool active = T < 3 || hg == 2 + m;
  f32x16 acc = {};
#pragma unroll
  for (int hf = 0; hf < 2; ++hf) {
    if (hf) __syncthreads();
#pragma unroll
    for (int i = 0; i < 9; ++i) { int e = tid + 512 * i, k = (e >> 3) - 288 * hf; if (k >= 0 && k < 288) *(u32x4*)(WL + k * WROW + ((e >> 1) & 3) * 32 + (e & 1) * 16) = wm[i]; }
#pragma unroll
    for (int i = 0; i < 5; ++i) { int e = tid + 512 * i, k = (e >> 2) - 288 * hf; if (k >= 0 && k < 288) *(u32*)(WL + k * WROW + 168 + (e & 3) * 4) = wl[i]; }
#pragma unroll
    for (int i = 0; i < 3; ++i) { int e = tid + 512 * i, k = (e >> 1) - 288 * hf; if (k >= 0 && k < 288) *(u32x4*)(WL + k * WROW + 128 + (e & 1) * 16) = we[i]; }
#pragma unroll
    for (int i = 0; i < 2; ++i) { int e = tid + 512 * i, k = e - 288 * hf; if (k >= 0 && k < 288) *(u32*)(WL + k * WROW + 160) = wd[i]; }
    __syncthreads();
    if (active) {
      if (T < 3) {
#pragma unroll 6
        for (int s = 0; s < 18; ++s) acc = mfma(trread(WL, woff + s * 16 * WROW, 4 * WROW), *(const u32x4*)(XN + xoff + hf * 576 + s * 32), acc);
      } else {
#pragma unroll 6
        for (int s = 0; s < 18; ++s) acc = mfma(*(const u32x4*)(XN + xoff + hf * 576 + s * 32), trread(WL, woff + s * 16 * WROW, 4 * WROW), acc);
      }
    }
  }
  const int mt = 2 * mg + m;
  const float qs = 0.23570226039551584f * 1.4426950408889634f;
  char* kv = ws + KV_OFF + (size_t)mt * KVT;
  if (T < 2) {
    const int hA = 4 * hg + 2 * T;
    *(u32x4*)(ws + QF0_OFF + ((size_t)(hA * MT + mt) * 64 + lane) * 16) = pack8(acc, 0, qs);
    *(u32x4*)(ws + QF0_OFF + ((size_t)((hA + 1) * MT + mt) * 64 + lane) * 16) = pack8(acc, 1, qs);
  } else if (T == 2) {
    const u32 a0 = h ? cvtpk(acc[8] * qs, acc[9] * qs) : cvtpk(acc[12] * qs, acc[13] * qs);
    const u32 a1 = h ? cvtpk(acc[10] * qs, acc[11] * qs) : cvtpk(acc[14] * qs, acc[15] * qs);
    u32* q1 = (u32*)(ws + QF1_OFF) + (size_t)(4 * hg + (h ? 0 : 2)) * S + mt * 32 + t32;
    q1[0] = a0; q1[S] = a1;
    if (hg == m) {
      const u32x4 k0 = pack8(acc, 0, 1.f);
      const u32 k1 = cvtpk(acc[8], acc[9]);
      *(u32x4*)(kv + lane * 16) = k0;
      if (!h) *(u32*)(kv + 1024 + t32 * 4) = k1;
      float n2 = h ? 0.f : dot2(k1, k1, 0.f);
#pragma unroll
      for (int j = 0; j < 4; ++j) n2 = dot2(k0[j], k0[j], n2);
      n2 += xhalf(n2);
#pragma unroll
      for (int o = 1; o < 32; o <<= 1) n2 = fmaxf(n2, __shfl_xor(n2, o, 64));
      if (lane == 0) ((float*)(ws + KMAX_OFF))[mt] = n2;
    }
  } else if (active) {
    if (t32 <= 18) {
      const u32x4 ones = {0x3f803f80u, 0x3f803f80u, 0x3f803f80u, 0x3f803f80u};
      *(u32x4*)(kv + 1152 + h * 304 + t32 * 16) = t32 == 18 ? ones : pack8(acc, 0, 1.f);
      *(u32x4*)(kv + 1152 + 608 + h * 304 + t32 * 16) = t32 == 18 ? ones : pack8(acc, 1, 1.f);
    }
  }
}

DI int perm16(int hh, int j) { return 8 * (j >> 2) + 4 * hh + (j & 3); }
DI void retile_unit(int u, const u16* w1, const u16* w2, const u16* wo, char* ws, char* stage, int lane) {
  const int r = lane >> 2, hh = r >> 3, j = r & 7;
  const char* src; u32 stride; int fs = 0, tl = 0, ph = 0;
  if (u < 144) { fs = u / 18; tl = u % 18; src = (const char*)w1 + u * 64; stride = FF * 2; }
  else if (u < 288) { fs = (u - 144) / 18; tl = (u - 144) % 18; ph = 1; src = (const char*)w2 + (size_t)fs * 576 * (D * 2) + tl * 64; stride = D * 2; }
  else { tl = u - 288; src = (const char*)wo + tl * 64; stride = D * 2; }
  src += (lane & 3) * 16;
  const u32 troff = (8 * (lane >> 5) + ((lane & 15) >> 2)) * 64 + ((lane >> 4) & 1) * 32 + (lane & 3) * 8;
  f32x16 cs = {};
  const u32x4 ones = {0x3f803f80u, 0x3f803f80u, 0x3f803f80u, 0x3f803f80u};
  for (int c = 0; c < 3; ++c) {
    u32x4 v[12];
#pragma unroll
    for (int i = 0; i < 12; ++i) {
      const int ks = 12 * c + i;
      int row = 16 * ks + perm16(hh, j);
      if (u >= 288) row = ks < 32 ? 18 * ks + perm16(hh, j) : 18 * (8 * (ks - 32) + 4 * hh + (j >> 1)) + 16 + (j & 1);
      v[i] = *(const u32x4*)(src + (size_t)row * stride);
    }
#pragma unroll
    for (int i = 0; i < 12; ++i) {
      const int ks = 12 * c + i;
      char* st = stage + (i & 3) * 1024;
      *(u32x4*)(st + lane * 16) = v[i];
      __builtin_amdgcn_fence(__ATOMIC_RELEASE, "wavefront"); __builtin_amdgcn_wave_barrier();
      const u32x4 f = trread(st, troff, 4 * 64);
      __builtin_amdgcn_wave_barrier();
      size_t dst;
      if (u >= 288) dst = WOF_OFF + (size_t)(tl * KS + ks) * 1024;
      else {
        int wv, idx;
        if (tl < 16) { wv = tl >> 2; int sl = ks - 18 * (wv & 1); sl = sl < 0 ? sl + 36 : sl; idx = 18 + 36 * (tl & 3) + sl; }
        else { wv = 2 * (tl - 16) + (ks >= 18); idx = ks >= 18 ? ks - 18 : ks; }
        dst = WS_OFF + (size_t)((fs * 4 + wv) * 324 + ph * 162 + idx) * 1024;
      }
      *(u32x4*)(ws + dst + lane * 16) = f;
      if (u < 144) cs = mfma(f, ones, cs);
    }
  }
  if (u < 144 && (lane & 31) == 0) {
    float* o = (float*)(ws + CS_OFF) + u * 32 + (lane >> 5) * 16;
#pragma unroll
    for (int i = 0; i < 4; ++i) *(f32x4*)(o + 4 * i) = f32x4{cs[4 * i], cs[4 * i + 1], cs[4 * i + 2], cs[4 * i + 3]};
  }
}

constexpr int K2_LDS = 163840;
constexpr int KV_BYTES = MT * KVT;
__global__ __launch_bounds__(640) void k2_attn(const u16* __restrict__ w1, const u16* __restrict__ w2, const u16* __restrict__ wo, char* __restrict__ ws) {
  extern __shared__ __attribute__((aligned(16))) char smem[];
  const int tid = threadIdx.x, lane = tid & 63, w = __builtin_amdgcn_readfirstlane(tid >> 6);
  const int b = blockIdx.x;
  for (int i = tid; i < KV_BYTES / 16; i += 640) *(u32x4*)(smem + i * 16) = *(const u32x4*)(ws + KV_OFF + i * 16);
  __syncthreads();
  if (w >= 8) {
    const int u = 2 * b + (w - 8);
    if (u < 306) retile_unit(u, w1, w2, wo, ws, smem + KV_BYTES + (w - 8) * 4096, lane);
    return;
  }
  const int mg = b >> 3, hg = b & 7, head = 4 * hg + (w >> 1), mt = 2 * mg + (w & 1);
  const int h = lane >> 5, t32 = lane & 31;
  const u32x4 q0 = *(const u32x4*)(ws + QF0_OFF + ((size_t)(head * MT + mt) * 64 + lane) * 16);
  const u32 q1w = h ? 0u : ((const u32*)(ws + QF1_OFF))[(size_t)head * S + mt * 32 + t32];
  float km = ((const float*)(ws + KMAX_OFF))[lane];
#pragma unroll
  for (int o = 1; o < 64; o <<= 1) km = fmaxf(km, __shfl_xor(km, o, 64));
  float qn = dot2(q1w, q1w, 0.f);
#pragma unroll
  for (int j = 0; j < 4; ++j) qn = dot2(q0[j], q0[j], qn);
  qn += xhalf(qn);
  float mx = __builtin_amdgcn_sqrtf(qn * km) * 1.001f;
  const int vd = t32 <= 18 ? t32 : (t32 >= 20 && t32 <= 27 ? 0 : 4);
  const u32 k0off = lane * 16, k1off = 1024 + t32 * 4, voff = 1152 + h * 304 + vd * 16;
  if (!__all(mx <= 40.f)) {
    const u32x4 q1 = {q1w, 0, 0, 0};
    float tm = -INFINITY;
    for (int t = 0; t < MT; ++t) {
      const char* kt = smem + t * KVT;
      f32x16 s = mfma(*(const u32x4*)(kt + k0off), q0, f32x16{});
      s = mfma(u32x4{*(const u32*)(kt + k1off), 0, 0, 0}, q1, s);
#pragma unroll
      for (int i = 0; i < 16; ++i) tm = fmaxf(tm, s[i]);
    }
    mx = fmaxf(tm, xhalf(tm));
  }
  const float mh = bfround(mx);
  const u32x4 q1 = {q1w, h ? 0u : cvtpk(-mh, mh - mx), 0, 0};
  f32x16 o = {};
  for (int t = 0; t < MT; ++t) {
    const char* kt = smem + t * KVT;
    f32x16 s = mfma(*(const u32x4*)(kt + k0off), q0, f32x16{});
    s = mfma(u32x4{*(const u32*)(kt + k1off), 0x3f803f80u, 0, 0}, q1, s);
#pragma unroll
    for (int i = 0; i < 16; ++i) s[i] = __builtin_amdgcn_exp2f(s[i]);
    o = mfma(*(const u32x4*)(kt + voff), pack8(s, 0, 1.f), o);
    o = mfma(*(const u32x4*)(kt + voff + 608), pack8(s, 1, 1.f), o);
  }
  const float l = __shfl(o[10], t32, 64);
  const float inv = 1.f / l;
  char* of = ws + OF_OFF + (size_t)mt * KS * 1024;
  *(u32x4*)(of + head * 1024 + lane * 16) = pack8(o, 0, inv);
  if (!h) *(u32*)(of + (32 + (head >> 3)) * 1024 + ((head >> 2) & 1) * 512 + t32 * 16 + (head & 3) * 4) = cvtpk(o[8] * inv, o[9] * inv);
}

__global__ __launch_bounds__(256) void k3_oproj(const u16* __restrict__ x, char* __restrict__ ws) {
  const int tid = threadIdx.x, lane = tid & 63, w = __builtin_amdgcn_readfirstlane(tid >> 6);
  if (w == 3) return;
  const int b = blockIdx.x, mg = (b & 7) * 4 + ((b >> 3) & 3), ng = b >> 5, nt = 3 * ng + w;
  const int h = lane >> 5, t32 = lane & 31;
  f32x16 a0 = {}, a1 = {};
  const char* wp = ws + WOF_OFF + (size_t)nt * KS * 1024 + lane * 16;
  const char* op = ws + OF_OFF + (size_t)(2 * mg) * KS * 1024 + lane * 16;
#pragma unroll 6
  for (int s = 0; s < KS; ++s) {
    const u32x4 a = *(const u32x4*)(wp + s * 1024);
    a0 = mfma(a, *(const u32x4*)(op + s * 1024), a0);
    a1 = mfma(a, *(const u32x4*)(op + (KS + s) * 1024), a1);
  }
#pragma unroll
  for (int m = 0; m < 2; ++m) {
    f32x16 a = m ? a1 : a0;
    const int mt = 2 * mg + m;
    const char* xp = (const char*)x + (size_t)(mt * 32 + t32) * (D * 2) + nt * 64 + h * 8;
    u32 p[8]; float sum = 0.f, sq = 0.f;
#pragma unroll
    for (int g = 0; g < 4; ++g) {
      const u32x2 xv = *(const u32x2*)(xp + g * 16);
      p[2 * g] = cvtpk(bfround(a[4 * g]) + bflo(xv[0]), bfround(a[4 * g + 1]) + bfhi(xv[0]));
      p[2 * g + 1] = cvtpk(bfround(a[4 * g + 2]) + bflo(xv[1]), bfround(a[4 * g + 3]) + bfhi(xv[1]));
    }
#pragma unroll
    for (int i = 0; i < 8; ++i) { sum = dot2(p[i], 0x3f803f80u, sum); sq = dot2(p[i], p[i], sq); }
    sum += xhalf(sum); sq += xhalf(sq);
    char* hp = ws + HF_OFF + (size_t)(mt * KS + 2 * nt) * 1024 + lane * 16;
    *(u32x4*)hp = u32x4{p[0], p[1], p[2], p[3]};
    *(u32x4*)(hp + 1024) = u32x4{p[4], p[5], p[6], p[7]};
    if (!h) *(f32x2*)(ws + ST_OFF + (size_t)(mt * 18 + nt) * 256 + t32 * 8) = f32x2{sum, sq};
  }
}

constexpr int K4_LDS = 2 * 73728 + 16384;
__global__ __launch_bounds__(256) void k4_ffn(char* __restrict__ ws) {
  extern __shared__ __attribute__((aligned(16))) char smem[];
  char* XB = smem; char* HB = smem + 73728; char* EX = smem + 2 * 73728;
  const int tid = threadIdx.x, lane = tid & 63, w = __builtin_amdgcn_readfirstlane(tid >> 6);
  const int b = blockIdx.x, fs = b & 7, mg = b >> 3, h = lane >> 5, t32 = lane & 31;
  for (int i = tid; i < 73728 / 16; i += 256) *(u32x4*)(XB + i * 16) = *(const u32x4*)(ws + HF_OFF + (size_t)mg * 73728 + i * 16);
  f32x2 ms[2];
#pragma unroll
  for (int m = 0; m < 2; ++m) {
    float sum = 0.f, sq = 0.f;
    for (int i = 0; i < 9; ++i) { f32x2 v = *(const f32x2*)(ws + ST_OFF + (size_t)((2 * mg + m) * 18 + 9 * h + i) * 256 + t32 * 8); sum += v[0]; sq += v[1]; }
    sum += xhalf(sum); sq += xhalf(sq);
    ms[m] = ln_scalars(sum, sq);
  }
  __syncthreads();
  const char* wp = ws + WS_OFF + (size_t)(fs * 4 + w) * 324 * 1024 + lane * 16;
  const int rot = 18 * (w & 1);
  for (int ph = 0; ph < 2; ++ph) {
    const char* B = ph ? HB : XB;
    for (int it = 0; it < 5; ++it) {
      const int tl = it == 0 ? 16 + (w >> 1) : 4 * w + it - 1, n = it == 0 ? 18 : 36;
      f32x16 a[2];
      const float* cs = (const float*)(ws + CS_OFF) + (fs * 18 + tl) * 32 + h * 16;
#pragma unroll
      for (int m = 0; m < 2; ++m)
#pragma unroll
        for (int i = 0; i < 16; ++i) a[m][i] = (ph == 0 && (it > 0 || !(w & 1))) ? -ms[m][0] * cs[i] : 0.f;
      for (int s = 0; s < n; ++s) {
        const u32x4 wf = *(const u32x4*)wp; wp += 1024;
        int ks = s + rot; ks = ks >= 36 ? ks - 36 : ks;
        a[0] = mfma(wf, *(const u32x4*)(B + ks * 1024 + lane * 16), a[0]);
        a[1] = mfma(wf, *(const u32x4*)(B + (KS + ks) * 1024 + lane * 16), a[1]);
      }
      if (it == 0) {
        const int keep = w & 1;
#pragma unroll
        for (int m = 0; m < 2; ++m)
#pragma unroll
          for (int i = 0; i < 8; ++i) ((float*)(EX + w * 4096 + m * 2048))[i * 64 + lane] = keep ? a[m][i] : a[m][8 + i];
        __syncthreads();
#pragma unroll
        for (int m = 0; m < 2; ++m)
#pragma unroll
          for (int i = 0; i < 8; ++i) { float o = ((float*)(EX + (w ^ 1) * 4096 + m * 2048))[i * 64 + lane]; a[m][i] += o; a[m][8 + i] += o; }
        __syncthreads();
      }
#pragma unroll
      for (int m = 0; m < 2; ++m)
#pragma unroll
        for (int s = 0; s < 2; ++s) {
          if (it == 0 && s != (w & 1)) continue;
          if (ph == 0) {
            f32x16 r = a[m];
#pragma unroll
            for (int i = 0; i < 16; ++i) r[i] = fmaxf(r[i], 0.f);
            *(u32x4*)(HB + (m * KS + 2 * tl + s) * 1024 + lane * 16) = pack8(r, s, 1.f);
          } else {
            *(u32x4*)(ws + PART_OFF + ((size_t)(fs * MT + 2 * mg + m) * KS + 2 * tl + s) * 1024 + lane * 16) = pack8(a[m], s, ms[m][1]);
          }
        }
    }
    __syncthreads();
  }
}

__global__ __launch_bounds__(256) void k5_reduce(const char* __restrict__ ws, u16* __restrict__ out) {
  const int tid = threadIdx.x, lane = tid & 63, w = tid >> 6;
  const int fr = blockIdx.x * 4 + w;
  const int mt = fr / KS, ks = fr % KS;
  u32x4 v[9];
  v[8] = *(const u32x4*)(ws + HF_OFF + (size_t)fr * 1024 + lane * 16);
#pragma unroll
  for (int i = 0; i < 8; ++i) v[i] = *(const u32x4*)(ws + PART_OFF + ((size_t)i * MT * KS + fr) * 1024 + lane * 16);
  u32 p[4];
#pragma unroll
  for (int j = 0; j < 4; ++j) {
    float lo = 0.f, hi = 0.f;
#pragma unroll
    for (int i = 0; i < 8; ++i) { lo += bflo(v[i][j]); hi += bfhi(v[i][j]); }
    p[j] = cvtpk(bfround(lo) + bflo(v[8][j]), bfround(hi) + bfhi(v[8][j]));
  }
  auto r0 = __builtin_amdgcn_permlane32_swap(p[0], p[2], false, false);
  auto r1 = __builtin_amdgcn_permlane32_swap(p[1], p[3], false, false);
  *(u32x4*)((char*)out + (size_t)(mt * 32 + (lane & 31)) * (D * 2) + ks * 32 + (lane >> 5) * 16) = u32x4{r0[0], r1[0], r0[1], r1[1]};
}

extern "C" void kernel_launch(void* const* d_in, const int* in_sizes, int n_in, void* d_out, int out_size, void* d_ws, size_t ws_size,
                              hipStream_t stream) {
  const u16* x = (const u16*)d_in[0]; const u16* wq = (const u16*)d_in[1]; const u16* wk = (const u16*)d_in[2];
  const u16* wv = (const u16*)d_in[3]; const u16* wo = (const u16*)d_in[4];
  const u16* w1 = (const u16*)d_in[6]; const u16* w2 = (const u16*)d_in[7];
  char* ws = (char*)d_ws;
  (void)hipFuncSetAttribute((const void*)k1_qkv, hipFuncAttributeMaxDynamicSharedMemorySize, K1_LDS);
  (void)hipFuncSetAttribute((const void*)k2_attn, hipFuncAttributeMaxDynamicSharedMemorySize, K2_LDS);
  (void)hipFuncSetAttribute((const void*)k4_ffn, hipFuncAttributeMaxDynamicSharedMemorySize, K4_LDS);
  k1_qkv<<<256, 512, K1_LDS, stream>>>(x, wq, wk, wv, ws);
  k2_attn<<<256, 640, K2_LDS, stream>>>(w1, w2, wo, ws);
  k3_oproj<<<192, 256, 0, stream>>>(x, ws);
  k4_ffn<<<256, 256, K4_LDS, stream>>>(ws);
  k5_reduce<<<576, 256, 0, stream>>>(ws, (u16*)d_out);
}
```

```cpp
#include <hip/hip_runtime.h>
#include <hip/hip_bf16.h>

constexpr int W_DELAY = 6;
constexpr int X_DELAY = 4;
constexpr int POLL_DELAY = 34;
constexpr int KA_LINES = 183, KB_LINES = 232;
constexpr int KV_FIRST = 4;
constexpr int POLL_DELAY2 = 118;
constexpr int POLL_SLEEP = 4;
typedef unsigned short u16;
typedef unsigned u32;
typedef float f32x2 __attribute__((ext_vector_type(2)));
typedef float f32x4 __attribute__((ext_vector_type(4)));
typedef float f32x16 __attribute__((ext_vector_type(16)));
typedef __bf16 bf16x2 __attribute__((ext_vector_type(2)));
typedef __bf16 bf16x8 __attribute__((ext_vector_type(8)));
typedef short s16x4 __attribute__((ext_vector_type(4)));
typedef u32 u32x2 __attribute__((ext_vector_type(2)));
typedef u32 u32x4 __attribute__((ext_vector_type(4)));
#define LAS __attribute__((address_space(3)))
#define DI __device__ __forceinline__

constexpr int S = 2048, D = 576, FF = 4608;
constexpr int MT = S / 32;
constexpr int KS = D / 16;
constexpr int VR = 20, VH = VR * 16;
constexpr int KVT = 1152 + 4 * VH;
constexpr int KV_BYTES = MT * KVT;
constexpr size_t GEN_OFF = 0;
constexpr size_t FLG_OFF = 1024;
constexpr size_t KV_OFF = FLG_OFF + 65536;
constexpr size_t OF_OFF = KV_OFF + (size_t)MT * 2432;
constexpr size_t HF_OFF = OF_OFF + (size_t)MT * KS * 1024;
constexpr size_t ST_OFF = HF_OFF + (size_t)MT * KS * 1024;
constexpr size_t WOF_OFF = ST_OFF + (size_t)MT * 4608;
constexpr size_t WS_OFF = WOF_OFF + (size_t)18 * KS * 1024;
constexpr size_t CSP_OFF = WS_OFF + (size_t)8 * 4 * 324 * 1024;
constexpr size_t CNT_OFF = CSP_OFF + (size_t)144 * 384;
constexpr size_t PART_OFF = CNT_OFF + (size_t)32 * 512;

DI u32 cvtpk(float lo, float hi) { f32x2 v = {lo, hi}; return __builtin_bit_cast(u32, __builtin_convertvector(v, bf16x2)); }
DI float bflo(u32 w) { return __builtin_bit_cast(float, w << 16); }
DI float bfhi(u32 w) { return __builtin_bit_cast(float, w & 0xffff0000u); }
DI float bfround(float x) { return bflo(cvtpk(x, 0.f)); }
DI float dot2(u32 a, u32 b, float c) { return __builtin_amdgcn_fdot2_f32_bf16(__builtin_bit_cast(bf16x2, a), __builtin_bit_cast(bf16x2, b), c, false); }
DI f32x16 mfma(u32x4 a, u32x4 b, f32x16 c) { return __builtin_amdgcn_mfma_f32_32x32x16_bf16(__builtin_bit_cast(bf16x8, a), __builtin_bit_cast(bf16x8, b), c, 0, 0, 0); }
DI f32x4 mfma16(u32x4 a, u32x4 b, f32x4 c) { return __builtin_amdgcn_mfma_f32_16x16x32_bf16(__builtin_bit_cast(bf16x8, a), __builtin_bit_cast(bf16x8, b), c, 0, 0, 0); }
DI void store16_wt(char* base, u32 off, u32x4 v) {
  asm volatile("global_store_dwordx4 %0, %1, %2 sc1\n\ts_nop 1" :: "v"(off), "v"(v), "s"(base) : "memory");
}
DI u32x4 pack8(const f32x16& a, int s, float sc) {
  u32x4 r;
#pragma unroll
  for (int i = 0; i < 4; ++i) r[i] = cvtpk(a[8 * s + 2 * i] * sc, a[8 * s + 2 * i + 1] * sc);
  return r;
}
DI float xadd(float v) {
  const u32 c = __builtin_bit_cast(u32, v);
  const auto r = __builtin_amdgcn_permlane32_swap(c, c, false, false);
  const u32 r0 = r[0], r1 = r[1];
  return __builtin_bit_cast(float, r0) + __builtin_bit_cast(float, r1);
}
template <int C> DI float dpp(float v) { return __builtin_bit_cast(float, __builtin_amdgcn_update_dpp(0, __builtin_bit_cast(int, v), C, 0xf, 0xf, true)); }
DI int lane_id() { int l; asm volatile("v_mbcnt_lo_u32_b32 %0, -1, 0\n\tv_mbcnt_hi_u32_b32 %0, -1, %0" : "=v"(l)); return l; }
DI float rdl(float v, int l) { return __builtin_bit_cast(float, __builtin_amdgcn_readlane(__builtin_bit_cast(int, v), l)); }
DI float xhalf(float v) { return __shfl_xor(v, 32, 64); }
DI u32x4 trread(const char* lds_base, u32 off, u32 step) {
  s16x4 a = __builtin_amdgcn_ds_read_tr16_b64_v4i16((LAS s16x4*)(lds_base + off));
  s16x4 b = __builtin_amdgcn_ds_read_tr16_b64_v4i16((LAS s16x4*)(lds_base + off + step));
  u32x2 x = __builtin_bit_cast(u32x2, a), y = __builtin_bit_cast(u32x2, b);
  return u32x4{x[0], x[1], y[0], y[1]};
}
DI f32x2 ln_scalars(float sum, float sumsq) {
  float mean = sum * (1.f / D);
  float var = fmaxf(sumsq * (1.f / D) - mean * mean, 0.f);
  float sd = bfround(__builtin_amdgcn_sqrtf(bfround(bfround(var) + 1e-5f)));
  return f32x2{bfround(mean), __builtin_amdgcn_rcpf(sd)};
}

DI void retile_unit(int u, const u16* w1, const u16* w2, const u16* wo, char* ws, char* stage, int lane) {
  u32x4 v[12];
  const u32 lane16 = lane * 16;
  if (u >= 864) {
    const int nt = (u - 864) / 3, c = (u - 864) % 3;
    const int r = lane >> 2, hh = r >> 3, j = r & 7;
    const char* src = (const char*)wo + nt * 64 + (lane & 3) * 16;
#pragma unroll
    for (int i = 0; i < 12; ++i) {
      const int ks = 12 * c + i;
      const int row = ks < 32 ? 18 * ks + 8 * (j >> 2) + 4 * hh + (j & 3) : 18 * (8 * (ks - 32) + 4 * hh + (j >> 1)) + 16 + (j & 1);
      v[i] = *(const u32x4*)(src + row * (D * 2));
    }
    const u32 tro = (8 * (lane >> 5) + ((lane & 15) >> 2)) * 64 + ((lane >> 4) & 1) * 32 + (lane & 3) * 8;
    char* dst = ws + WOF_OFF + (size_t)(nt * KS + 12 * c) * 1024;
#pragma unroll
    for (int i = 0; i < 12; ++i) {
      *(u32x4*)(stage + lane16) = v[i];
      __builtin_amdgcn_wave_barrier();
      const u32x4 f = trread(stage, tro, 256);
      __builtin_amdgcn_wave_barrier();
      store16_wt(dst + i * 1024, lane16, f);
    }
    return;
  }
  const int ph = u >= 432, uu = u - 432 * ph, tile = uu / 3, c = uu % 3, fs = tile / 18, tl = tile % 18;
  const int slot = lane >> 1, g = slot >> 3, j = (slot & 7) ^ (4 * (g & 1)), pc = lane & 1;
  const char* src; u32 kstep, sub;
  if (!ph) { src = (const char*)w1 + tile * 64 + pc * 16 + (size_t)(192 * c + 16 * (g >> 1) + 8 * (j >> 2) + 4 * (g & 1) + (j & 3)) * (FF * 2); kstep = 32 * FF * 2; sub = 32; }
  else { src = (const char*)w2 + tl * 64 + pc * 32 + (size_t)(fs * 576 + 192 * c + 16 * (j >> 2) + 4 * g + (j & 3)) * (D * 2); kstep = 32 * D * 2; sub = 16; }
#pragma unroll
  for (int i = 0; i < 12; i += 2) v[i] = *(const u32x4*)(src + (i >> 1) * kstep);
#pragma unroll
  for (int i = 0; i < 12; i += 2) asm volatile("" : "+v"(v[i]) :: "memory");
#pragma unroll
  for (int i = 1; i < 12; i += 2) v[i] = *(const u32x4*)(src + (i >> 1) * kstep + sub);
  const int lg = lane >> 4;
  const u32 tro = ((8 * lg + ((lane & 15) >> 2)) ^ (4 * (lg & 1))) * 32 + (lane & 3) * 8;
  const int step = (lg & 1) ? -128 : 128;
  f32x4 cs0 = {}, cs1 = {};
  const u32x4 ones = {0x3f803f80u, 0x3f803f80u, 0x3f803f80u, 0x3f803f80u};
#pragma unroll
  for (int i = 0; i < 12; ++i) {
    const int ks = 6 * c + (i >> 1);
    int wv, idx;
    if (tl < 16) { wv = tl >> 2; idx = 36 * (tl & 3) + (ph && (tl & 3) ? 18 : 0) + 2 * ks + (i & 1); }
    else { wv = 2 * (tl - 16) + (i & 1); idx = (ph ? 36 : 144) + ks; }
    *(u32x4*)(stage + lane16) = v[i];
    __builtin_amdgcn_wave_barrier();
    const u32x4 f = trread(stage, tro, step);
    __builtin_amdgcn_wave_barrier();
    store16_wt(ws + WS_OFF + (size_t)((fs * 4 + wv) * 324 + ph * 162 + idx) * 1024, lane16, f);
    if (!ph) { if (i & 1) cs1 = mfma16(f, ones, cs1); else cs0 = mfma16(f, ones, cs0); }
  }
  if (!ph && (lane & 15) == 0) {
    float* o = (float*)(ws + CSP_OFF) + uu * 32 + lg * 8;
    *(f32x4*)o = cs0; *(f32x4*)(o + 4) = cs1;
  }
}

constexpr int KA_LDS = 163840;
constexpr float QS = 0.23570226039551584f * 1.4426950408889634f;
constexpr int K1_W = 73728, K1_KV = K1_W + 576 * 144;
constexpr int KA_FLAG = KA_LDS - 256, K2_STAGE = KV_BYTES;
DI float wait_flags(const char* flg, u32 ta, u32 tb, int lane) {
  float km = 0.f;
  for (int spin = 0; spin < (1 << 20); ++spin) {
    u32x4 f0, f1;
    asm volatile("global_load_dwordx4 %0, %2, %3 sc1\n\tglobal_load_dwordx4 %1, %2, %3 offset:1024 sc1\n\ts_waitcnt vmcnt(0)" : "=&v"(f0), "=&v"(f1) : "v"(lane * 16), "s"(flg) : "memory");
    km = fmaxf(__builtin_bit_cast(float, (u32)f0[1]), __builtin_bit_cast(float, (u32)f1[1]));
    if (__all(f0[0] == ta && f0[2] == tb && f1[0] == ta && f1[2] == tb)) break;
    __builtin_amdgcn_s_sleep(POLL_SLEEP);
  }
  return km;
}
#define LD_OUT() asm volatile("; USE_LD" : "={v[106:107]}"(ldg), "={v[108:111]}"(ld[0]), "={v[112:115]}"(ld[1]), "={v[116:119]}"(ld[2]), "={v[120:123]}"(ld[3]), "={v[124:127]}"(ld[4]), "={v[128:131]}"(ld[5]), "={v[132:135]}"(ld[6]), "={v[136:139]}"(ld[7]), "={v[140:143]}"(ld[8]), "={v[144:147]}"(ld[9]), "={v[148:151]}"(ld[10]), "={v[152:155]}"(ld[11]), "={v[156:159]}"(ld[12]), "={v[160:163]}"(ld[13]), "={v[164:167]}"(ld[14]))
template <bool ISK, int PART> DI float kv_wave(u32x2& gen, char* kv, char* smem, int m, int lane) {
  constexpr int R0 = PART ? 5 : 0, NR = PART ? 4 : 5;
  volatile LAS u32* flags = (volatile LAS u32*)(smem + KA_FLAG);
  const int h = lane >> 5, t32 = lane & 31;
  u32x4 ld[15]; u32x2 ldg;
  LD_OUT();
  char* st = smem + K1_KV + PART * 3072;
  const u32 woff = (8 * h + ((lane & 15) >> 2)) * 48 + ((lane & 16) ? 32 : (lane & 3) * 8);
  u32x4 af[4 * NR];
#pragma unroll
  for (int i = 0; i < NR; ++i) {
    asm volatile("s_waitcnt vmcnt(%3)" : "+v"(ld[3 * i]), "+v"(ld[3 * i + 1]), "+v"(ld[3 * i + 2]) : "i"(3 * (NR - 1 - i) + !PART) : "memory");
    *(u32x4*)(st + lane * 48) = ld[3 * i]; *(u32x4*)(st + lane * 48 + 16) = ld[3 * i + 1]; *(u32*)(st + lane * 48 + 32) = ld[3 * i + 2][3];
    __builtin_amdgcn_wave_barrier();
#pragma unroll
    for (int s = 0; s < 4; ++s) af[4 * i + s] = trread(st, woff + s * 768, 192);
    __builtin_amdgcn_wave_barrier();
  }
  const int xrow = 32 * m + t32, z = (xrow >> 1) & 7;
  const char* xb = smem + xrow * (D * 2) + R0 * 128;
  u32 xo[4];
#pragma unroll
  for (int s = 0; s < 4; ++s) xo[s] = ((2 * s + h) ^ z) * 16;
#pragma unroll
  for (int s = 0; s < 4 * NR; ++s) asm volatile("" : "+v"(af[s]));
  if (!PART) { asm volatile("s_waitcnt vmcnt(0)" : "+v"(ldg)); gen = ldg; flags[26] = gen[0]; flags[27] = gen[1]; flags[28] = 1; }
  while (flags[24] < 256) __builtin_amdgcn_s_sleep(1);
  __builtin_amdgcn_s_setprio(3);
  u32x4 bf[8];
#pragma unroll
  for (int s = 0; s < 8; ++s) bf[s] = *(const u32x4*)(xb + xo[s & 3] + (s >> 2) * 128);
  f32x16 acc = {};
#pragma unroll
  for (int s = 0; s < 4 * NR; ++s) {
    __builtin_amdgcn_sched_barrier(0);
    acc = ISK ? mfma(af[s], bf[s % 8], acc) : mfma(bf[s % 8], af[s], acc);
    if (s + 8 < 4 * NR) bf[s % 8] = *(const u32x4*)(xb + xo[s & 3] + ((s + 8) >> 2) * 128);
  }
  asm volatile("" : "+v"(acc));
  char* ex = smem + K1_KV + 3072 + (ISK ? lane * 16 : (h * 18 + min(t32, 17)) * 16);
  constexpr int NQ = ISK ? 3 : 4, QS_ = ISK ? 1024 : 576;
  if (PART) {
    if (ISK || t32 < 18) {
#pragma unroll
      for (int q = 0; q < NQ; ++q) *(f32x4*)(ex + q * QS_) = f32x4{acc[4 * q], acc[4 * q + 1], acc[4 * q + 2], acc[4 * q + 3]};
    }
    asm volatile("s_waitcnt lgkmcnt(0)" ::: "memory");
    flags[25] = 1;
    return 0.f;
  }
  while (flags[25] == 0) {}
  asm volatile("" ::: "memory");
#pragma unroll
  for (int q = 0; q < NQ; ++q) {
    const f32x4 o = *(const f32x4*)(ex + q * QS_);
#pragma unroll
    for (int i = 0; i < 4; ++i) acc[4 * q + i] += o[i];
  }
  float n2 = 0.f;
  if (ISK) {
    const u32x4 k0 = pack8(acc, 0, 1.f);
    const u32 k4 = cvtpk(acc[8], acc[9]);
    store16_wt(kv, lane * 16, k0);
    if (!h) { asm volatile("global_store_dword %0, %1, %2 offset:1024 sc1" :: "v"(t32 * 4), "v"(k4), "s"(kv) : "memory"); n2 = dot2(k4, k4, 0.f); }
#pragma unroll
    for (int j = 0; j < 4; ++j) n2 = dot2(k0[j], k0[j], n2);
    n2 = xadd(n2);
    n2 = fmaxf(n2, dpp<0xB1>(n2)); n2 = fmaxf(n2, dpp<0x4E>(n2)); n2 = fmaxf(n2, dpp<0x141>(n2)); n2 = fmaxf(n2, dpp<0x140>(n2));
    n2 = fmaxf(n2, __shfl_xor(n2, 16, 64));
  } else {
    const u32 cv = t32 == 18 ? 0x3f803f80u : 0u;
    const u32x4 cst = {cv, cv, cv, cv};
    if (t32 < VR) {
      store16_wt(kv + 1152, h * VH + t32 * 16, t32 >= 18 ? cst : pack8(acc, 0, 1.f));
      store16_wt(kv + 1152 + 2 * VH, h * VH + t32 * 16, t32 >= 18 ? cst : pack8(acc, 1, 1.f));
    }
  }
  return n2;
}
__global__ __launch_bounds__(768, 3) void ka_attn(const u16* __restrict__ x, const u16* __restrict__ wq, const u16* __restrict__ wk, const u16* __restrict__ wv,
                                               const u16* __restrict__ wo, const u16* __restrict__ w1, const u16* __restrict__ w2, char* __restrict__ ws) {
  extern __shared__ __attribute__((aligned(16))) char smem[];
  const int w = __builtin_amdgcn_readfirstlane(threadIdx.x >> 6);
  const int b = blockIdx.x, mg = (b & 7) * 4 + (b >> 6), hg = (b >> 3) & 7;
  const u32 lds0 = (u32)(size_t)smem;
  volatile LAS u32* flags = (volatile LAS u32*)(smem + KA_FLAG);
  char* myflag = ws + FLG_OFF + (hg >> 2) * 32768 + (mg * 4 + (hg & 3)) * 16;
  const char* myrep = ws + FLG_OFF + (b & 15) * 2048;
  if (w == 9) {
    asm volatile("s_getpc_b64 s[20:21]\n\ts_add_u32 s20, s20, _Z7ka_attnPKtS0_S0_S0_S0_S0_S0_Pc@rel32@lo+4\n\ts_addc_u32 s21, s21, _Z7ka_attnPKtS0_S0_S0_S0_S0_S0_Pc@rel32@hi+12\n\t"
                 "global_load_dword v105, %0, s[20:21]" :: "v"(min((b >> 3) * 8 + (lane_id() & 7), KA_LINES - 1) * 128) : "memory", "scc", "s20", "s21", "v105");
  }
  if (w == 8) flags[lane_id()] = 0;
  asm volatile("s_waitcnt lgkmcnt(0)\n\ts_barrier" ::: "memory");
  const bool kvw = w >= 10 && hg < 4;
  if (w < 8 || kvw) {
    const int l = lane_id();
    const int xr = 8 * (w ^ (4 * (hg & 1))) + (l >> 3);
    const char* p = kvw ? (const char*)(hg < 2 ? wk : wv) + (w == 10 ? 5 * 2304 : 0) : (const char*)x + (size_t)mg * 64 * (D * 2);
    if (!kvw && hg < 4) __builtin_amdgcn_s_sleep(KV_FIRST);
    if (w >= 4 && w < 8) __builtin_amdgcn_s_sleep(X_DELAY);
    asm volatile(
        "s_mov_b64 s[20:21], %1\n"
        "global_load_dwordx4 v[108:111], %0, s[20:21] sc1\n"
        "s_add_u32 s20, s20, %2\n"
        "s_addc_u32 s21, s21, 0\n"
        "global_load_dwordx4 v[112:115], %0, s[20:21] sc1\n"
        "s_add_u32 s20, s20, %3\n"
        "s_addc_u32 s21, s21, 0\n"
        "global_load_dwordx4 v[116:119], %0, s[20:21] sc1\n"
        "s_add_u32 s20, s20, %4\n"
        "s_addc_u32 s21, s21, 0\n"
        "global_load_dwordx4 v[120:123], %0, s[20:21] sc1\n"
        "s_add_u32 s20, s20, %2\n"
        "s_addc_u32 s21, s21, 0\n"
        "global_load_dwordx4 v[124:127], %0, s[20:21] sc1\n"
        "s_add_u32 s20, s20, %3\n"
        "s_addc_u32 s21, s21, 0\n"
        "global_load_dwordx4 v[128:131], %0, s[20:21] sc1\n"
        "s_add_u32 s20, s20, %4\n"
        "s_addc_u32 s21, s21, 0\n"
        "global_load_dwordx4 v[132:135], %0, s[20:21] sc1\n"
        "s_add_u32 s20, s20, %2\n"
        "s_addc_u32 s21, s21, 0\n"
        "global_load_dwordx4 v[136:139], %0, s[20:21] sc1\n"
        "s_add_u32 s20, s20, %3\n"
        "s_addc_u32 s21, s21, 0\n"
        "global_load_dwordx4 v[140:143], %0, s[20:21] sc1\n"
        "s_add_u32 s20, s20, %4\n"
        "s_addc_u32 s21, s21, 0\n"
        "s_cmp_lt_u32 %5, 10\n"
        "s_cbranch_scc1 .Lld%=\n"
        "global_load_dwordx4 v[144:147], %0, s[20:21] sc1\n"
        "s_add_u32 s20, s20, %2\n"
        "s_addc_u32 s21, s21, 0\n"
        "global_load_dwordx4 v[148:151], %0, s[20:21] sc1\n"
        "s_add_u32 s20, s20, %3\n"
        "s_addc_u32 s21, s21, 0\n"
        "global_load_dwordx4 v[152:155], %0, s[20:21] sc1\n"
        "s_add_u32 s20, s20, %4\n"
        "s_addc_u32 s21, s21, 0\n"
        "s_cmp_lt_u32 %5, 13\n"
        "s_cbranch_scc1 .Lld%=\n"
        "global_load_dwordx4 v[156:159], %0, s[20:21] sc1\n"
        "s_add_u32 s20, s20, %2\n"
        "s_addc_u32 s21, s21, 0\n"
        "global_load_dwordx4 v[160:163], %0, s[20:21] sc1\n"
        "s_add_u32 s20, s20, %3\n"
        "s_addc_u32 s21, s21, 0\n"
        "global_load_dwordx4 v[164:167], %0, s[20:21] sc1\n"
        "v_mov_b32 v106, 0\n"
        "global_load_dwordx2 v[106:107], v106, %6\n"
        ".Lld%=:\n"
        :: "v"(kvw ? l * 36 : xr * (D * 2) + (l & 7) * 16), "s"(p), "s"(kvw ? 16 : 128), "s"(kvw ? 4 : 128), "s"(kvw ? 2284 : 128), "s"(kvw ? (w == 11 ? 15 : 12) : 9), "s"(ws + GEN_OFF)
        : "memory", "scc", "s20", "s21", "v106", "v107", "v108","v109","v110","v111","v112","v113","v114","v115","v116","v117","v118","v119","v120","v121","v122","v123","v124","v125","v126","v127","v128","v129","v130","v131","v132","v133","v134","v135","v136","v137","v138","v139","v140","v141","v142","v143","v144","v145","v146","v147","v148","v149","v150","v151","v152","v153","v154","v155","v156","v157","v158","v159","v160","v161","v162","v163","v164","v165","v166","v167");
  }
  if (w >= 8) {
    const int r = w - 8;
    int lane = lane_id();
    u32x2 gen = {0, 0};
    float km = 0.f;
    if (r >= 2) {
      if (hg < 4) {
        char* kv = ws + KV_OFF + (size_t)(2 * mg + (hg & 1)) * KVT;
        if (r == 2) {
          if (hg < 2) kv_wave<true, 1>(gen, kv, smem, hg & 1, lane); else kv_wave<false, 1>(gen, kv, smem, hg & 1, lane);
        } else {
          const float n2 = hg < 2 ? kv_wave<true, 0>(gen, kv, smem, hg & 1, lane) : kv_wave<false, 0>(gen, kv, smem, hg & 1, lane);
          asm volatile("s_waitcnt vmcnt(0)" ::: "memory");
          if (lane < 16) store16_wt(myflag, lane * 2048, u32x4{gen[0] + 1, __builtin_bit_cast(u32, n2), gen[1] ^ 0x9e3779b9u, 0});
        }
      } else { asm volatile("; LD_NONE"); }
    } else {
      asm volatile("; LD_NONE");
      const char* wqb = (const char*)wq + hg * 144;
      __builtin_amdgcn_s_sleep(W_DELAY);
      const u32 lrow = (lane * 7282) >> 16, lo = lrow * (D * 2 - 144) + lane * 16;
      u32 tv;
      asm volatile(
          "s_mov_b64 s[20:21], %2\n"
          "s_mov_b32 m0, %3\n"
          "s_bitset0_b64 exec, 63\n"
          "global_load_lds_dwordx4 %1, s[20:21]\n"
          "s_add_u32 s20, s20, 16128\n"
          "s_addc_u32 s21, s21, 0\n"
          "s_add_u32 m0, m0, 2016\n"
          "global_load_lds_dwordx4 %1, s[20:21]\n"
          "s_add_u32 s20, s20, 16128\n"
          "s_addc_u32 s21, s21, 0\n"
          "s_add_u32 m0, m0, 2016\n"
          "global_load_lds_dwordx4 %1, s[20:21]\n"
          "s_add_u32 s20, s20, 16128\n"
          "s_addc_u32 s21, s21, 0\n"
          "s_add_u32 m0, m0, 2016\n"
          "global_load_lds_dwordx4 %1, s[20:21]\n"
          "s_add_u32 s20, s20, 16128\n"
          "s_addc_u32 s21, s21, 0\n"
          "s_add_u32 m0, m0, 2016\n"
          "global_load_lds_dwordx4 %1, s[20:21]\n"
          "s_add_u32 s20, s20, 16128\n"
          "s_addc_u32 s21, s21, 0\n"
          "s_add_u32 m0, m0, 2016\n"
          "global_load_lds_dwordx4 %1, s[20:21]\n"
          "s_add_u32 s20, s20, 16128\n"
          "s_addc_u32 s21, s21, 0\n"
          "s_add_u32 m0, m0, 2016\n"
          "global_load_lds_dwordx4 %1, s[20:21]\n"
          "s_add_u32 s20, s20, 16128\n"
          "s_addc_u32 s21, s21, 0\n"
          "s_add_u32 m0, m0, 2016\n"
          "global_load_lds_dwordx4 %1, s[20:21]\n"
          "s_add_u32 s20, s20, 16128\n"
          "s_addc_u32 s21, s21, 0\n"
          "s_add_u32 m0, m0, 2016\n"
          "global_load_lds_dwordx4 %1, s[20:21]\n"
          "s_add_u32 s20, s20, 16128\n"
          "s_addc_u32 s21, s21, 0\n"
          "s_add_u32 m0, m0, 2016\n"
          "global_load_lds_dwordx4 %1, s[20:21]\n"
          "s_add_u32 s20, s20, 16128\n"
          "s_addc_u32 s21, s21, 0\n"
          "s_add_u32 m0, m0, 2016\n"
          "global_load_lds_dwordx4 %1, s[20:21]\n"
          "s_add_u32 s20, s20, 16128\n"
          "s_addc_u32 s21, s21, 0\n"
          "s_add_u32 m0, m0, 2016\n"
          "global_load_lds_dwordx4 %1, s[20:21]\n"
          "s_add_u32 s20, s20, 16128\n"
          "s_addc_u32 s21, s21, 0\n"
          "s_add_u32 m0, m0, 2016\n"
          "global_load_lds_dwordx4 %1, s[20:21]\n"
          "s_add_u32 s20, s20, 16128\n"
          "s_addc_u32 s21, s21, 0\n"
          "s_add_u32 m0, m0, 2016\n"
          "global_load_lds_dwordx4 %1, s[20:21]\n"
          "s_add_u32 s20, s20, 16128\n"
          "s_addc_u32 s21, s21, 0\n"
          "s_add_u32 m0, m0, 2016\n"
          "global_load_lds_dwordx4 %1, s[20:21]\n"
          "s_add_u32 s20, s20, 16128\n"
          "s_addc_u32 s21, s21, 0\n"
          "s_add_u32 m0, m0, 2016\n"
          "global_load_lds_dwordx4 %1, s[20:21]\n"
          "s_add_u32 s20, s20, 16128\n"
          "s_addc_u32 s21, s21, 0\n"
          "s_add_u32 m0, m0, 2016\n"
          "global_load_lds_dwordx4 %1, s[20:21]\n"
          "s_add_u32 s20, s20, 16128\n"
          "s_addc_u32 s21, s21, 0\n"
          "s_add_u32 m0, m0, 2016\n"
          "global_load_lds_dwordx4 %1, s[20:21]\n"
          "s_add_u32 s20, s20, 16128\n"
          "s_addc_u32 s21, s21, 0\n"
          "s_add_u32 m0, m0, 2016\n"
          "global_load_lds_dwordx4 %1, s[20:21]\n"
          "s_add_u32 s20, s20, 16128\n"
          "s_addc_u32 s21, s21, 0\n"
          "s_add_u32 m0, m0, 2016\n"
          "v_mov_b32 %0, 1\n"
          "s_waitcnt vmcnt(12)\n"
          "ds_write_b32 %4, %0\n"
          "global_load_lds_dwordx4 %1, s[20:21]\n"
          "s_add_u32 s20, s20, 16128\n"
          "s_addc_u32 s21, s21, 0\n"
          "s_add_u32 m0, m0, 2016\n"
          "global_load_lds_dwordx4 %1, s[20:21]\n"
          "s_add_u32 s20, s20, 16128\n"
          "s_addc_u32 s21, s21, 0\n"
          "s_add_u32 m0, m0, 2016\n"
          "global_load_lds_dwordx4 %1, s[20:21]\n"
          "s_add_u32 s20, s20, 16128\n"
          "s_addc_u32 s21, s21, 0\n"
          "s_add_u32 m0, m0, 2016\n"
          "global_load_lds_dwordx4 %1, s[20:21]\n"
          "s_add_u32 s20, s20, 16128\n"
          "s_addc_u32 s21, s21, 0\n"
          "s_add_u32 m0, m0, 2016\n"
          "global_load_lds_dwordx4 %1, s[20:21]\n"
          "s_add_u32 s20, s20, 16128\n"
          "s_addc_u32 s21, s21, 0\n"
          "s_add_u32 m0, m0, 2016\n"
          "global_load_lds_dwordx4 %1, s[20:21]\n"
          "s_add_u32 s20, s20, 16128\n"
          "s_addc_u32 s21, s21, 0\n"
          "s_add_u32 m0, m0, 2016\n"
          "global_load_lds_dwordx4 %1, s[20:21]\n"
          "s_add_u32 s20, s20, 16128\n"
          "s_addc_u32 s21, s21, 0\n"
          "s_add_u32 m0, m0, 2016\n"
          "v_mov_b32 %0, 2\n"
          "s_waitcnt vmcnt(12)\n"
          "ds_write_b32 %4, %0\n"
          "global_load_lds_dwordx4 %1, s[20:21]\n"
          "s_add_u32 s20, s20, 16128\n"
          "s_addc_u32 s21, s21, 0\n"
          "s_add_u32 m0, m0, 2016\n"
          "global_load_lds_dwordx4 %1, s[20:21]\n"
          "s_add_u32 s20, s20, 16128\n"
          "s_addc_u32 s21, s21, 0\n"
          "s_add_u32 m0, m0, 2016\n"
          "global_load_lds_dwordx4 %1, s[20:21]\n"
          "s_add_u32 s20, s20, 16128\n"
          "s_addc_u32 s21, s21, 0\n"
          "s_add_u32 m0, m0, 2016\n"
          "global_load_lds_dwordx4 %1, s[20:21]\n"
          "s_add_u32 s20, s20, 16128\n"
          "s_addc_u32 s21, s21, 0\n"
          "s_add_u32 m0, m0, 2016\n"
          "global_load_lds_dwordx4 %1, s[20:21]\n"
          "s_add_u32 s20, s20, 16128\n"
          "s_addc_u32 s21, s21, 0\n"
          "s_add_u32 m0, m0, 2016\n"
          "global_load_lds_dwordx4 %1, s[20:21]\n"
          "s_add_u32 s20, s20, 16128\n"
          "s_addc_u32 s21, s21, 0\n"
          "s_add_u32 m0, m0, 2016\n"
          "global_load_lds_dwordx4 %1, s[20:21]\n"
          "s_add_u32 s20, s20, 16128\n"
          "s_addc_u32 s21, s21, 0\n"
          "s_add_u32 m0, m0, 2016\n"
          "v_mov_b32 %0, 3\n"
          "s_waitcnt vmcnt(12)\n"
          "ds_write_b32 %4, %0\n"
          "global_load_lds_dwordx4 %1, s[20:21]\n"
          "s_add_u32 s20, s20, 16128\n"
          "s_addc_u32 s21, s21, 0\n"
          "s_add_u32 m0, m0, 2016\n"
          "global_load_lds_dwordx4 %1, s[20:21]\n"
          "s_add_u32 s20, s20, 16128\n"
          "s_addc_u32 s21, s21, 0\n"
          "s_add_u32 m0, m0, 2016\n"
          "global_load_lds_dwordx4 %1, s[20:21]\n"
          "s_add_u32 s20, s20, 16128\n"
          "s_addc_u32 s21, s21, 0\n"
          "s_add_u32 m0, m0, 2016\n"
          "global_load_lds_dwordx4 %1, s[20:21]\n"
          "s_add_u32 s20, s20, 16128\n"
          "s_addc_u32 s21, s21, 0\n"
          "s_add_u32 m0, m0, 2016\n"
          "global_load_lds_dwordx4 %1, s[20:21]\n"
          "s_add_u32 s20, s20, 16128\n"
          "s_addc_u32 s21, s21, 0\n"
          "s_add_u32 m0, m0, 2016\n"
          "global_load_lds_dwordx4 %1, s[20:21]\n"
          "s_add_u32 s20, s20, 16128\n"
          "s_addc_u32 s21, s21, 0\n"
          "s_add_u32 m0, m0, 2016\n"
          "global_load_lds_dwordx4 %1, s[20:21]\n"
          "s_add_u32 s20, s20, 16128\n"
          "s_addc_u32 s21, s21, 0\n"
          "s_add_u32 m0, m0, 2016\n"
          "v_mov_b32 %0, 4\n"
          "s_waitcnt vmcnt(12)\n"
          "ds_write_b32 %4, %0\n"
          "global_load_lds_dwordx4 %1, s[20:21]\n"
          "s_mov_b64 s[20:21], %5\n"
          "s_mov_b32 m0, %6\n"
          "s_mov_b64 exec, 0x3ffff\n"
          "global_load_lds_dwordx4 %1, s[20:21]\n"
          "s_mov_b64 exec, -1\n"
          "v_mov_b32 %0, 5\n"
          "s_waitcnt vmcnt(7)\n"
          "ds_write_b32 %4, %0\n"
          "v_mov_b32 %0, 6\n"
          "s_waitcnt vmcnt(0)\n"
          "ds_write_b32 %4, %0\n"
          "s_setprio 0\n"
          : "=&v"(tv) : "v"(lo), "s"(wqb + r * 7 * D * 2), "s"(lds0 + K1_W + r * 1008), "v"(lds0 + KA_FLAG + (16 + r) * 4),
            "s"(wqb + 82 * 7 * D * 2), "s"(lds0 + K1_W + 82 * 1008) : "memory", "scc", "s20", "s21");
    }
    if (r == 2) {
      __builtin_amdgcn_s_setprio(0);
      if (hg >= 4) { gen = *(const u32x2*)(ws + GEN_OFF); __builtin_amdgcn_s_sleep(POLL_DELAY2); }
      else { __builtin_amdgcn_s_sleep(POLL_DELAY); while (!flags[28]) __builtin_amdgcn_s_sleep(1); gen = u32x2{flags[26], flags[27]}; }
      km = wait_flags(myrep, gen[0] + 1, gen[1] ^ 0x9e3779b9u, lane_id());
    }
    __builtin_amdgcn_s_setprio(3);
    asm volatile("s_waitcnt lgkmcnt(0)\n\ts_barrier" ::: "memory");
    if (r == 2) {
      km = fmaxf(km, dpp<0xB1>(km)); km = fmaxf(km, dpp<0x4E>(km)); km = fmaxf(km, dpp<0x141>(km)); km = fmaxf(km, dpp<0x140>(km));
      km = fmaxf(fmaxf(rdl(km, 0), rdl(km, 16)), fmaxf(rdl(km, 32), rdl(km, 48)));
      flags[20] = __builtin_bit_cast(u32, km) | 0x80000000u;
    }
    lane = lane_id();
    u32 tmp;
    const size_t src = (size_t)(ws + KV_OFF + r * 1024);
    asm volatile(
      "s_add_u32 s20, %2, 0\n"
      "s_addc_u32 s21, %3, 0\n"
      "s_add_u32 m0, %4, 0\n"
      "s_nop 0\n"
      "global_load_lds_dwordx4 %1, s[20:21] sc1\n"
      "s_add_u32 s20, %2, 77824\n"
      "s_addc_u32 s21, %3, 0\n"
      "s_add_u32 m0, %4, 77824\n"
      "s_nop 0\n"
      "global_load_lds_dwordx4 %1, s[20:21] sc1\n"
      "s_add_u32 s20, %2, 4096\n"
      "s_addc_u32 s21, %3, 0\n"
      "s_add_u32 m0, %4, 4096\n"
      "s_nop 0\n"
      "global_load_lds_dwordx4 %1, s[20:21] sc1\n"
      "s_add_u32 s20, %2, 81920\n"
      "s_addc_u32 s21, %3, 0\n"
      "s_add_u32 m0, %4, 81920\n"
      "s_nop 0\n"
      "global_load_lds_dwordx4 %1, s[20:21] sc1\n"
      "s_waitcnt vmcnt(2)\n"
      "v_mov_b32 %0, 1\n"
      "ds_write_b32 %5, %0\n"
      "s_waitcnt vmcnt(0)\n"
      "v_mov_b32 %0, 2\n"
      "ds_write_b32 %5, %0\n"
      "s_add_u32 s20, %2, 8192\n"
      "s_addc_u32 s21, %3, 0\n"
      "s_add_u32 m0, %4, 8192\n"
      "s_nop 0\n"
      "global_load_lds_dwordx4 %1, s[20:21] sc1\n"
      "s_add_u32 s20, %2, 86016\n"
      "s_addc_u32 s21, %3, 0\n"
      "s_add_u32 m0, %4, 86016\n"
      "s_nop 0\n"
      "global_load_lds_dwordx4 %1, s[20:21] sc1\n"
      "s_add_u32 s20, %2, 12288\n"
      "s_addc_u32 s21, %3, 0\n"
      "s_add_u32 m0, %4, 12288\n"
      "s_nop 0\n"
      "global_load_lds_dwordx4 %1, s[20:21] sc1\n"
      "s_add_u32 s20, %2, 90112\n"
      "s_addc_u32 s21, %3, 0\n"
      "s_add_u32 m0, %4, 90112\n"
      "s_nop 0\n"
      "global_load_lds_dwordx4 %1, s[20:21] sc1\n"
      "s_add_u32 s20, %2, 16384\n"
      "s_addc_u32 s21, %3, 0\n"
      "s_add_u32 m0, %4, 16384\n"
      "s_nop 0\n"
      "global_load_lds_dwordx4 %1, s[20:21] sc1\n"
      "s_add_u32 s20, %2, 94208\n"
      "s_addc_u32 s21, %3, 0\n"
      "s_add_u32 m0, %4, 94208\n"
      "s_nop 0\n"
      "global_load_lds_dwordx4 %1, s[20:21] sc1\n"
      "s_add_u32 s20, %2, 20480\n"
      "s_addc_u32 s21, %3, 0\n"
      "s_add_u32 m0, %4, 20480\n"
      "s_nop 0\n"
      "global_load_lds_dwordx4 %1, s[20:21] sc1\n"
      "s_add_u32 s20, %2, 98304\n"
      "s_addc_u32 s21, %3, 0\n"
      "s_add_u32 m0, %4, 98304\n"
      "s_nop 0\n"
      "global_load_lds_dwordx4 %1, s[20:21] sc1\n"
      "s_waitcnt vmcnt(2)\n"
      "v_mov_b32 %0, 3\n"
      "ds_write_b32 %5, %0\n"
      "s_add_u32 s20, %2, 24576\n"
      "s_addc_u32 s21, %3, 0\n"
      "s_add_u32 m0, %4, 24576\n"
      "s_nop 0\n"
      "global_load_lds_dwordx4 %1, s[20:21] sc1\n"
      "s_add_u32 s20, %2, 102400\n"
      "s_addc_u32 s21, %3, 0\n"
      "s_add_u32 m0, %4, 102400\n"
      "s_nop 0\n"
      "global_load_lds_dwordx4 %1, s[20:21] sc1\n"
      "s_add_u32 s20, %2, 28672\n"
      "s_addc_u32 s21, %3, 0\n"
      "s_add_u32 m0, %4, 28672\n"
      "s_nop 0\n"
      "global_load_lds_dwordx4 %1, s[20:21] sc1\n"
      "s_add_u32 s20, %2, 106496\n"
      "s_addc_u32 s21, %3, 0\n"
      "s_add_u32 m0, %4, 106496\n"
      "s_nop 0\n"
      "global_load_lds_dwordx4 %1, s[20:21] sc1\n"
      "s_add_u32 s20, %2, 32768\n"
      "s_addc_u32 s21, %3, 0\n"
      "s_add_u32 m0, %4, 32768\n"
      "s_nop 0\n"
      "global_load_lds_dwordx4 %1, s[20:21] sc1\n"
      "s_add_u32 s20, %2, 110592\n"
      "s_addc_u32 s21, %3, 0\n"
      "s_add_u32 m0, %4, 110592\n"
      "s_nop 0\n"
      "global_load_lds_dwordx4 %1, s[20:21] sc1\n"
      "s_waitcnt vmcnt(4)\n"
      "v_mov_b32 %0, 4\n"
      "ds_write_b32 %5, %0\n"
      "s_add_u32 s20, %2, 36864\n"
      "s_addc_u32 s21, %3, 0\n"
      "s_add_u32 m0, %4, 36864\n"
      "s_nop 0\n"
      "global_load_lds_dwordx4 %1, s[20:21] sc1\n"
      "s_add_u32 s20, %2, 114688\n"
      "s_addc_u32 s21, %3, 0\n"
      "s_add_u32 m0, %4, 114688\n"
      "s_nop 0\n"
      "global_load_lds_dwordx4 %1, s[20:21] sc1\n"
      "s_add_u32 s20, %2, 40960\n"
      "s_addc_u32 s21, %3, 0\n"
      "s_add_u32 m0, %4, 40960\n"
      "s_nop 0\n"
      "global_load_lds_dwordx4 %1, s[20:21] sc1\n"
      "s_add_u32 s20, %2, 118784\n"
      "s_addc_u32 s21, %3, 0\n"
      "s_add_u32 m0, %4, 118784\n"
      "s_nop 0\n"
      "global_load_lds_dwordx4 %1, s[20:21] sc1\n"
      "s_waitcnt vmcnt(4)\n"
      "v_mov_b32 %0, 5\n"
      "ds_write_b32 %5, %0\n"
      "s_add_u32 s20, %2, 45056\n"
      "s_addc_u32 s21, %3, 0\n"
      "s_add_u32 m0, %4, 45056\n"
      "s_nop 0\n"
      "global_load_lds_dwordx4 %1, s[20:21] sc1\n"
      "s_add_u32 s20, %2, 122880\n"
      "s_addc_u32 s21, %3, 0\n"
      "s_add_u32 m0, %4, 122880\n"
      "s_nop 0\n"
      "global_load_lds_dwordx4 %1, s[20:21] sc1\n"
      "s_add_u32 s20, %2, 49152\n"
      "s_addc_u32 s21, %3, 0\n"
      "s_add_u32 m0, %4, 49152\n"
      "s_nop 0\n"
      "global_load_lds_dwordx4 %1, s[20:21] sc1\n"
      "s_add_u32 s20, %2, 126976\n"
      "s_addc_u32 s21, %3, 0\n"
      "s_add_u32 m0, %4, 126976\n"
      "s_nop 0\n"
      "global_load_lds_dwordx4 %1, s[20:21] sc1\n"
      "s_add_u32 s20, %2, 53248\n"
      "s_addc_u32 s21, %3, 0\n"
      "s_add_u32 m0, %4, 53248\n"
      "s_nop 0\n"
      "global_load_lds_dwordx4 %1, s[20:21] sc1\n"
      "s_add_u32 s20, %2, 131072\n"
      "s_addc_u32 s21, %3, 0\n"
      "s_add_u32 m0, %4, 131072\n"
      "s_nop 0\n"
      "global_load_lds_dwordx4 %1, s[20:21] sc1\n"
      "s_waitcnt vmcnt(4)\n"
      "v_mov_b32 %0, 6\n"
      "ds_write_b32 %5, %0\n"
      "s_add_u32 s20, %2, 57344\n"
      "s_addc_u32 s21, %3, 0\n"
      "s_add_u32 m0, %4, 57344\n"
      "s_nop 0\n"
      "global_load_lds_dwordx4 %1, s[20:21] sc1\n"
      "s_add_u32 s20, %2, 135168\n"
      "s_addc_u32 s21, %3, 0\n"
      "s_add_u32 m0, %4, 135168\n"
      "s_nop 0\n"
      "global_load_lds_dwordx4 %1, s[20:21] sc1\n"
      "s_add_u32 s20, %2, 61440\n"
      "s_addc_u32 s21, %3, 0\n"
      "s_add_u32 m0, %4, 61440\n"
      "s_nop 0\n"
      "global_load_lds_dwordx4 %1, s[20:21] sc1\n"
      "s_add_u32 s20, %2, 139264\n"
      "s_addc_u32 s21, %3, 0\n"
      "s_add_u32 m0, %4, 139264\n"
      "s_nop 0\n"
      "global_load_lds_dwordx4 %1, s[20:21] sc1\n"
      "s_waitcnt vmcnt(4)\n"
      "v_mov_b32 %0, 7\n"
      "ds_write_b32 %5, %0\n"
      "s_add_u32 s20, %2, 65536\n"
      "s_addc_u32 s21, %3, 0\n"
      "s_add_u32 m0, %4, 65536\n"
      "s_nop 0\n"
      "global_load_lds_dwordx4 %1, s[20:21] sc1\n"
      "s_add_u32 s20, %2, 143360\n"
      "s_addc_u32 s21, %3, 0\n"
      "s_add_u32 m0, %4, 143360\n"
      "s_nop 0\n"
      "global_load_lds_dwordx4 %1, s[20:21] sc1\n"
      "s_add_u32 s20, %2, 69632\n"
      "s_addc_u32 s21, %3, 0\n"
      "s_add_u32 m0, %4, 69632\n"
      "s_nop 0\n"
      "global_load_lds_dwordx4 %1, s[20:21] sc1\n"
      "s_add_u32 s20, %2, 147456\n"
      "s_addc_u32 s21, %3, 0\n"
      "s_add_u32 m0, %4, 147456\n"
      "s_nop 0\n"
      "global_load_lds_dwordx4 %1, s[20:21] sc1\n"
      "s_add_u32 s20, %2, 73728\n"
      "s_addc_u32 s21, %3, 0\n"
      "s_add_u32 m0, %4, 73728\n"
      "s_nop 0\n"
      "global_load_lds_dwordx4 %1, s[20:21] sc1\n"
      "s_add_u32 s20, %2, 151552\n"
      "s_addc_u32 s21, %3, 0\n"
      "s_add_u32 m0, %4, 151552\n"
      "s_nop 0\n"
      "global_load_lds_dwordx4 %1, s[20:21] sc1\n"
      "s_waitcnt vmcnt(4)\n"
      "v_mov_b32 %0, 8\n"
      "ds_write_b32 %5, %0\n"
      "s_waitcnt vmcnt(0)\n"
      "v_mov_b32 %0, 9\n"
      "ds_write_b32 %5, %0\n"
      "v_mov_b32 %0, 10\n"
      "ds_write_b32 %5, %0\n"
        : "=&v"(tmp) : "v"(lane * 16), "s"((u32)src), "s"((u32)(src >> 32)), "s"(lds0 + r * 1024), "v"(lds0 + KA_FLAG + r * 4) : "memory", "scc", "s20", "s21");
    const int gi = mg * 4 + (hg & 3);
    const int u = r < 3 ? r * 256 + b : (hg < 4 ? (gi < 96 ? 768 + gi : -1) : (gi < 54 ? 864 + gi : -1));
    if (u >= 0) retile_unit(u, w1, w2, wo, ws, smem + K2_STAGE + r * 1024, lane);
    if (r == 3) {
      if (hg >= 4) {
        if (gi >= 64 && gi < 96) *(u32x2*)(ws + CNT_OFF + (gi - 64) * 512 + lane * 8) = u32x2{0, 0};
      }
      if (hg >= 4) gen = *(const u32x2*)(ws + GEN_OFF);
      const u32 ta = gen[0] + 1, tb = gen[1] ^ 0x9e3779b9u;
      asm volatile("s_waitcnt vmcnt(0)" ::: "memory");
      if (hg >= 4 && lane < 16) store16_wt(myflag, lane * 2048, u32x4{ta, 0, tb, 0});
      wait_flags(myrep + 32768, ta, tb, lane);
      if (b == 0 && lane == 0) __hip_atomic_store((u32*)(ws + GEN_OFF), ta, __ATOMIC_RELAXED, __HIP_MEMORY_SCOPE_AGENT);
      u32 d0, d1, d2, d3;
      asm volatile("global_load_dword %0, %4, %6\n\tglobal_load_dword %1, %4, %6 offset:128\n\tglobal_load_dword %2, %4, %6 offset:256\n\t"
                   "s_getpc_b64 s[20:21]\n\ts_add_u32 s20, s20, _Z6kb_ffnPKtPcPt@rel32@lo+4\n\ts_addc_u32 s21, s21, _Z6kb_ffnPKtPcPt@rel32@hi+12\n\t"
                   "global_load_dword %3, %5, s[20:21]"
                   : "=&v"(d0), "=&v"(d1), "=&v"(d2), "=&v"(d3)
                   : "v"(min(lane, 53) * 384), "v"(min((b >> 3) * 8 + (lane & 7), KB_LINES - 1) * 128), "s"(ws + WOF_OFF + (b >> 3) * 20736) : "memory", "scc", "s20", "s21");
    }
    return;
  }
  const int lane = lane_id(), h = lane >> 5, t32 = lane & 31;
  {
    const int m = w & 1, T = w >> 1;
    const int xr = 8 * (w ^ (4 * (hg & 1))) + (lane >> 3);
    u32x4 ld[15], (&xv)[15] = ld; u32x2 ldg;
    LD_OUT();
    float sum = 0.f, sq = 0.f, sum2 = 0.f, sq2 = 0.f;
#pragma unroll
    for (int i = 0; i < 9; ++i) {
      asm volatile("s_waitcnt vmcnt(%1)" : "+v"(xv[i]) : "i"(8 - i));
      sum = dot2(xv[i][0], 0x3f803f80u, sum); sq = dot2(xv[i][0], xv[i][0], sq); sum2 = dot2(xv[i][1], 0x3f803f80u, sum2); sq2 = dot2(xv[i][1], xv[i][1], sq2);
      sum = dot2(xv[i][2], 0x3f803f80u, sum); sq = dot2(xv[i][2], xv[i][2], sq); sum2 = dot2(xv[i][3], 0x3f803f80u, sum2); sq2 = dot2(xv[i][3], xv[i][3], sq2);
    }
    sum += sum2; sq += sq2;
    sum += dpp<0xB1>(sum); sq += dpp<0xB1>(sq);
    sum += dpp<0x4E>(sum); sq += dpp<0x4E>(sq);
    sum += dpp<0x141>(sum); sq += dpp<0x141>(sq);
    const f32x2 ms = ln_scalars(sum, sq);
    const float nb = -ms[0] * ms[1];
    char* xw = smem + xr * (D * 2) + (((lane & 7) ^ ((xr >> 1) & 7)) * 16);
#pragma unroll
    for (int i = 0; i < 9; ++i) {
      u32x4 o;
#pragma unroll
      for (int j = 0; j < 4; ++j) o[j] = cvtpk(fmaf(bflo(xv[i][j]), ms[1], nb), fmaf(bfhi(xv[i][j]), ms[1], nb));
      *(u32x4*)(xw + i * 128) = o;
    }
    const int xrow = 32 * m + t32, z = (xrow >> 1) & 7;
    const char* xb = smem + xrow * (D * 2);
    u32 xo[4];
#pragma unroll
    for (int i = 0; i < 4; ++i) xo[i] = ((2 * i + h) ^ z) * 16;
    const u32 col = T < 2 ? T * 72 + ((lane >> 4) & 1) * 40 + (lane & 3) * 8 : (lane & 1 ? 104 : 32);
    const u32 woff = K1_W + (8 * h + ((lane & 15) >> 2)) * 144 + col;
    f32x16 acc;
    auto arrive = [&](int word) {
      asm volatile("s_waitcnt lgkmcnt(0)" ::: "memory");
      __hip_atomic_fetch_add((LAS u32*)(smem + KA_FLAG) + word, 1u, __ATOMIC_RELAXED, __HIP_MEMORY_SCOPE_WORKGROUP);
    };
    auto await = [&](int word) { while (flags[word] < 512) __builtin_amdgcn_s_sleep(1); asm volatile("" ::: "memory"); };
    if (w < 4) arrive(24);
    arrive(23); await(23);
    {
      const u32 xa = lds0 + xrow * (D * 2);
      u32 sp;
      asm volatile(
          "v_mov_b32 v96, %9\n"
          "v_mov_b32 v97, 1\n"
          "s_mov_b32 %1, 0\n"
          "s_cmp_eq_u32 %8, 3\n"
          "s_cbranch_scc1 .Lq3%=\n"
          "s_cmp_lt_u32 %1, 1\n"
          "s_cbranch_scc1 .Lqp1_%=\n"
          ".Lqr1_%=:\n"
          "ds_read_b64_tr_b16 v[64:65], %2 offset:0\n"
          "ds_read_b64_tr_b16 v[66:67], %2 offset:576\n"
          "ds_read_b128 v[68:71], %4 offset:0\n"
          "ds_read_b64_tr_b16 v[72:73], %2 offset:2304\n"
          "ds_read_b64_tr_b16 v[74:75], %2 offset:2880\n"
          "ds_read_b128 v[76:79], %5 offset:0\n"
          "ds_read_b64_tr_b16 v[80:81], %2 offset:4608\n"
          "ds_read_b64_tr_b16 v[82:83], %2 offset:5184\n"
          "ds_read_b128 v[84:87], %6 offset:0\n"
          "ds_read_b64_tr_b16 v[88:89], %2 offset:6912\n"
          "ds_read_b64_tr_b16 v[90:91], %2 offset:7488\n"
          "ds_read_b128 v[92:95], %7 offset:0\n"
          "s_cmp_lt_u32 %1, 2\n"
          "s_cbranch_scc1 .Lqp2_%=\n"
          ".Lqr2_%=:\n"
          "s_waitcnt lgkmcnt(9)\n"
          "v_mfma_f32_32x32x16_bf16 %0, v[64:67], v[68:71], 0\n"
          "ds_read_b64_tr_b16 v[64:65], %2 offset:9216\n"
          "ds_read_b64_tr_b16 v[66:67], %2 offset:9792\n"
          "ds_read_b128 v[68:71], %4 offset:128\n"
          "s_waitcnt lgkmcnt(9)\n"
          "v_mfma_f32_32x32x16_bf16 %0, v[72:75], v[76:79], %0\n"
          "ds_read_b64_tr_b16 v[72:73], %2 offset:11520\n"
          "ds_read_b64_tr_b16 v[74:75], %2 offset:12096\n"
          "ds_read_b128 v[76:79], %5 offset:128\n"
          "s_waitcnt lgkmcnt(9)\n"
          "v_mfma_f32_32x32x16_bf16 %0, v[80:83], v[84:87], %0\n"
          "ds_read_b64_tr_b16 v[80:81], %2 offset:13824\n"
          "ds_read_b64_tr_b16 v[82:83], %2 offset:14400\n"
          "ds_read_b128 v[84:87], %6 offset:128\n"
          "s_waitcnt lgkmcnt(9)\n"
          "v_mfma_f32_32x32x16_bf16 %0, v[88:91], v[92:95], %0\n"
          "ds_read_b64_tr_b16 v[88:89], %2 offset:16128\n"
          "ds_read_b64_tr_b16 v[90:91], %2 offset:16704\n"
          "ds_read_b128 v[92:95], %7 offset:128\n"
          "s_waitcnt lgkmcnt(9)\n"
          "v_mfma_f32_32x32x16_bf16 %0, v[64:67], v[68:71], %0\n"
          "ds_read_b64_tr_b16 v[64:65], %2 offset:18432\n"
          "ds_read_b64_tr_b16 v[66:67], %2 offset:19008\n"
          "ds_read_b128 v[68:71], %4 offset:256\n"
          "s_waitcnt lgkmcnt(9)\n"
          "v_mfma_f32_32x32x16_bf16 %0, v[72:75], v[76:79], %0\n"
          "ds_read_b64_tr_b16 v[72:73], %2 offset:20736\n"
          "ds_read_b64_tr_b16 v[74:75], %2 offset:21312\n"
          "ds_read_b128 v[76:79], %5 offset:256\n"
          "s_waitcnt lgkmcnt(9)\n"
          "v_mfma_f32_32x32x16_bf16 %0, v[80:83], v[84:87], %0\n"
          "ds_read_b64_tr_b16 v[80:81], %2 offset:23040\n"
          "ds_read_b64_tr_b16 v[82:83], %2 offset:23616\n"
          "ds_read_b128 v[84:87], %6 offset:256\n"
          "s_waitcnt lgkmcnt(9)\n"
          "v_mfma_f32_32x32x16_bf16 %0, v[88:91], v[92:95], %0\n"
          "ds_read_b64_tr_b16 v[88:89], %2 offset:25344\n"
          "ds_read_b64_tr_b16 v[90:91], %2 offset:25920\n"
          "ds_read_b128 v[92:95], %7 offset:256\n"
          "s_cmp_lt_u32 %1, 3\n"
          "s_cbranch_scc1 .Lqp3_%=\n"
          ".Lqr3_%=:\n"
          "s_waitcnt lgkmcnt(9)\n"
          "v_mfma_f32_32x32x16_bf16 %0, v[64:67], v[68:71], %0\n"
          "ds_read_b64_tr_b16 v[64:65], %2 offset:27648\n"
          "ds_read_b64_tr_b16 v[66:67], %2 offset:28224\n"
          "ds_read_b128 v[68:71], %4 offset:384\n"
          "s_waitcnt lgkmcnt(9)\n"
          "v_mfma_f32_32x32x16_bf16 %0, v[72:75], v[76:79], %0\n"
          "ds_read_b64_tr_b16 v[72:73], %2 offset:29952\n"
          "ds_read_b64_tr_b16 v[74:75], %2 offset:30528\n"
          "ds_read_b128 v[76:79], %5 offset:384\n"
          "s_waitcnt lgkmcnt(9)\n"
          "v_mfma_f32_32x32x16_bf16 %0, v[80:83], v[84:87], %0\n"
          "ds_read_b64_tr_b16 v[80:81], %2 offset:32256\n"
          "ds_read_b64_tr_b16 v[82:83], %2 offset:32832\n"
          "ds_read_b128 v[84:87], %6 offset:384\n"
          "s_waitcnt lgkmcnt(9)\n"
          "v_mfma_f32_32x32x16_bf16 %0, v[88:91], v[92:95], %0\n"
          "ds_read_b64_tr_b16 v[88:89], %2 offset:34560\n"
          "ds_read_b64_tr_b16 v[90:91], %2 offset:35136\n"
          "ds_read_b128 v[92:95], %7 offset:384\n"
          "ds_add_u32 v96, v97 offset:84\n"
          "s_cmp_lt_u32 %1, 4\n"
          "s_cbranch_scc1 .Lqp4_%=\n"
          ".Lqr4_%=:\n"
          "s_waitcnt lgkmcnt(9)\n"
          "v_mfma_f32_32x32x16_bf16 %0, v[64:67], v[68:71], %0\n"
          "ds_read_b64_tr_b16 v[64:65], %2 offset:36864\n"
          "ds_read_b64_tr_b16 v[66:67], %2 offset:37440\n"
          "ds_read_b128 v[68:71], %4 offset:512\n"
          "s_waitcnt lgkmcnt(9)\n"
          "v_mfma_f32_32x32x16_bf16 %0, v[72:75], v[76:79], %0\n"
          "ds_read_b64_tr_b16 v[72:73], %2 offset:39168\n"
          "ds_read_b64_tr_b16 v[74:75], %2 offset:39744\n"
          "ds_read_b128 v[76:79], %5 offset:512\n"
          "s_waitcnt lgkmcnt(9)\n"
          "v_mfma_f32_32x32x16_bf16 %0, v[80:83], v[84:87], %0\n"
          "ds_read_b64_tr_b16 v[80:81], %2 offset:41472\n"
          "ds_read_b64_tr_b16 v[82:83], %2 offset:42048\n"
          "ds_read_b128 v[84:87], %6 offset:512\n"
          "s_waitcnt lgkmcnt(9)\n"
          "v_mfma_f32_32x32x16_bf16 %0, v[88:91], v[92:95], %0\n"
          "ds_read_b64_tr_b16 v[88:89], %2 offset:43776\n"
          "ds_read_b64_tr_b16 v[90:91], %2 offset:44352\n"
          "ds_read_b128 v[92:95], %7 offset:512\n"
          "s_cmp_eq_u32 %8, 2\n"
          "s_cbranch_scc1 .Lq2%=\n"
          "s_waitcnt lgkmcnt(9)\n"
          "v_mfma_f32_32x32x16_bf16 %0, v[64:67], v[68:71], %0\n"
          "ds_read_b64_tr_b16 v[64:65], %3 offset:0\n"
          "ds_read_b64_tr_b16 v[66:67], %3 offset:576\n"
          "ds_read_b128 v[68:71], %4 offset:640\n"
          "s_waitcnt lgkmcnt(9)\n"
          "v_mfma_f32_32x32x16_bf16 %0, v[72:75], v[76:79], %0\n"
          "ds_read_b64_tr_b16 v[72:73], %3 offset:2304\n"
          "ds_read_b64_tr_b16 v[74:75], %3 offset:2880\n"
          "ds_read_b128 v[76:79], %5 offset:640\n"
          "s_waitcnt lgkmcnt(9)\n"
          "v_mfma_f32_32x32x16_bf16 %0, v[80:83], v[84:87], %0\n"
          "ds_read_b64_tr_b16 v[80:81], %3 offset:4608\n"
          "ds_read_b64_tr_b16 v[82:83], %3 offset:5184\n"
          "ds_read_b128 v[84:87], %6 offset:640\n"
          "s_waitcnt lgkmcnt(9)\n"
          "v_mfma_f32_32x32x16_bf16 %0, v[88:91], v[92:95], %0\n"
          "ds_read_b64_tr_b16 v[88:89], %3 offset:6912\n"
          "ds_read_b64_tr_b16 v[90:91], %3 offset:7488\n"
          "ds_read_b128 v[92:95], %7 offset:640\n"
          "s_waitcnt lgkmcnt(9)\n"
          "v_mfma_f32_32x32x16_bf16 %0, v[64:67], v[68:71], %0\n"
          ".Lq3r%=:\n"
          "s_cmp_lt_u32 %1, 5\n"
          "s_cbranch_scc1 .Lqp5_%=\n"
          ".Lqr5_%=:\n"
          "ds_read_b64_tr_b16 v[64:65], %3 offset:9216\n"
          "ds_read_b64_tr_b16 v[66:67], %3 offset:9792\n"
          "ds_read_b128 v[68:71], %4 offset:768\n"
          "s_waitcnt lgkmcnt(9)\n"
          "v_mfma_f32_32x32x16_bf16 %0, v[72:75], v[76:79], %0\n"
          "ds_read_b64_tr_b16 v[72:73], %3 offset:11520\n"
          "ds_read_b64_tr_b16 v[74:75], %3 offset:12096\n"
          "ds_read_b128 v[76:79], %5 offset:768\n"
          "s_waitcnt lgkmcnt(9)\n"
          "v_mfma_f32_32x32x16_bf16 %0, v[80:83], v[84:87], %0\n"
          "ds_read_b64_tr_b16 v[80:81], %3 offset:13824\n"
          "ds_read_b64_tr_b16 v[82:83], %3 offset:14400\n"
          "ds_read_b128 v[84:87], %6 offset:768\n"
          "s_waitcnt lgkmcnt(9)\n"
          "v_mfma_f32_32x32x16_bf16 %0, v[88:91], v[92:95], %0\n"
          "ds_read_b64_tr_b16 v[88:89], %3 offset:16128\n"
          "ds_read_b64_tr_b16 v[90:91], %3 offset:16704\n"
          "ds_read_b128 v[92:95], %7 offset:768\n"
          "s_cmp_lt_u32 %1, 6\n"
          "s_cbranch_scc1 .Lqp6_%=\n"
          ".Lqr6_%=:\n"
          "s_waitcnt lgkmcnt(9)\n"
          "v_mfma_f32_32x32x16_bf16 %0, v[64:67], v[68:71], %0\n"
          "ds_read_b64_tr_b16 v[64:65], %3 offset:18432\n"
          "ds_read_b64_tr_b16 v[66:67], %3 offset:19008\n"
          "ds_read_b128 v[68:71], %4 offset:896\n"
          "s_waitcnt lgkmcnt(9)\n"
          "v_mfma_f32_32x32x16_bf16 %0, v[72:75], v[76:79], %0\n"
          "ds_read_b64_tr_b16 v[72:73], %3 offset:20736\n"
          "ds_read_b64_tr_b16 v[74:75], %3 offset:21312\n"
          "ds_read_b128 v[76:79], %5 offset:896\n"
          "s_waitcnt lgkmcnt(9)\n"
          "v_mfma_f32_32x32x16_bf16 %0, v[80:83], v[84:87], %0\n"
          "ds_read_b64_tr_b16 v[80:81], %3 offset:23040\n"
          "ds_read_b64_tr_b16 v[82:83], %3 offset:23616\n"
          "ds_read_b128 v[84:87], %6 offset:896\n"
          "s_waitcnt lgkmcnt(9)\n"
          "v_mfma_f32_32x32x16_bf16 %0, v[88:91], v[92:95], %0\n"
          "ds_read_b64_tr_b16 v[88:89], %3 offset:25344\n"
          "ds_read_b64_tr_b16 v[90:91], %3 offset:25920\n"
          "ds_read_b128 v[92:95], %7 offset:896\n"
          "s_waitcnt lgkmcnt(9)\n"
          "v_mfma_f32_32x32x16_bf16 %0, v[64:67], v[68:71], %0\n"
          "ds_read_b64_tr_b16 v[64:65], %3 offset:27648\n"
          "ds_read_b64_tr_b16 v[66:67], %3 offset:28224\n"
          "ds_read_b128 v[68:71], %4 offset:1024\n"
          "s_waitcnt lgkmcnt(9)\n"
          "v_mfma_f32_32x32x16_bf16 %0, v[72:75], v[76:79], %0\n"
          "ds_read_b64_tr_b16 v[72:73], %3 offset:29952\n"
          "ds_read_b64_tr_b16 v[74:75], %3 offset:30528\n"
          "ds_read_b128 v[76:79], %5 offset:1024\n"
          "s_waitcnt lgkmcnt(9)\n"
          "v_mfma_f32_32x32x16_bf16 %0, v[80:83], v[84:87], %0\n"
          "ds_read_b64_tr_b16 v[80:81], %3 offset:32256\n"
          "ds_read_b64_tr_b16 v[82:83], %3 offset:32832\n"
          "ds_read_b128 v[84:87], %6 offset:1024\n"
          "s_waitcnt lgkmcnt(9)\n"
          "v_mfma_f32_32x32x16_bf16 %0, v[88:91], v[92:95], %0\n"
          "ds_read_b64_tr_b16 v[88:89], %3 offset:34560\n"
          "ds_read_b64_tr_b16 v[90:91], %3 offset:35136\n"
          "ds_read_b128 v[92:95], %7 offset:1024\n"
          "s_waitcnt lgkmcnt(9)\n"
          "v_mfma_f32_32x32x16_bf16 %0, v[64:67], v[68:71], %0\n"
          "s_waitcnt lgkmcnt(6)\n"
          "v_mfma_f32_32x32x16_bf16 %0, v[72:75], v[76:79], %0\n"
          "s_waitcnt lgkmcnt(3)\n"
          "v_mfma_f32_32x32x16_bf16 %0, v[80:83], v[84:87], %0\n"
          "s_waitcnt lgkmcnt(0)\n"
          "v_mfma_f32_32x32x16_bf16 %0, v[88:91], v[92:95], %0\n"
          "s_branch .Lqz%=\n"
          ".Lq2%=:\n"
          "s_waitcnt lgkmcnt(9)\n"
          "v_mfma_f32_32x32x16_bf16 %0, v[64:67], v[68:71], %0\n"
          "s_waitcnt lgkmcnt(6)\n"
          "v_mfma_f32_32x32x16_bf16 %0, v[72:75], v[76:79], %0\n"
          "s_waitcnt lgkmcnt(3)\n"
          "v_mfma_f32_32x32x16_bf16 %0, v[80:83], v[84:87], %0\n"
          "s_waitcnt lgkmcnt(0)\n"
          "v_mfma_f32_32x32x16_bf16 %0, v[88:91], v[92:95], %0\n"
          "s_branch .Lqz%=\n"
          ".Lq3%=:\n"
          "ds_add_u32 v96, v97 offset:84\n"
          "s_cmp_lt_u32 %1, 4\n"
          "s_cbranch_scc1 .Lqp7_%=\n"
          ".Lqr7_%=:\n"
          "ds_read_b64_tr_b16 v[64:65], %3 offset:0\n"
          "ds_read_b64_tr_b16 v[66:67], %3 offset:576\n"
          "ds_read_b128 v[68:71], %4 offset:640\n"
          "ds_read_b64_tr_b16 v[72:73], %3 offset:2304\n"
          "ds_read_b64_tr_b16 v[74:75], %3 offset:2880\n"
          "ds_read_b128 v[76:79], %5 offset:640\n"
          "ds_read_b64_tr_b16 v[80:81], %3 offset:4608\n"
          "ds_read_b64_tr_b16 v[82:83], %3 offset:5184\n"
          "ds_read_b128 v[84:87], %6 offset:640\n"
          "ds_read_b64_tr_b16 v[88:89], %3 offset:6912\n"
          "ds_read_b64_tr_b16 v[90:91], %3 offset:7488\n"
          "ds_read_b128 v[92:95], %7 offset:640\n"
          "s_waitcnt lgkmcnt(9)\n"
          "v_mfma_f32_32x32x16_bf16 %0, v[64:67], v[68:71], 0\n"
          "s_branch .Lq3r%=\n"
          ".Lqp1_%=:\n"
          "ds_read_b64 v[98:99], v96 offset:64\n"
          "s_waitcnt lgkmcnt(0)\n"
          "v_min_u32 v98, v98, v99\n"
          "s_nop 0\n"
          "v_readfirstlane_b32 %1, v98\n"
          "s_nop 3\n"
          "s_cmp_ge_u32 %1, 1\n"
          "s_cbranch_scc1 .Lqr1_%=\n"
          "s_sleep 1\n"
          "s_branch .Lqp1_%=\n"
          ".Lqp2_%=:\n"
          "ds_read_b64 v[98:99], v96 offset:64\n"
          "s_waitcnt lgkmcnt(0)\n"
          "v_min_u32 v98, v98, v99\n"
          "s_nop 0\n"
          "v_readfirstlane_b32 %1, v98\n"
          "s_nop 3\n"
          "s_cmp_ge_u32 %1, 2\n"
          "s_cbranch_scc1 .Lqr2_%=\n"
          "s_sleep 1\n"
          "s_branch .Lqp2_%=\n"
          ".Lqp3_%=:\n"
          "ds_read_b64 v[98:99], v96 offset:64\n"
          "s_waitcnt lgkmcnt(0)\n"
          "v_min_u32 v98, v98, v99\n"
          "s_nop 0\n"
          "v_readfirstlane_b32 %1, v98\n"
          "s_nop 3\n"
          "s_cmp_ge_u32 %1, 3\n"
          "s_cbranch_scc1 .Lqr3_%=\n"
          "s_sleep 1\n"
          "s_branch .Lqp3_%=\n"
          ".Lqp4_%=:\n"
          "ds_read_b64 v[98:99], v96 offset:64\n"
          "s_waitcnt lgkmcnt(0)\n"
          "v_min_u32 v98, v98, v99\n"
          "s_nop 0\n"
          "v_readfirstlane_b32 %1, v98\n"
          "s_nop 3\n"
          "s_cmp_ge_u32 %1, 4\n"
          "s_cbranch_scc1 .Lqr4_%=\n"
          "s_sleep 1\n"
          "s_branch .Lqp4_%=\n"
          ".Lqp5_%=:\n"
          "ds_read_b64 v[98:99], v96 offset:64\n"
          "s_waitcnt lgkmcnt(0)\n"
          "v_min_u32 v98, v98, v99\n"
          "s_nop 0\n"
          "v_readfirstlane_b32 %1, v98\n"
          "s_nop 3\n"
          "s_cmp_ge_u32 %1, 5\n"
          "s_cbranch_scc1 .Lqr5_%=\n"
          "s_sleep 1\n"
          "s_branch .Lqp5_%=\n"
          ".Lqp6_%=:\n"
          "ds_read_b64 v[98:99], v96 offset:64\n"
          "s_waitcnt lgkmcnt(0)\n"
          "v_min_u32 v98, v98, v99\n"
          "s_nop 0\n"
          "v_readfirstlane_b32 %1, v98\n"
          "s_nop 3\n"
          "s_cmp_ge_u32 %1, 6\n"
          "s_cbranch_scc1 .Lqr6_%=\n"
          "s_sleep 1\n"
          "s_branch .Lqp6_%=\n"
          ".Lqp7_%=:\n"
          "ds_read_b64 v[98:99], v96 offset:64\n"
          "s_waitcnt lgkmcnt(0)\n"
          "v_min_u32 v98, v98, v99\n"
          "s_nop 0\n"
          "v_readfirstlane_b32 %1, v98\n"
          "s_nop 3\n"
          "s_cmp_ge_u32 %1, 4\n"
          "s_cbranch_scc1 .Lqr7_%=\n"
          "s_sleep 1\n"
          "s_branch .Lqp7_%=\n"
          ".Lqz%=:\n"
          "s_nop 7\n"
          "s_nop 3\n"
          : "=&v"(acc), "=&s"(sp)
          : "v"(lds0 + woff), "v"(lds0 + woff + 20 * 2304), "v"(xa + xo[0]), "v"(xa + xo[1]), "v"(xa + xo[2]), "v"(xa + xo[3]), "s"(T), "s"(lds0 + KA_FLAG)
          : "memory", "scc", "v64","v65","v66","v67","v68","v69","v70","v71","v72","v73","v74","v75","v76","v77","v78","v79","v80","v81","v82","v83","v84","v85","v86","v87","v88","v89","v90","v91","v92","v93","v94","v95","v96","v97","v98","v99");
    }
    await(21);
    if (T < 2) {
      char* qp = smem + K1_W + (2 * T * 64 + xrow) * 40 + h * 8;
#pragma unroll
      for (int i = 0; i < 4; ++i) {
        *(u32*)(qp + (i >> 1) * 16 + (i & 1) * 4) = cvtpk(acc[2 * i] * QS, acc[2 * i + 1] * QS);
        *(u32*)(qp + 2560 + 4 + (i >> 1) * 16 + (i & 1) * 4) = cvtpk(acc[8 + 2 * i] * QS, acc[8 + 2 * i + 1] * QS);
      }
    } else {
      *(f32x4*)(smem + K1_W + 10240 + (w - 4) * 1024 + lane * 16) = f32x4{acc[0], acc[1], acc[2], acc[3]};
    }
    asm volatile("" ::: "memory");
    __hip_atomic_fetch_add((LAS u32*)(smem + KA_FLAG) + 22, 1u, __ATOMIC_RELAXED, __HIP_MEMORY_SCOPE_WORKGROUP);
    while (flags[22] < 512) {}
    asm volatile("" ::: "memory");
  }
  const int head = 4 * hg + (w & 3), kh = w >> 2;
  u32x4 q0[2]; u32 q1w[2];
#pragma unroll
  for (int q = 0; q < 2; ++q) {
    const char* qp = smem + K1_W + ((w & 3) * 64 + 32 * (q ^ kh) + t32) * 40;
    const u32x2 a = *(const u32x2*)(qp + h * 8), c = *(const u32x2*)(qp + 16 + h * 8);
    const char* pp = smem + K1_W + 10240 + (q ^ kh) * 1024 + (t32 + (w & 2) * 16) * 16 + (w & 1) * 8;
    const f32x2 pa = *(const f32x2*)pp, pb = *(const f32x2*)(pp + 2048);
    const u32 pair = cvtpk((pa[0] + pb[0]) * QS, (pa[1] + pb[1]) * QS);
    q0[q] = u32x4{(w & 1) && !h ? pair : a[0], a[1], c[0], c[1]};
    q1w[q] = (w & 1) ? *(const u32*)(qp + 32) : pair;
  }
  asm volatile("s_waitcnt lgkmcnt(0)\n\ts_barrier" : "+v"(q0[0]), "+v"(q0[1]), "+v"(q1w[0]), "+v"(q1w[1]) :: "memory");
  float mx[2];
  const u32 q1r[2] = {q1w[0], q1w[1]};
#pragma unroll
  for (int q = 0; q < 2; ++q) {
    q1w[q] = h ? 0u : q1w[q];
    float qn = dot2(q1w[q], q1w[q], 0.f);
#pragma unroll
    for (int j = 0; j < 4; ++j) qn = dot2(q0[q][j], q0[q][j], qn);
    mx[q] = xadd(qn);
  }
  u32 kb_;
  while (!(kb_ = flags[20])) {}
  const float km = __builtin_bit_cast(float, kb_ & 0x7fffffffu);
#pragma unroll
  for (int q = 0; q < 2; ++q) mx[q] = __builtin_amdgcn_sqrtf(mx[q] * km) * 1.001f;
  const int vd = min(t32, VR - 1);
  u32 k0off = lane * 16, k1off = 1024 + t32 * 4, voff = 1152 + h * VH + vd * 16;
  if (!__all(fmaxf(mx[0], mx[1]) <= 40.f)) {
    while (flags[0] < 10 || flags[1] < 10 || flags[2] < 10 || flags[3] < 10) __builtin_amdgcn_s_sleep(1);
#pragma unroll
    for (int q = 0; q < 2; ++q) {
      const u32x4 q1 = {q1w[q], 0, 0, 0};
      float tm = -INFINITY;
      for (int t = 0; t < MT; ++t) {
        const char* kt = smem + t * KVT;
        f32x16 s = mfma(*(const u32x4*)(kt + k0off), q0[q], f32x16{});
        s = mfma(u32x4{*(const u32*)(kt + k1off), 0, 0, 0}, q1, s);
#pragma unroll
        for (int i = 0; i < 16; ++i) tm = fmaxf(tm, s[i]);
      }
      mx[q] = fmaxf(tm, xhalf(tm));
    }
  }
  asm volatile("" : "+v"(q0[0]), "+v"(q0[1]));
  const float mh0 = bfround(mx[0]), mh1 = bfround(mx[1]);
  const u32x2 q1a = {h ? q1r[1] : q1r[0], h ? cvtpk(-mh1, mh1 - mx[1]) : cvtpk(-mh0, mh0 - mx[0])};
  const u32 q1b = 0;
  float o0, o1, o2, o3, o4, o5, o6, o7, o8, o9, o10;
  u32 sp, si;
  const u32 hb = lds0 + kh * (32 * KVT);
  k0off += hb; k1off += hb; voff += hb;
  asm volatile(
      "s_mov_b32 %14, 0\n"
      "s_mov_b32 %15, 0\n"
      "s_cmp_eq_u32 %25, 0\n"
      "s_cselect_b32 s21, 0x40, 612\n"
      "v_mov_b32 v164, 0\n"
      "v_mov_b32 v165, 0\n"
      "v_mov_b32 v166, 0\n"
      "v_mov_b32 v167, 0\n"
      "v_mov_b32 v157, 0x3f803f80\n"
      "s_cmp_le_u32 %14, %15\n"
      "s_cbranch_scc1 .Lap%=\n"
      ".Lag%=:\n"
      "s_mov_b32 %15, 1\n"
      "ds_read_b32 v156, %12\n"
      "ds_read_b128 v[152:155], %11\n"
      "ds_read_b128 v[160:163], %13\n"
      "ds_read_b128 v[136:139], %11 offset:2432\n"
      "v_mfma_f32_32x32x16_bf16 v[96:111], v[164:167], %16, 0\n"
      "v_mfma_f32_32x32x16_bf16 v[112:127], v[164:167], %17, 0\n"
      "s_waitcnt lgkmcnt(3)\n"
      "v_mfma_f32_32x32x4_2b_bf16 v[32:63], v[156:157], %18, 0\n"
      "v_mov_b32 v132, 0\n"
      "v_mov_b32 v148, 0\n"
      "v_mov_b32 v133, 0\n"
      "v_mov_b32 v149, 0\n"
      "v_mov_b32 v134, 0\n"
      "v_mov_b32 v150, 0\n"
      "v_mov_b32 v135, 0\n"
      "v_mov_b32 v151, 0\n"
      "ds_read_b32 v156, %12 offset:2432\n"
      "s_waitcnt lgkmcnt(1)\n"
      "s_nop 7\n"
      "v_mfma_f32_32x32x16_bf16 v[32:47], v[152:155], %16, v[32:47]\n"
      "v_mfma_f32_32x32x16_bf16 v[48:63], v[152:155], %17, v[48:63]\n"
      "s_nop 1\n"
      "v_mov_b32 v152, v136\n"
      "v_mov_b32 v153, v137\n"
      "v_mov_b32 v154, v138\n"
      "v_mov_b32 v155, v139\n"
      "s_waitcnt lgkmcnt(0)\n"
      "s_cmp_le_u32 %14, %15\n"
      "s_cbranch_scc1 .Lhp%=\n"
      ".Lhg%=:\n"
      "s_mov_b32 %15, 2\n"
      "s_nop 0\n"
      ".Ltop%=:\n"
      "s_bitcmp1_b32 s21, %15\n"
      "s_cbranch_scc1 .Lev%=\n"
      ".Lew%=:\n"
      "v_mfma_f32_32x32x4_2b_bf16 v[64:95], v[156:157], %18, 0\n"
      "v_cvt_pk_bf16_f32 v150, v132, v133\n"
      "v_cvt_pk_bf16_f32 v151, v134, v135\n"
      "v_exp_f32 v128, v32\n"
      "v_exp_f32 v129, v33\n"
      "v_exp_f32 v130, v34\n"
      "v_exp_f32 v131, v35\n"
      "v_mfma_f32_32x32x16_bf16 v[112:127], v[164:167], v[148:151], v[112:127]\n"
      "v_cvt_pk_bf16_f32 v136, v128, v129\n"
      "v_cvt_pk_bf16_f32 v137, v130, v131\n"
      "ds_read_b128 v[164:167], %13 offset:640\n"
      "v_exp_f32 v132, v36\n"
      "v_exp_f32 v133, v37\n"
      "v_exp_f32 v134, v38\n"
      "v_exp_f32 v135, v39\n"
      "s_nop 0\n"
      "v_cvt_pk_bf16_f32 v138, v132, v133\n"
      "v_cvt_pk_bf16_f32 v139, v134, v135\n"
      "ds_read_b32 v156, %12 offset:4864\n"
      "v_exp_f32 v128, v40\n"
      "v_exp_f32 v129, v41\n"
      "v_exp_f32 v130, v42\n"
      "v_exp_f32 v131, v43\n"
      "s_waitcnt lgkmcnt(2)\n"
      "v_mfma_f32_32x32x16_bf16 v[96:111], v[160:163], v[136:139], v[96:111]\n"
      "v_cvt_pk_bf16_f32 v140, v128, v129\n"
      "v_cvt_pk_bf16_f32 v141, v130, v131\n"
      "v_exp_f32 v132, v44\n"
      "v_exp_f32 v133, v45\n"
      "v_exp_f32 v134, v46\n"
      "v_exp_f32 v135, v47\n"
      "v_mfma_f32_32x32x16_bf16 v[64:79], v[152:155], %16, v[64:79]\n"
      "v_cvt_pk_bf16_f32 v142, v132, v133\n"
      "v_cvt_pk_bf16_f32 v143, v134, v135\n"
      "v_exp_f32 v128, v48\n"
      "v_exp_f32 v129, v49\n"
      "v_exp_f32 v130, v50\n"
      "v_exp_f32 v131, v51\n"
      "s_waitcnt lgkmcnt(0)\n"
      "v_mfma_f32_32x32x16_bf16 v[96:111], v[164:167], v[140:143], v[96:111]\n"
      "v_cvt_pk_bf16_f32 v144, v128, v129\n"
      "v_cvt_pk_bf16_f32 v145, v130, v131\n"
      "v_exp_f32 v132, v52\n"
      "v_exp_f32 v133, v53\n"
      "v_exp_f32 v134, v54\n"
      "v_exp_f32 v135, v55\n"
      "v_mfma_f32_32x32x16_bf16 v[80:95], v[152:155], %17, v[80:95]\n"
      "v_cvt_pk_bf16_f32 v146, v132, v133\n"
      "v_cvt_pk_bf16_f32 v147, v134, v135\n"
      "ds_read_b128 v[152:155], %11 offset:4864\n"
      "v_exp_f32 v128, v56\n"
      "v_exp_f32 v129, v57\n"
      "v_exp_f32 v130, v58\n"
      "v_exp_f32 v131, v59\n"
      "v_mfma_f32_32x32x16_bf16 v[112:127], v[160:163], v[144:147], v[112:127]\n"
      "v_cvt_pk_bf16_f32 v148, v128, v129\n"
      "v_cvt_pk_bf16_f32 v149, v130, v131\n"
      "ds_read_b128 v[160:163], %13 offset:2432\n"
      "v_exp_f32 v132, v60\n"
      "v_exp_f32 v133, v61\n"
      "v_exp_f32 v134, v62\n"
      "v_exp_f32 v135, v63\n"
      "s_cmp_le_u32 %14, %15\n"
      "s_cbranch_scc1 .Lbp%=\n"
      ".Lbg%=:\n"
      "v_mfma_f32_32x32x4_2b_bf16 v[32:63], v[156:157], %18, 0\n"
      "v_cvt_pk_bf16_f32 v150, v132, v133\n"
      "v_cvt_pk_bf16_f32 v151, v134, v135\n"
      "v_exp_f32 v128, v64\n"
      "v_exp_f32 v129, v65\n"
      "v_exp_f32 v130, v66\n"
      "v_exp_f32 v131, v67\n"
      "v_mfma_f32_32x32x16_bf16 v[112:127], v[164:167], v[148:151], v[112:127]\n"
      "v_cvt_pk_bf16_f32 v136, v128, v129\n"
      "v_cvt_pk_bf16_f32 v137, v130, v131\n"
      "ds_read_b128 v[164:167], %13 offset:3072\n"
      "v_exp_f32 v132, v68\n"
      "v_exp_f32 v133, v69\n"
      "v_exp_f32 v134, v70\n"
      "v_exp_f32 v135, v71\n"
      "s_nop 0\n"
      "v_cvt_pk_bf16_f32 v138, v132, v133\n"
      "v_cvt_pk_bf16_f32 v139, v134, v135\n"
      "ds_read_b32 v156, %12 offset:7296\n"
      "v_exp_f32 v128, v72\n"
      "v_exp_f32 v129, v73\n"
      "v_exp_f32 v130, v74\n"
      "v_exp_f32 v131, v75\n"
      "s_waitcnt lgkmcnt(2)\n"
      "v_mfma_f32_32x32x16_bf16 v[96:111], v[160:163], v[136:139], v[96:111]\n"
      "v_cvt_pk_bf16_f32 v140, v128, v129\n"
      "v_cvt_pk_bf16_f32 v141, v130, v131\n"
      "v_exp_f32 v132, v76\n"
      "v_exp_f32 v133, v77\n"
      "v_exp_f32 v134, v78\n"
      "v_exp_f32 v135, v79\n"
      "v_mfma_f32_32x32x16_bf16 v[32:47], v[152:155], %16, v[32:47]\n"
      "v_cvt_pk_bf16_f32 v142, v132, v133\n"
      "v_cvt_pk_bf16_f32 v143, v134, v135\n"
      "v_exp_f32 v128, v80\n"
      "v_exp_f32 v129, v81\n"
      "v_exp_f32 v130, v82\n"
      "v_exp_f32 v131, v83\n"
      "s_waitcnt lgkmcnt(0)\n"
      "v_mfma_f32_32x32x16_bf16 v[96:111], v[164:167], v[140:143], v[96:111]\n"
      "v_cvt_pk_bf16_f32 v144, v128, v129\n"
      "v_cvt_pk_bf16_f32 v145, v130, v131\n"
      "v_exp_f32 v132, v84\n"
      "v_exp_f32 v133, v85\n"
      "v_exp_f32 v134, v86\n"
      "v_exp_f32 v135, v87\n"
      "v_mfma_f32_32x32x16_bf16 v[48:63], v[152:155], %17, v[48:63]\n"
      "v_cvt_pk_bf16_f32 v146, v132, v133\n"
      "v_cvt_pk_bf16_f32 v147, v134, v135\n"
      "ds_read_b128 v[152:155], %11 offset:7296\n"
      "v_exp_f32 v128, v88\n"
      "v_exp_f32 v129, v89\n"
      "v_exp_f32 v130, v90\n"
      "v_exp_f32 v131, v91\n"
      "v_mfma_f32_32x32x16_bf16 v[112:127], v[160:163], v[144:147], v[112:127]\n"
      "v_cvt_pk_bf16_f32 v148, v128, v129\n"
      "v_cvt_pk_bf16_f32 v149, v130, v131\n"
      "ds_read_b128 v[160:163], %13 offset:4864\n"
      "v_exp_f32 v132, v92\n"
      "v_exp_f32 v133, v93\n"
      "v_exp_f32 v134, v94\n"
      "v_exp_f32 v135, v95\n"
      "v_mfma_f32_32x32x4_2b_bf16 v[64:95], v[156:157], %18, 0\n"
      "v_cvt_pk_bf16_f32 v150, v132, v133\n"
      "v_cvt_pk_bf16_f32 v151, v134, v135\n"
      "v_exp_f32 v128, v32\n"
      "v_exp_f32 v129, v33\n"
      "v_exp_f32 v130, v34\n"
      "v_exp_f32 v131, v35\n"
      "v_mfma_f32_32x32x16_bf16 v[112:127], v[164:167], v[148:151], v[112:127]\n"
      "v_cvt_pk_bf16_f32 v136, v128, v129\n"
      "v_cvt_pk_bf16_f32 v137, v130, v131\n"
      "ds_read_b128 v[164:167], %13 offset:5504\n"
      "v_exp_f32 v132, v36\n"
      "v_exp_f32 v133, v37\n"
      "v_exp_f32 v134, v38\n"
      "v_exp_f32 v135, v39\n"
      "s_nop 0\n"
      "v_cvt_pk_bf16_f32 v138, v132, v133\n"
      "v_cvt_pk_bf16_f32 v139, v134, v135\n"
      "ds_read_b32 v156, %12 offset:9728\n"
      "v_exp_f32 v128, v40\n"
      "v_exp_f32 v129, v41\n"
      "v_exp_f32 v130, v42\n"
      "v_exp_f32 v131, v43\n"
      "s_waitcnt lgkmcnt(2)\n"
      "v_mfma_f32_32x32x16_bf16 v[96:111], v[160:163], v[136:139], v[96:111]\n"
      "v_cvt_pk_bf16_f32 v140, v128, v129\n"
      "v_cvt_pk_bf16_f32 v141, v130, v131\n"
      "v_exp_f32 v132, v44\n"
      "v_exp_f32 v133, v45\n"
      "v_exp_f32 v134, v46\n"
      "v_exp_f32 v135, v47\n"
      "v_mfma_f32_32x32x16_bf16 v[64:79], v[152:155], %16, v[64:79]\n"
      "v_cvt_pk_bf16_f32 v142, v132, v133\n"
      "v_cvt_pk_bf16_f32 v143, v134, v135\n"
      "v_exp_f32 v128, v48\n"
      "v_exp_f32 v129, v49\n"
      "v_exp_f32 v130, v50\n"
      "v_exp_f32 v131, v51\n"
      "s_waitcnt lgkmcnt(0)\n"
      "v_mfma_f32_32x32x16_bf16 v[96:111], v[164:167], v[140:143], v[96:111]\n"
      "v_cvt_pk_bf16_f32 v144, v128, v129\n"
      "v_cvt_pk_bf16_f32 v145, v130, v131\n"
      "v_exp_f32 v132, v52\n"
      "v_exp_f32 v133, v53\n"
      "v_exp_f32 v134, v54\n"
      "v_exp_f32 v135, v55\n"
      "v_mfma_f32_32x32x16_bf16 v[80:95], v[152:155], %17, v[80:95]\n"
      "v_cvt_pk_bf16_f32 v146, v132, v133\n"
      "v_cvt_pk_bf16_f32 v147, v134, v135\n"
      "ds_read_b128 v[152:155], %11 offset:9728\n"
      "v_exp_f32 v128, v56\n"
      "v_exp_f32 v129, v57\n"
      "v_exp_f32 v130, v58\n"
      "v_exp_f32 v131, v59\n"
      "v_mfma_f32_32x32x16_bf16 v[112:127], v[160:163], v[144:147], v[112:127]\n"
      "v_cvt_pk_bf16_f32 v148, v128, v129\n"
      "v_cvt_pk_bf16_f32 v149, v130, v131\n"
      "ds_read_b128 v[160:163], %13 offset:7296\n"
      "v_exp_f32 v132, v60\n"
      "v_exp_f32 v133, v61\n"
      "v_exp_f32 v134, v62\n"
      "v_exp_f32 v135, v63\n"
      "v_mfma_f32_32x32x4_2b_bf16 v[32:63], v[156:157], %18, 0\n"
      "v_cvt_pk_bf16_f32 v150, v132, v133\n"
      "v_cvt_pk_bf16_f32 v151, v134, v135\n"
      "v_exp_f32 v128, v64\n"
      "v_exp_f32 v129, v65\n"
      "v_exp_f32 v130, v66\n"
      "v_exp_f32 v131, v67\n"
      "v_mfma_f32_32x32x16_bf16 v[112:127], v[164:167], v[148:151], v[112:127]\n"
      "v_cvt_pk_bf16_f32 v136, v128, v129\n"
      "v_cvt_pk_bf16_f32 v137, v130, v131\n"
      "ds_read_b128 v[164:167], %13 offset:7936\n"
      "v_exp_f32 v132, v68\n"
      "v_exp_f32 v133, v69\n"
      "v_exp_f32 v134, v70\n"
      "v_exp_f32 v135, v71\n"
      "s_nop 0\n"
      "v_cvt_pk_bf16_f32 v138, v132, v133\n"
      "v_cvt_pk_bf16_f32 v139, v134, v135\n"
      "ds_read_b32 v156, %12 offset:12160\n"
      "v_exp_f32 v128, v72\n"
      "v_exp_f32 v129, v73\n"
      "v_exp_f32 v130, v74\n"
      "v_exp_f32 v131, v75\n"
      "s_waitcnt lgkmcnt(2)\n"
      "v_mfma_f32_32x32x16_bf16 v[96:111], v[160:163], v[136:139], v[96:111]\n"
      "v_cvt_pk_bf16_f32 v140, v128, v129\n"
      "v_cvt_pk_bf16_f32 v141, v130, v131\n"
      "v_exp_f32 v132, v76\n"
      "v_exp_f32 v133, v77\n"
      "v_exp_f32 v134, v78\n"
      "v_exp_f32 v135, v79\n"
      "v_mfma_f32_32x32x16_bf16 v[32:47], v[152:155], %16, v[32:47]\n"
      "v_cvt_pk_bf16_f32 v142, v132, v133\n"
      "v_cvt_pk_bf16_f32 v143, v134, v135\n"
      "v_exp_f32 v128, v80\n"
      "v_exp_f32 v129, v81\n"
      "v_exp_f32 v130, v82\n"
      "v_exp_f32 v131, v83\n"
      "s_waitcnt lgkmcnt(0)\n"
      "v_mfma_f32_32x32x16_bf16 v[96:111], v[164:167], v[140:143], v[96:111]\n"
      "v_cvt_pk_bf16_f32 v144, v128, v129\n"
      "v_cvt_pk_bf16_f32 v145, v130, v131\n"
      "v_exp_f32 v132, v84\n"
      "v_exp_f32 v133, v85\n"
      "v_exp_f32 v134, v86\n"
      "v_exp_f32 v135, v87\n"
      "v_mfma_f32_32x32x16_bf16 v[48:63], v[152:155], %17, v[48:63]\n"
      "v_cvt_pk_bf16_f32 v146, v132, v133\n"
      "v_cvt_pk_bf16_f32 v147, v134, v135\n"
      "ds_read_b128 v[152:155], %11 offset:12160\n"
      "v_exp_f32 v128, v88\n"
      "v_exp_f32 v129, v89\n"
      "v_exp_f32 v130, v90\n"
      "v_exp_f32 v131, v91\n"
      "v_mfma_f32_32x32x16_bf16 v[112:127], v[160:163], v[144:147], v[112:127]\n"
      "v_cvt_pk_bf16_f32 v148, v128, v129\n"
      "v_cvt_pk_bf16_f32 v149, v130, v131\n"
      "ds_read_b128 v[160:163], %13 offset:9728\n"
      "v_exp_f32 v132, v92\n"
      "v_exp_f32 v133, v93\n"
      "v_exp_f32 v134, v94\n"
      "v_exp_f32 v135, v95\n"
      "v_add_u32 %11, 9728, %11\n"
      "v_add_u32 %12, 9728, %12\n"
      "v_add_u32 %13, 9728, %13\n"
      "s_add_u32 %15, %15, 1\n"
      "s_cmp_le_u32 %15, 9\n"
      "s_cbranch_scc1 .Ltop%=\n"
      "s_setprio 0\n"
      "v_cvt_pk_bf16_f32 v150, v132, v133\n"
      "v_cvt_pk_bf16_f32 v151, v134, v135\n"
      "s_nop 1\n"
      "v_mfma_f32_32x32x16_bf16 v[112:127], v[164:167], v[148:151], v[112:127]\n"
      "v_mov_b32 v64, %20\n"
      "v_add_u32 v65, %21, %11\n"
      "v_add_u32 v66, %22, %11\n"
      ".Le%=:\n"
      "ds_read_b32 v128, v64 offset:16\n"
      "v_mov_b32 v67, %23\n"
      "v_mov_b32 v68, %24\n"
      "v_mov_b32 v69, 1\n"
      "s_waitcnt lgkmcnt(0)\n"
      "v_readfirstlane_b32 %14, v128\n"
      "s_nop 3\n"
      "s_cmp_lt_u32 %14, 512\n"
      "s_cbranch_scc1 .Le%=\n"
      "ds_write_b128 v65, v[112:115] offset:0\n"
      "ds_write_b128 v65, v[116:119] offset:1024\n"
      "ds_write_b128 v65, v[120:123] offset:2048\n"
      "ds_write_b32 v67, v69\n"
      ".Lf%=:\n"
      "ds_read_b32 v129, v68\n"
      "ds_read_b128 v[32:35], v66 offset:0\n"
      "ds_read_b128 v[36:39], v66 offset:1024\n"
      "ds_read_b128 v[40:43], v66 offset:2048\n"
      "s_waitcnt lgkmcnt(0)\n"
      "v_readfirstlane_b32 %14, v129\n"
      "s_nop 3\n"
      "s_cmp_eq_u32 %14, 0\n"
      "s_cbranch_scc1 .Lf%=\n"
      "v_add_f32 %0, v96, v32\n"
      "v_add_f32 %1, v97, v33\n"
      "v_add_f32 %2, v98, v34\n"
      "v_add_f32 %3, v99, v35\n"
      "v_add_f32 %4, v100, v36\n"
      "v_add_f32 %5, v101, v37\n"
      "v_add_f32 %6, v102, v38\n"
      "v_add_f32 %7, v103, v39\n"
      "v_add_f32 %8, v104, v40\n"
      "v_add_f32 %9, v105, v41\n"
      "v_add_f32 %10, v106, v42\n"
      "s_branch .Lz%=\n"
      ".Lap%=:\n"
      "v_mov_b32 v128, %20\n"
      "ds_read_b128 v[128:131], v128\n"
      "s_waitcnt lgkmcnt(0)\n"
      "v_min3_u32 v128, v128, v129, v130\n"
      "v_min_u32 v128, v128, v131\n"
      "s_nop 0\n"
      "v_readfirstlane_b32 %14, v128\n"
      "s_nop 3\n"
      "s_cmp_gt_u32 %14, %15\n"
      "s_cbranch_scc1 .Lag%=\n"
      "s_sleep 1\n"
      "s_branch .Lap%=\n"
      ".Lhp%=:\n"
      "v_mov_b32 v128, %20\n"
      "ds_read_b128 v[128:131], v128\n"
      "s_waitcnt lgkmcnt(0)\n"
      "v_min3_u32 v128, v128, v129, v130\n"
      "v_min_u32 v128, v128, v131\n"
      "s_nop 0\n"
      "v_readfirstlane_b32 %14, v128\n"
      "s_nop 3\n"
      "s_cmp_gt_u32 %14, %15\n"
      "s_cbranch_scc1 .Lhg%=\n"
      "s_sleep 1\n"
      "s_branch .Lhp%=\n"
      ".Lev%=:\n"
      "s_cmp_lg_u32 %15, 6\n"
      "s_cbranch_scc1 .Lc%=\n"
      "v_mov_b32 v128, 1\n"
      "v_mov_b32 v129, %20\n"
      "ds_add_u32 v129, v128 offset:16\n"
      ".Lc%=:\n"
      "s_mov_b32 s20, 544\n"
      "s_bitcmp1_b32 s20, %15\n"
      "s_cselect_b32 s20, 0, %25\n"
      "s_cmp_eq_u32 s20, 0\n"
      "s_setprio 0\n"
      "s_cbranch_scc1 .Lew%=\n"
      "s_setprio 1\n"
      "s_branch .Lew%=\n"
      ".Lbp%=:\n"
      "v_mov_b32 v128, %20\n"
      "ds_read_b128 v[128:131], v128\n"
      "s_waitcnt lgkmcnt(0)\n"
      "v_min3_u32 v128, v128, v129, v130\n"
      "v_min_u32 v128, v128, v131\n"
      "s_nop 0\n"
      "v_readfirstlane_b32 %14, v128\n"
      "s_nop 3\n"
      "s_cmp_gt_u32 %14, %15\n"
      "s_cbranch_scc1 .Lbg%=\n"
      "s_sleep 1\n"
      "s_branch .Lbp%=\n"
      ".Lz%=:\n"
      : "=v"(o0), "=v"(o1), "=v"(o2), "=v"(o3), "=v"(o4), "=v"(o5), "=v"(o6), "=v"(o7), "=v"(o8), "=v"(o9), "=v"(o10),
        "+v"(k0off), "+v"(k1off), "+v"(voff), "=&s"(sp), "=&s"(si)
      : "v"(q0[0]), "v"(q0[1]), "v"(q1a), "v"(q1b), "s"(lds0 + KA_FLAG), "s"(w * 3072 - (kh + 1) * (32 * KVT)), "s"((w ^ 4) * 3072 - (kh + 1) * (32 * KVT)),
        "s"(lds0 + KA_FLAG + 32 + w * 4), "s"(lds0 + KA_FLAG + 32 + (w ^ 4) * 4), "s"(kh)
      : "memory", "scc", "s20", "s21", "v32","v33","v34","v35","v36","v37","v38","v39","v40","v41","v42","v43","v44","v45","v46","v47","v48","v49","v50","v51","v52","v53","v54","v55","v56","v57","v58","v59","v60","v61","v62","v63","v64","v65","v66","v67","v68","v69","v70","v71","v72","v73","v74","v75","v76","v77","v78","v79","v80","v81","v82","v83","v84","v85","v86","v87","v88","v89","v90","v91","v92","v93","v94","v95","v96","v97","v98","v99","v100","v101","v102","v103","v104","v105","v106","v107","v108","v109","v110","v111","v112","v113","v114","v115","v116","v117","v118","v119","v120","v121","v122","v123","v124","v125","v126","v127","v128","v129","v130","v131","v132","v133","v134","v135","v136","v137","v138","v139","v140","v141","v142","v143","v144","v145","v146","v147","v148","v149","v150","v151","v152","v153","v154","v155","v156","v157","v158","v159","v160","v161","v162","v163","v164","v165","v166","v167");
  const int ln = lane_id();
  const u32 lsum = __builtin_amdgcn_permlane32_swap(__builtin_bit_cast(u32, o10), 0u, false, false)[0];
  const u32 lsw = __builtin_amdgcn_permlane32_swap(0u, __builtin_bit_cast(u32, o10), false, false)[0];
  const float inv = __builtin_amdgcn_rcpf(__builtin_bit_cast(float, ln < 32 ? lsum : lsw));
  char* of = ws + OF_OFF + (size_t)mg * 73728 + kh * 1024;
  *(u32x4*)(of + head * 2048 + ln * 16) = u32x4{cvtpk(o0 * inv, o1 * inv), cvtpk(o2 * inv, o3 * inv), cvtpk(o4 * inv, o5 * inv), cvtpk(o6 * inv, o7 * inv)};
  if (ln < 32) *(u32*)(of + (32 + (head >> 3)) * 2048 + ((head >> 2) & 1) * 512 + ln * 16 + (head & 3) * 4) = cvtpk(o8 * inv, o9 * inv);
}

constexpr int K4_LF = 1;
constexpr int K4_EX = 73728, K4_CS = K4_EX + 8192, K4_MS = K4_CS + 2560, K4_GO = K4_MS + 1024, K4_OWN = K4_GO + 512, K4_PF = K4_OWN + 9216 + 12288, K4_LDS = K4_PF + 4 * 13312;
DI float F(u32 v) { return __builtin_bit_cast(float, v); }
__global__ __launch_bounds__(256, 1) void kb_ffn(const u16* __restrict__ x, char* __restrict__ ws, u16* __restrict__ out) {
  extern __shared__ __attribute__((aligned(16))) char smem[];
  const int lane = threadIdx.x & 63, w = __builtin_amdgcn_readfirstlane(threadIdx.x >> 6);
  const int b = blockIdx.x, xcd = b & 7, jj = b >> 3, g = lane >> 4, t16 = lane & 15, par = w & 1;
  const int fs = ((xcd << K4_LF) & 7) + (jj & ((1 << K4_LF) - 1)), mg = (xcd >> (3 - K4_LF)) * (32 >> K4_LF) + (jj >> K4_LF);
  const u32 lds0 = (u32)(size_t)smem, lane16 = lane * 16;
  const char* wstream = ws + WS_OFF + (size_t)(fs * 4 + w) * 324 * 1024;
  const int pg = xcd * 4 + (jj & 3), pn = jj >> 2;
  const char* op = ws + OF_OFF + (size_t)pg * 73728 + w * 18432;
  if (pn < 6 && w < 3) {
    char* hf = ws + HF_OFF + (size_t)pg * 73728;
    char* stp = ws + ST_OFF + (size_t)pg * 9216;
    const int nt = 3 * pn + w, h = lane >> 5, t32 = lane & 31;
    f32x16 a0, a1;
    u32x2 x0, x1, x2, x3, x4, x5, x6, x7;
    const char* xp = (const char*)x + (size_t)pg * 64 * (D * 2) + nt * 64;
    asm volatile(
      "s_mov_b32 m0, %17\n"
      "s_mov_b64 s[20:21], %16\n"
      "s_nop 0\n"
      "global_load_lds_dwordx4 %10, s[20:21] offset:0\n"
      "global_load_lds_dwordx4 %10, s[20:21] offset:1024\n"
      "global_load_lds_dwordx4 %10, s[20:21] offset:2048\n"
      "global_load_lds_dwordx4 %10, s[20:21] offset:3072\n"
      "s_add_u32 s20, s20, 4096\n"
      "s_addc_u32 s21, s21, 0\n"
      "s_add_u32 m0, m0, 4096\n"
      "s_nop 0\n"
      "global_load_lds_dwordx4 %10, s[20:21] offset:0\n"
      "global_load_lds_dwordx4 %10, s[20:21] offset:1024\n"
      "global_load_lds_dwordx4 %10, s[20:21] offset:2048\n"
      "global_load_lds_dwordx4 %10, s[20:21] offset:3072\n"
      "s_add_u32 s20, s20, 4096\n"
      "s_addc_u32 s21, s21, 0\n"
      "s_add_u32 m0, m0, 4096\n"
      "s_nop 0\n"
      "global_load_lds_dwordx4 %10, s[20:21] offset:0\n"
      "global_load_lds_dwordx4 %10, s[20:21] offset:1024\n"
      "global_load_lds_dwordx4 %10, s[20:21] offset:2048\n"
      "global_load_lds_dwordx4 %10, s[20:21] offset:3072\n"
      "s_add_u32 s20, s20, 4096\n"
      "s_addc_u32 s21, s21, 0\n"
      "s_add_u32 m0, m0, 4096\n"
      "s_nop 0\n"
      "global_load_lds_dwordx4 %10, s[20:21] offset:0\n"
      "global_load_lds_dwordx4 %10, s[20:21] offset:1024\n"
      "global_load_lds_dwordx4 %10, s[20:21] offset:2048\n"
      "global_load_lds_dwordx4 %10, s[20:21] offset:3072\n"
      "s_add_u32 s20, s20, 4096\n"
      "s_addc_u32 s21, s21, 0\n"
      "s_add_u32 m0, m0, 4096\n"
      "s_nop 0\n"
      "global_load_lds_dwordx4 %10, s[20:21] offset:0\n"
      "global_load_lds_dwordx4 %10, s[20:21] offset:1024\n"
      "global_load_dwordx2 %2, %12, %14\n"
      "global_load_dwordx2 %6, %12, %15\n"
      "s_mov_b64 s[20:21], %13\n"
      "s_add_u32 s20, s20, 4096\n"
      "s_addc_u32 s21, s21, 0\n"
      "global_load_dwordx4 v[64:67], %10, s[20:21] offset:-4096\n"
      "global_load_dwordx4 v[68:71], %10, s[20:21] offset:-3072\n"
      "global_load_dwordx4 v[72:75], %10, s[20:21] offset:-2048\n"
      "global_load_dwordx4 v[76:79], %10, s[20:21] offset:-1024\n"
      "global_load_dwordx4 v[80:83], %10, s[20:21] offset:0\n"
      "global_load_dwordx4 v[84:87], %10, s[20:21] offset:1024\n"
      "global_load_dwordx4 v[88:91], %10, s[20:21] offset:2048\n"
      "global_load_dwordx4 v[92:95], %10, s[20:21] offset:3072\n"
      "s_add_u32 s20, s20, 8192\n"
      "s_addc_u32 s21, s21, 0\n"
      "global_load_dwordx4 v[96:99], %10, s[20:21] offset:-4096\n"
      "global_load_dwordx4 v[100:103], %10, s[20:21] offset:-3072\n"
      "global_load_dwordx4 v[104:107], %10, s[20:21] offset:-2048\n"
      "global_load_dwordx4 v[108:111], %10, s[20:21] offset:-1024\n"
      "global_load_dwordx4 v[112:115], %10, s[20:21] offset:0\n"
      "global_load_dwordx4 v[116:119], %10, s[20:21] offset:1024\n"
      "global_load_dwordx4 v[120:123], %10, s[20:21] offset:2048\n"
      "global_load_dwordx4 v[124:127], %10, s[20:21] offset:3072\n"
      "s_add_u32 s20, s20, 8192\n"
      "s_addc_u32 s21, s21, 0\n"
      "global_load_dwordx4 v[128:131], %10, s[20:21] offset:-4096\n"
      "global_load_dwordx4 v[132:135], %10, s[20:21] offset:-3072\n"
      "v_add_u32 v232, 0x10000, %11\n"
      "s_waitcnt vmcnt(20)\n"
      "s_barrier\n"
      "ds_read_b128 v[208:211], %11 offset:0\n"
      "ds_read_b128 v[212:215], %11 offset:1024\n"
      "ds_read_b128 v[216:219], %11 offset:2048\n"
      "ds_read_b128 v[220:223], %11 offset:3072\n"
      "ds_read_b128 v[224:227], %11 offset:4096\n"
      "ds_read_b128 v[228:231], %11 offset:5120\n"
      "s_waitcnt vmcnt(17) lgkmcnt(5)\n"
      "v_mfma_f32_32x32x16_bf16 %0, v[64:67], v[208:211], 0\n"
      "global_load_dwordx4 v[136:139], %10, s[20:21] offset:-2048\n"
      "global_load_dwordx4 v[140:143], %10, s[20:21] offset:-1024\n"
      "s_waitcnt lgkmcnt(4)\n"
      "v_mfma_f32_32x32x16_bf16 %1, v[64:67], v[212:215], 0\n"
      "ds_read_b128 v[208:211], %11 offset:6144\n"
      "ds_read_b128 v[212:215], %11 offset:7168\n"
      "s_waitcnt vmcnt(18) lgkmcnt(5)\n"
      "v_mfma_f32_32x32x16_bf16 %0, v[68:71], v[216:219], %0\n"
      "global_load_dwordx4 v[144:147], %10, s[20:21] offset:0\n"
      "global_load_dwordx4 v[148:151], %10, s[20:21] offset:1024\n"
      "s_waitcnt lgkmcnt(4)\n"
      "v_mfma_f32_32x32x16_bf16 %1, v[68:71], v[220:223], %1\n"
      "ds_read_b128 v[216:219], %11 offset:8192\n"
      "ds_read_b128 v[220:223], %11 offset:9216\n"
      "s_waitcnt vmcnt(19) lgkmcnt(5)\n"
      "v_mfma_f32_32x32x16_bf16 %0, v[72:75], v[224:227], %0\n"
      "global_load_dwordx4 v[152:155], %10, s[20:21] offset:2048\n"
      "global_load_dwordx4 v[156:159], %10, s[20:21] offset:3072\n"
      "s_waitcnt lgkmcnt(4)\n"
      "v_mfma_f32_32x32x16_bf16 %1, v[72:75], v[228:231], %1\n"
      "ds_read_b128 v[224:227], %11 offset:10240\n"
      "ds_read_b128 v[228:231], %11 offset:11264\n"
      "s_waitcnt vmcnt(20) lgkmcnt(5)\n"
      "v_mfma_f32_32x32x16_bf16 %0, v[76:79], v[208:211], %0\n"
      "s_add_u32 s20, s20, 8192\n"
      "s_addc_u32 s21, s21, 0\n"
      "global_load_dwordx4 v[160:163], %10, s[20:21] offset:-4096\n"
      "global_load_dwordx4 v[164:167], %10, s[20:21] offset:-3072\n"
      "s_waitcnt lgkmcnt(4)\n"
      "v_mfma_f32_32x32x16_bf16 %1, v[76:79], v[212:215], %1\n"
      "ds_read_b128 v[208:211], %11 offset:12288\n"
      "ds_read_b128 v[212:215], %11 offset:13312\n"
      "s_waitcnt vmcnt(21) lgkmcnt(5)\n"
      "v_mfma_f32_32x32x16_bf16 %0, v[80:83], v[216:219], %0\n"
      "global_load_dwordx4 v[168:171], %10, s[20:21] offset:-2048\n"
      "global_load_dwordx4 v[172:175], %10, s[20:21] offset:-1024\n"
      "s_waitcnt lgkmcnt(4)\n"
      "v_mfma_f32_32x32x16_bf16 %1, v[80:83], v[220:223], %1\n"
      "ds_read_b128 v[216:219], %11 offset:14336\n"
      "ds_read_b128 v[220:223], %11 offset:15360\n"
      "s_waitcnt vmcnt(22) lgkmcnt(5)\n"
      "v_mfma_f32_32x32x16_bf16 %0, v[84:87], v[224:227], %0\n"
      "global_load_dwordx4 v[176:179], %10, s[20:21] offset:0\n"
      "global_load_dwordx4 v[180:183], %10, s[20:21] offset:1024\n"
      "s_waitcnt lgkmcnt(4)\n"
      "v_mfma_f32_32x32x16_bf16 %1, v[84:87], v[228:231], %1\n"
      "ds_read_b128 v[224:227], %11 offset:16384\n"
      "ds_read_b128 v[228:231], %11 offset:17408\n"
      "s_waitcnt vmcnt(23) lgkmcnt(5)\n"
      "v_mfma_f32_32x32x16_bf16 %0, v[88:91], v[208:211], %0\n"
      "global_load_dwordx4 v[184:187], %10, s[20:21] offset:2048\n"
      "global_load_dwordx4 v[188:191], %10, s[20:21] offset:3072\n"
      "s_waitcnt lgkmcnt(4)\n"
      "v_mfma_f32_32x32x16_bf16 %1, v[88:91], v[212:215], %1\n"
      "ds_read_b128 v[208:211], %11 offset:18432\n"
      "ds_read_b128 v[212:215], %11 offset:19456\n"
      "s_waitcnt vmcnt(24) lgkmcnt(5)\n"
      "v_mfma_f32_32x32x16_bf16 %0, v[92:95], v[216:219], %0\n"
      "s_add_u32 s20, s20, 8192\n"
      "s_addc_u32 s21, s21, 0\n"
      "global_load_dwordx4 v[192:195], %10, s[20:21] offset:-4096\n"
      "global_load_dwordx4 v[196:199], %10, s[20:21] offset:-3072\n"
      "s_waitcnt lgkmcnt(4)\n"
      "v_mfma_f32_32x32x16_bf16 %1, v[92:95], v[220:223], %1\n"
      "ds_read_b128 v[216:219], %11 offset:20480\n"
      "ds_read_b128 v[220:223], %11 offset:21504\n"
      "s_waitcnt vmcnt(25) lgkmcnt(5)\n"
      "v_mfma_f32_32x32x16_bf16 %0, v[96:99], v[224:227], %0\n"
      "global_load_dwordx4 v[200:203], %10, s[20:21] offset:-2048\n"
      "global_load_dwordx4 v[204:207], %10, s[20:21] offset:-1024\n"
      "s_waitcnt lgkmcnt(4)\n"
      "v_mfma_f32_32x32x16_bf16 %1, v[96:99], v[228:231], %1\n"
      "global_load_dwordx2 %3, %12, %14 offset:16\n"
      "ds_read_b128 v[224:227], %11 offset:22528\n"
      "ds_read_b128 v[228:231], %11 offset:23552\n"
      "s_waitcnt vmcnt(27) lgkmcnt(5)\n"
      "v_mfma_f32_32x32x16_bf16 %0, v[100:103], v[208:211], %0\n"
      "s_waitcnt lgkmcnt(4)\n"
      "v_mfma_f32_32x32x16_bf16 %1, v[100:103], v[212:215], %1\n"
      "global_load_dwordx2 %7, %12, %15 offset:16\n"
      "ds_read_b128 v[208:211], %11 offset:24576\n"
      "ds_read_b128 v[212:215], %11 offset:25600\n"
      "s_waitcnt vmcnt(27) lgkmcnt(5)\n"
      "v_mfma_f32_32x32x16_bf16 %0, v[104:107], v[216:219], %0\n"
      "s_waitcnt lgkmcnt(4)\n"
      "v_mfma_f32_32x32x16_bf16 %1, v[104:107], v[220:223], %1\n"
      "global_load_dwordx2 %4, %12, %14 offset:32\n"
      "ds_read_b128 v[216:219], %11 offset:26624\n"
      "ds_read_b128 v[220:223], %11 offset:27648\n"
      "s_waitcnt vmcnt(27) lgkmcnt(5)\n"
      "v_mfma_f32_32x32x16_bf16 %0, v[108:111], v[224:227], %0\n"
      "s_waitcnt lgkmcnt(4)\n"
      "v_mfma_f32_32x32x16_bf16 %1, v[108:111], v[228:231], %1\n"
      "global_load_dwordx2 %8, %12, %15 offset:32\n"
      "ds_read_b128 v[224:227], %11 offset:28672\n"
      "ds_read_b128 v[228:231], %11 offset:29696\n"
      "s_waitcnt vmcnt(27) lgkmcnt(5)\n"
      "v_mfma_f32_32x32x16_bf16 %0, v[112:115], v[208:211], %0\n"
      "s_waitcnt lgkmcnt(4)\n"
      "v_mfma_f32_32x32x16_bf16 %1, v[112:115], v[212:215], %1\n"
      "global_load_dwordx2 %5, %12, %14 offset:48\n"
      "ds_read_b128 v[208:211], %11 offset:30720\n"
      "ds_read_b128 v[212:215], %11 offset:31744\n"
      "s_waitcnt vmcnt(27) lgkmcnt(5)\n"
      "v_mfma_f32_32x32x16_bf16 %0, v[116:119], v[216:219], %0\n"
      "s_waitcnt lgkmcnt(4)\n"
      "v_mfma_f32_32x32x16_bf16 %1, v[116:119], v[220:223], %1\n"
      "global_load_dwordx2 %9, %12, %15 offset:48\n"
      "ds_read_b128 v[216:219], %11 offset:32768\n"
      "ds_read_b128 v[220:223], %11 offset:33792\n"
      "s_waitcnt vmcnt(27) lgkmcnt(5)\n"
      "v_mfma_f32_32x32x16_bf16 %0, v[120:123], v[224:227], %0\n"
      "s_waitcnt lgkmcnt(4)\n"
      "v_mfma_f32_32x32x16_bf16 %1, v[120:123], v[228:231], %1\n"
      "ds_read_b128 v[224:227], %11 offset:34816\n"
      "ds_read_b128 v[228:231], %11 offset:35840\n"
      "s_waitcnt vmcnt(26) lgkmcnt(5)\n"
      "v_mfma_f32_32x32x16_bf16 %0, v[124:127], v[208:211], %0\n"
      "s_waitcnt lgkmcnt(4)\n"
      "v_mfma_f32_32x32x16_bf16 %1, v[124:127], v[212:215], %1\n"
      "ds_read_b128 v[208:211], %11 offset:36864\n"
      "ds_read_b128 v[212:215], %11 offset:37888\n"
      "s_waitcnt vmcnt(25) lgkmcnt(5)\n"
      "v_mfma_f32_32x32x16_bf16 %0, v[128:131], v[216:219], %0\n"
      "s_waitcnt lgkmcnt(4)\n"
      "v_mfma_f32_32x32x16_bf16 %1, v[128:131], v[220:223], %1\n"
      "ds_read_b128 v[216:219], %11 offset:38912\n"
      "ds_read_b128 v[220:223], %11 offset:39936\n"
      "s_waitcnt vmcnt(24) lgkmcnt(5)\n"
      "v_mfma_f32_32x32x16_bf16 %0, v[132:135], v[224:227], %0\n"
      "s_waitcnt lgkmcnt(4)\n"
      "v_mfma_f32_32x32x16_bf16 %1, v[132:135], v[228:231], %1\n"
      "ds_read_b128 v[224:227], %11 offset:40960\n"
      "ds_read_b128 v[228:231], %11 offset:41984\n"
      "s_waitcnt vmcnt(23) lgkmcnt(5)\n"
      "v_mfma_f32_32x32x16_bf16 %0, v[136:139], v[208:211], %0\n"
      "s_waitcnt lgkmcnt(4)\n"
      "v_mfma_f32_32x32x16_bf16 %1, v[136:139], v[212:215], %1\n"
      "ds_read_b128 v[208:211], %11 offset:43008\n"
      "ds_read_b128 v[212:215], %11 offset:44032\n"
      "s_waitcnt vmcnt(22) lgkmcnt(5)\n"
      "v_mfma_f32_32x32x16_bf16 %0, v[140:143], v[216:219], %0\n"
      "s_waitcnt lgkmcnt(4)\n"
      "v_mfma_f32_32x32x16_bf16 %1, v[140:143], v[220:223], %1\n"
      "ds_read_b128 v[216:219], %11 offset:45056\n"
      "ds_read_b128 v[220:223], %11 offset:46080\n"
      "s_waitcnt vmcnt(21) lgkmcnt(5)\n"
      "v_mfma_f32_32x32x16_bf16 %0, v[144:147], v[224:227], %0\n"
      "s_waitcnt lgkmcnt(4)\n"
      "v_mfma_f32_32x32x16_bf16 %1, v[144:147], v[228:231], %1\n"
      "ds_read_b128 v[224:227], %11 offset:47104\n"
      "ds_read_b128 v[228:231], %11 offset:48128\n"
      "s_waitcnt vmcnt(20) lgkmcnt(5)\n"
      "v_mfma_f32_32x32x16_bf16 %0, v[148:151], v[208:211], %0\n"
      "s_waitcnt lgkmcnt(4)\n"
      "v_mfma_f32_32x32x16_bf16 %1, v[148:151], v[212:215], %1\n"
      "ds_read_b128 v[208:211], %11 offset:49152\n"
      "ds_read_b128 v[212:215], %11 offset:50176\n"
      "s_waitcnt vmcnt(19) lgkmcnt(5)\n"
      "v_mfma_f32_32x32x16_bf16 %0, v[152:155], v[216:219], %0\n"
      "s_waitcnt lgkmcnt(4)\n"
      "v_mfma_f32_32x32x16_bf16 %1, v[152:155], v[220:223], %1\n"
      "ds_read_b128 v[216:219], %11 offset:51200\n"
      "ds_read_b128 v[220:223], %11 offset:52224\n"
      "s_waitcnt vmcnt(18) lgkmcnt(5)\n"
      "v_mfma_f32_32x32x16_bf16 %0, v[156:159], v[224:227], %0\n"
      "s_waitcnt lgkmcnt(4)\n"
      "v_mfma_f32_32x32x16_bf16 %1, v[156:159], v[228:231], %1\n"
      "ds_read_b128 v[224:227], %11 offset:53248\n"
      "ds_read_b128 v[228:231], %11 offset:54272\n"
      "s_waitcnt vmcnt(17) lgkmcnt(5)\n"
      "v_mfma_f32_32x32x16_bf16 %0, v[160:163], v[208:211], %0\n"
      "s_waitcnt lgkmcnt(4)\n"
      "v_mfma_f32_32x32x16_bf16 %1, v[160:163], v[212:215], %1\n"
      "ds_read_b128 v[208:211], %11 offset:55296\n"
      "ds_read_b128 v[212:215], %11 offset:56320\n"
      "s_waitcnt vmcnt(16) lgkmcnt(5)\n"
      "v_mfma_f32_32x32x16_bf16 %0, v[164:167], v[216:219], %0\n"
      "s_waitcnt lgkmcnt(4)\n"
      "v_mfma_f32_32x32x16_bf16 %1, v[164:167], v[220:223], %1\n"
      "ds_read_b128 v[216:219], %11 offset:57344\n"
      "ds_read_b128 v[220:223], %11 offset:58368\n"
      "s_waitcnt vmcnt(15) lgkmcnt(5)\n"
      "v_mfma_f32_32x32x16_bf16 %0, v[168:171], v[224:227], %0\n"
      "s_waitcnt lgkmcnt(4)\n"
      "v_mfma_f32_32x32x16_bf16 %1, v[168:171], v[228:231], %1\n"
      "ds_read_b128 v[224:227], %11 offset:59392\n"
      "ds_read_b128 v[228:231], %11 offset:60416\n"
      "s_waitcnt vmcnt(14) lgkmcnt(5)\n"
      "v_mfma_f32_32x32x16_bf16 %0, v[172:175], v[208:211], %0\n"
      "s_waitcnt lgkmcnt(4)\n"
      "v_mfma_f32_32x32x16_bf16 %1, v[172:175], v[212:215], %1\n"
      "ds_read_b128 v[208:211], %11 offset:61440\n"
      "ds_read_b128 v[212:215], %11 offset:62464\n"
      "s_waitcnt vmcnt(13) lgkmcnt(5)\n"
      "v_mfma_f32_32x32x16_bf16 %0, v[176:179], v[216:219], %0\n"
      "s_waitcnt lgkmcnt(4)\n"
      "v_mfma_f32_32x32x16_bf16 %1, v[176:179], v[220:223], %1\n"
      "ds_read_b128 v[216:219], %11 offset:63488\n"
      "ds_read_b128 v[220:223], %11 offset:64512\n"
      "s_waitcnt vmcnt(12) lgkmcnt(5)\n"
      "v_mfma_f32_32x32x16_bf16 %0, v[180:183], v[224:227], %0\n"
      "s_waitcnt lgkmcnt(4)\n"
      "v_mfma_f32_32x32x16_bf16 %1, v[180:183], v[228:231], %1\n"
      "ds_read_b128 v[224:227], v232 offset:0\n"
      "ds_read_b128 v[228:231], v232 offset:1024\n"
      "s_waitcnt vmcnt(11) lgkmcnt(5)\n"
      "v_mfma_f32_32x32x16_bf16 %0, v[184:187], v[208:211], %0\n"
      "s_waitcnt lgkmcnt(4)\n"
      "v_mfma_f32_32x32x16_bf16 %1, v[184:187], v[212:215], %1\n"
      "ds_read_b128 v[208:211], v232 offset:2048\n"
      "ds_read_b128 v[212:215], v232 offset:3072\n"
      "s_waitcnt vmcnt(10) lgkmcnt(5)\n"
      "v_mfma_f32_32x32x16_bf16 %0, v[188:191], v[216:219], %0\n"
      "s_waitcnt lgkmcnt(4)\n"
      "v_mfma_f32_32x32x16_bf16 %1, v[188:191], v[220:223], %1\n"
      "ds_read_b128 v[216:219], v232 offset:4096\n"
      "ds_read_b128 v[220:223], v232 offset:5120\n"
      "s_waitcnt vmcnt(9) lgkmcnt(5)\n"
      "v_mfma_f32_32x32x16_bf16 %0, v[192:195], v[224:227], %0\n"
      "s_waitcnt lgkmcnt(4)\n"
      "v_mfma_f32_32x32x16_bf16 %1, v[192:195], v[228:231], %1\n"
      "ds_read_b128 v[224:227], v232 offset:6144\n"
      "ds_read_b128 v[228:231], v232 offset:7168\n"
      "s_waitcnt vmcnt(8) lgkmcnt(5)\n"
      "v_mfma_f32_32x32x16_bf16 %0, v[196:199], v[208:211], %0\n"
      "s_waitcnt lgkmcnt(4)\n"
      "v_mfma_f32_32x32x16_bf16 %1, v[196:199], v[212:215], %1\n"
      "s_waitcnt vmcnt(7) lgkmcnt(3)\n"
      "v_mfma_f32_32x32x16_bf16 %0, v[200:203], v[216:219], %0\n"
      "s_waitcnt lgkmcnt(2)\n"
      "v_mfma_f32_32x32x16_bf16 %1, v[200:203], v[220:223], %1\n"
      "s_waitcnt vmcnt(6) lgkmcnt(1)\n"
      "v_mfma_f32_32x32x16_bf16 %0, v[204:207], v[224:227], %0\n"
      "s_waitcnt lgkmcnt(0)\n"
      "v_mfma_f32_32x32x16_bf16 %1, v[204:207], v[228:231], %1\n"
      "s_waitcnt vmcnt(0)\n"
      "s_mov_b64 s[20:21], %18\n"
      "global_load_dwordx4 a[144:147], %10, s[20:21] offset:0\n"
      "global_load_dwordx4 a[148:151], %10, s[20:21] offset:1024\n"
      "global_load_dwordx4 a[152:155], %10, s[20:21] offset:2048\n"
      "global_load_dwordx4 a[156:159], %10, s[20:21] offset:3072\n"
      "s_add_u32 s20, s20, 4096\n"
      "s_addc_u32 s21, s21, 0\n"
      "global_load_dwordx4 a[160:163], %10, s[20:21] offset:0\n"
      "global_load_dwordx4 a[164:167], %10, s[20:21] offset:1024\n"
      "global_load_dwordx4 a[168:171], %10, s[20:21] offset:2048\n"
      "global_load_dwordx4 a[172:175], %10, s[20:21] offset:3072\n"
      "s_nop 7\n"
      "s_nop 3\n"
        : "=&v"(a0), "=&v"(a1), "=&v"(x0), "=&v"(x1), "=&v"(x2), "=&v"(x3), "=&v"(x4), "=&v"(x5), "=&v"(x6), "=&v"(x7)
        : "v"(lane16), "v"(lds0 + lane16), "v"((u32)(t32 * (D * 2) + h * 8)), "s"(ws + WOF_OFF + (size_t)nt * KS * 1024), "s"(xp), "s"(xp + 32 * D * 2),
          "s"(op), "s"(lds0 + w * 18432), "s"(wstream)
        : "memory", "scc", "s20", "s21", "v64","v65","v66","v67","v68","v69","v70","v71","v72","v73","v74","v75","v76","v77","v78","v79","v80","v81","v82","v83","v84","v85","v86","v87","v88","v89","v90","v91","v92","v93","v94","v95","v96","v97","v98","v99","v100","v101","v102","v103","v104","v105","v106","v107","v108","v109","v110","v111","v112","v113","v114","v115","v116","v117","v118","v119","v120","v121","v122","v123","v124","v125","v126","v127","v128","v129","v130","v131","v132","v133","v134","v135","v136","v137","v138","v139","v140","v141","v142","v143","v144","v145","v146","v147","v148","v149","v150","v151","v152","v153","v154","v155","v156","v157","v158","v159","v160","v161","v162","v163","v164","v165","v166","v167","v168","v169","v170","v171","v172","v173","v174","v175","v176","v177","v178","v179","v180","v181","v182","v183","v184","v185","v186","v187","v188","v189","v190","v191","v192","v193","v194","v195","v196","v197","v198","v199","v200","v201","v202","v203","v204","v205","v206","v207","v208","v209","v210","v211","v212","v213","v214","v215","v216","v217","v218","v219","v220","v221","v222","v223","v224","v225","v226","v227","v228","v229","v230","v231","v232", "a0","a1","a2","a3","a4","a5","a6","a7","a8","a9","a10","a11","a12","a13","a14","a15","a16","a17","a18","a19","a20","a21","a22","a23","a24","a25","a26","a27","a28","a29","a30","a31","a32","a33","a34","a35","a36","a37","a38","a39","a40","a41","a42","a43","a44","a45","a46","a47","a48","a49","a50","a51","a52","a53","a54","a55","a56","a57","a58","a59","a60","a61","a62","a63","a64","a65","a66","a67","a68","a69","a70","a71","a72","a73","a74","a75","a76","a77","a78","a79","a80","a81","a82","a83","a84","a85","a86","a87","a88","a89","a90","a91","a92","a93","a94","a95","a96","a97","a98","a99","a100","a101","a102","a103","a104","a105","a106","a107","a108","a109","a110","a111","a112","a113","a114","a115","a116","a117","a118","a119","a120","a121","a122","a123","a124","a125","a126","a127","a128","a129","a130","a131","a132","a133","a134","a135","a136","a137","a138","a139","a140","a141","a142","a143","a144","a145","a146","a147","a148","a149","a150","a151","a152","a153","a154","a155","a156","a157","a158","a159","a160","a161","a162","a163","a164","a165","a166","a167","a168","a169","a170","a171","a172","a173","a174","a175","a176","a177","a178","a179","a180","a181","a182","a183","a184","a185","a186","a187","a188","a189","a190","a191","a192","a193","a194","a195","a196","a197","a198","a199","a200","a201","a202","a203","a204","a205","a206","a207","a208","a209","a210","a211","a212","a213","a214","a215","a216","a217","a218","a219","a220","a221","a222","a223","a224","a225","a226","a227","a228","a229","a230","a231","a232","a233","a234","a235","a236","a237","a238","a239","a240","a241","a242","a243","a244","a245","a246","a247","a248","a249","a250","a251","a252","a253","a254","a255");
    const u32x2 xv[2][4] = {{x0, x1, x2, x3}, {x4, x5, x6, x7}};
#pragma unroll
    for (int m = 0; m < 2; ++m) {
      const f32x16 a = m ? a1 : a0;
      u32 p[8]; float sum = 0.f, sq = 0.f;
#pragma unroll
      for (int q = 0; q < 4; ++q) {
        const u32 r0 = cvtpk(a[4 * q], a[4 * q + 1]), r1 = cvtpk(a[4 * q + 2], a[4 * q + 3]);
        p[2 * q] = cvtpk(bflo(r0) + bflo(xv[m][q][0]), bfhi(r0) + bfhi(xv[m][q][0]));
        p[2 * q + 1] = cvtpk(bflo(r1) + bflo(xv[m][q][1]), bfhi(r1) + bfhi(xv[m][q][1]));
      }
      const u32 ho = (2 * m + (t32 >> 4)) * 1024 + ((t32 & 15) + 16 * h) * 16;
      store16_wt(hf + nt * 4096, ho, u32x4{p[0], p[1], p[2], p[3]});
      store16_wt(hf + nt * 4096, ho + 512, u32x4{p[4], p[5], p[6], p[7]});
#pragma unroll
      for (int i = 0; i < 8; ++i) { sum = dot2(p[i], 0x3f803f80u, sum); sq = dot2(p[i], p[i], sq); }
      const f32x2 sv = {xadd(sum), xadd(sq)};
      if (!h) asm volatile("global_store_dwordx2 %0, %1, %2 sc1" :: "v"(m * 256 + t32 * 8), "v"(sv), "s"(stp + nt * 512) : "memory");
    }
  } else if (w == 3) {
    if (pn < 6) asm volatile(
      "s_mov_b32 m0, %2\n"
      "s_mov_b64 s[20:21], %1\n"
      "s_nop 0\n"
      "global_load_lds_dwordx4 %0, s[20:21] offset:0\n"
      "global_load_lds_dwordx4 %0, s[20:21] offset:1024\n"
      "global_load_lds_dwordx4 %0, s[20:21] offset:2048\n"
      "global_load_lds_dwordx4 %0, s[20:21] offset:3072\n"
      "s_add_u32 s20, s20, 4096\n"
      "s_addc_u32 s21, s21, 0\n"
      "s_add_u32 m0, m0, 4096\n"
      "s_nop 0\n"
      "global_load_lds_dwordx4 %0, s[20:21] offset:0\n"
      "global_load_lds_dwordx4 %0, s[20:21] offset:1024\n"
      "global_load_lds_dwordx4 %0, s[20:21] offset:2048\n"
      "global_load_lds_dwordx4 %0, s[20:21] offset:3072\n"
      "s_add_u32 s20, s20, 4096\n"
      "s_addc_u32 s21, s21, 0\n"
      "s_add_u32 m0, m0, 4096\n"
      "s_nop 0\n"
      "global_load_lds_dwordx4 %0, s[20:21] offset:0\n"
      "global_load_lds_dwordx4 %0, s[20:21] offset:1024\n"
      "global_load_lds_dwordx4 %0, s[20:21] offset:2048\n"
      "global_load_lds_dwordx4 %0, s[20:21] offset:3072\n"
      "s_add_u32 s20, s20, 4096\n"
      "s_addc_u32 s21, s21, 0\n"
      "s_add_u32 m0, m0, 4096\n"
      "s_nop 0\n"
      "global_load_lds_dwordx4 %0, s[20:21] offset:0\n"
      "global_load_lds_dwordx4 %0, s[20:21] offset:1024\n"
      "global_load_lds_dwordx4 %0, s[20:21] offset:2048\n"
      "global_load_lds_dwordx4 %0, s[20:21] offset:3072\n"
      "s_add_u32 s20, s20, 4096\n"
      "s_addc_u32 s21, s21, 0\n"
      "s_add_u32 m0, m0, 4096\n"
      "s_nop 0\n"
      "global_load_lds_dwordx4 %0, s[20:21] offset:0\n"
      "global_load_lds_dwordx4 %0, s[20:21] offset:1024\n"
      "s_waitcnt vmcnt(0)\n"
      "s_barrier\n"
        :: "v"(lane16), "s"(op), "s"(lds0 + 3 * 18432) : "memory", "scc", "s20", "s21");
    *(u32*)(smem + K4_GO + (lane & 1) * 4) = 0;
    float cs[10];
#pragma unroll
    for (int i = 0; i < 10; ++i) {
      const int v = i * 64 + lane, ww = v / 160, it = (v % 160) >> 5;
      const float* p = (const float*)(ws + CSP_OFF) + (fs * 18 + (it ? 4 * ww + it - 1 : 16 + (ww >> 1))) * 96 + (it ? v & 31 : (v & 27) | (ww & 1) * 4);
      cs[i] = p[0] + p[32] + p[64];
    }
#pragma unroll
    for (int i = 0; i < 10; ++i) *(float*)(smem + K4_CS + (i * 64 + lane) * 4) = cs[i];
    asm volatile(
      "s_mov_b64 s[20:21], %1\n"
      "global_load_dwordx4 a[144:147], %0, s[20:21] offset:0\n"
      "global_load_dwordx4 a[148:151], %0, s[20:21] offset:1024\n"
      "global_load_dwordx4 a[152:155], %0, s[20:21] offset:2048\n"
      "global_load_dwordx4 a[156:159], %0, s[20:21] offset:3072\n"
      "s_add_u32 s20, s20, 4096\n"
      "s_addc_u32 s21, s21, 0\n"
      "global_load_dwordx4 a[160:163], %0, s[20:21] offset:0\n"
      "global_load_dwordx4 a[164:167], %0, s[20:21] offset:1024\n"
      "global_load_dwordx4 a[168:171], %0, s[20:21] offset:2048\n"
      "global_load_dwordx4 a[172:175], %0, s[20:21] offset:3072\n"
      "s_add_u32 s20, s20, 4096\n"
      "s_addc_u32 s21, s21, 0\n"
      "global_load_dwordx4 a[176:179], %0, s[20:21] offset:0\n"
      "global_load_dwordx4 a[180:183], %0, s[20:21] offset:1024\n"
      "global_load_dwordx4 a[184:187], %0, s[20:21] offset:2048\n"
      "global_load_dwordx4 a[188:191], %0, s[20:21] offset:3072\n"
      "s_add_u32 s20, s20, 4096\n"
      "s_addc_u32 s21, s21, 0\n"
      "global_load_dwordx4 a[192:195], %0, s[20:21] offset:0\n"
      "global_load_dwordx4 a[196:199], %0, s[20:21] offset:1024\n"
      "global_load_dwordx4 a[200:203], %0, s[20:21] offset:2048\n"
      "global_load_dwordx4 a[204:207], %0, s[20:21] offset:3072\n"
      "s_add_u32 s20, s20, 4096\n"
      "s_addc_u32 s21, s21, 0\n"
      "global_load_dwordx4 a[208:211], %0, s[20:21] offset:0\n"
      "global_load_dwordx4 a[212:215], %0, s[20:21] offset:1024\n"
      "global_load_dwordx4 a[216:219], %0, s[20:21] offset:2048\n"
      "global_load_dwordx4 a[220:223], %0, s[20:21] offset:3072\n"
      "s_add_u32 s20, s20, 4096\n"
      "s_addc_u32 s21, s21, 0\n"
      "global_load_dwordx4 a[224:227], %0, s[20:21] offset:0\n"
      "global_load_dwordx4 a[228:231], %0, s[20:21] offset:1024\n"
      "global_load_dwordx4 a[232:235], %0, s[20:21] offset:2048\n"
      "global_load_dwordx4 a[236:239], %0, s[20:21] offset:3072\n"
      "s_add_u32 s20, s20, 4096\n"
      "s_addc_u32 s21, s21, 0\n"
      "global_load_dwordx4 a[240:243], %0, s[20:21] offset:0\n"
      "global_load_dwordx4 a[244:247], %0, s[20:21] offset:1024\n"
      "global_load_dwordx4 a[248:251], %0, s[20:21] offset:2048\n"
      "global_load_dwordx4 a[252:255], %0, s[20:21] offset:3072\n"
      "s_mov_b32 m0, %2\n"
      "s_add_u32 s20, s20, 196608\n"
      "s_addc_u32 s21, s21, 0\n"
      "global_load_lds_dwordx4 %0, s[20:21]\n"
      "s_add_u32 m0, m0, 1024\n"
      "s_add_u32 s20, s20, 8192\n"
      "s_addc_u32 s21, s21, 0\n"
      "global_load_lds_dwordx4 %0, s[20:21]\n"
      "s_add_u32 m0, m0, 1024\n"
      "s_add_u32 s20, s20, 8192\n"
      "s_addc_u32 s21, s21, 0\n"
      "global_load_lds_dwordx4 %0, s[20:21]\n"
      "s_add_u32 m0, m0, 1024\n"
      "s_add_u32 s20, s20, 8192\n"
      "s_addc_u32 s21, s21, 0\n"
      "global_load_lds_dwordx4 %0, s[20:21]\n"
      "s_add_u32 m0, m0, 1024\n"
      "s_add_u32 s20, s20, 8192\n"
      "s_addc_u32 s21, s21, 0\n"
      "global_load_lds_dwordx4 %0, s[20:21]\n"
      "s_add_u32 m0, m0, 1024\n"
      "s_add_u32 s20, s20, 8192\n"
      "s_addc_u32 s21, s21, 0\n"
      "global_load_lds_dwordx4 %0, s[20:21]\n"
      "s_add_u32 m0, m0, 1024\n"
      "s_add_u32 s20, s20, 8192\n"
      "s_addc_u32 s21, s21, 0\n"
      "global_load_lds_dwordx4 %0, s[20:21]\n"
      "s_add_u32 m0, m0, 1024\n"
      "s_add_u32 s20, s20, 8192\n"
      "s_addc_u32 s21, s21, 0\n"
      "global_load_lds_dwordx4 %0, s[20:21]\n"
      "s_add_u32 m0, m0, 1024\n"
      "s_add_u32 s20, s20, 8192\n"
      "s_addc_u32 s21, s21, 0\n"
      "global_load_lds_dwordx4 %0, s[20:21]\n"
      "s_add_u32 m0, m0, 1024\n"
      "s_add_u32 s20, s20, 8192\n"
      "s_addc_u32 s21, s21, 0\n"
      "global_load_lds_dwordx4 %0, s[20:21]\n"
      "s_add_u32 m0, m0, 1024\n"
      "s_add_u32 s20, s20, 8192\n"
      "s_addc_u32 s21, s21, 0\n"
      "global_load_lds_dwordx4 %0, s[20:21]\n"
      "s_add_u32 m0, m0, 1024\n"
      "s_add_u32 s20, s20, 8192\n"
      "s_addc_u32 s21, s21, 0\n"
      "global_load_lds_dwordx4 %0, s[20:21]\n"
      "s_add_u32 m0, m0, 1024\n"
        :: "v"(lane16), "s"(wstream), "s"(lds0 + K4_PF + 3 * 13312) : "memory", "scc", "s20", "s21", "a0","a1","a2","a3","a4","a5","a6","a7","a8","a9","a10","a11","a12","a13","a14","a15","a16","a17","a18","a19","a20","a21","a22","a23","a24","a25","a26","a27","a28","a29","a30","a31","a32","a33","a34","a35","a36","a37","a38","a39","a40","a41","a42","a43","a44","a45","a46","a47","a48","a49","a50","a51","a52","a53","a54","a55","a56","a57","a58","a59","a60","a61","a62","a63","a64","a65","a66","a67","a68","a69","a70","a71","a72","a73","a74","a75","a76","a77","a78","a79","a80","a81","a82","a83","a84","a85","a86","a87","a88","a89","a90","a91","a92","a93","a94","a95","a96","a97","a98","a99","a100","a101","a102","a103","a104","a105","a106","a107","a108","a109","a110","a111","a112","a113","a114","a115","a116","a117","a118","a119","a120","a121","a122","a123","a124","a125","a126","a127","a128","a129","a130","a131","a132","a133","a134","a135","a136","a137","a138","a139","a140","a141","a142","a143","a144","a145","a146","a147","a148","a149","a150","a151","a152","a153","a154","a155","a156","a157","a158","a159","a160","a161","a162","a163","a164","a165","a166","a167","a168","a169","a170","a171","a172","a173","a174","a175","a176","a177","a178","a179","a180","a181","a182","a183","a184","a185","a186","a187","a188","a189","a190","a191","a192","a193","a194","a195","a196","a197","a198","a199","a200","a201","a202","a203","a204","a205","a206","a207","a208","a209","a210","a211","a212","a213","a214","a215","a216","a217","a218","a219","a220","a221","a222","a223","a224","a225","a226","a227","a228","a229","a230","a231","a232","a233","a234","a235","a236","a237","a238","a239","a240","a241","a242","a243","a244","a245","a246","a247","a248","a249","a250","a251","a252","a253","a254","a255");
  }
  char* hf = ws + HF_OFF + (size_t)mg * 73728;
  char* stp = ws + ST_OFF + (size_t)mg * 9216;
  char* cnt = ws + CNT_OFF + mg * 512;
  const int qa = (4 * (fs >> 1) + (w >> 1)) * 4 + 2 * (fs & 1) + (w & 1);
  u32x2 s0, s1, s2, s3, s4;
  asm volatile(
      "s_bitcmp0_b32 %14, 0\n"
      "s_cbranch_scc1 .Lns%=\n"
      "s_waitcnt vmcnt(0)\n"
      "s_barrier\n"
      "s_bitcmp0_b32 %14, 1\n"
      "s_cbranch_scc1 .Lns%=\n"
      "s_mov_b64 exec, 1\n"
      "v_mov_b32 v32, 0\n"
      "v_mov_b32 v33, 1\n"
      "global_atomic_add v32, v33, %13 offset:0 sc1\n"
      "s_mov_b64 exec, -1\n"
      ".Lns%=:\n"
      "s_bitcmp1_b32 %14, 1\n"
      "s_cbranch_scc1 .Lw3%=\n"
      "s_bitcmp1_b32 %14, 0\n"
      "s_cbranch_scc1 .Lwe%=\n"
      "s_mov_b64 s[20:21], %10\n"
      "global_load_dwordx4 a[144:147], %5, s[20:21] offset:0\n"
      "global_load_dwordx4 a[148:151], %5, s[20:21] offset:1024\n"
      "global_load_dwordx4 a[152:155], %5, s[20:21] offset:2048\n"
      "global_load_dwordx4 a[156:159], %5, s[20:21] offset:3072\n"
      "s_add_u32 s20, s20, 4096\n"
      "s_addc_u32 s21, s21, 0\n"
      "global_load_dwordx4 a[160:163], %5, s[20:21] offset:0\n"
      "global_load_dwordx4 a[164:167], %5, s[20:21] offset:1024\n"
      "global_load_dwordx4 a[168:171], %5, s[20:21] offset:2048\n"
      "global_load_dwordx4 a[172:175], %5, s[20:21] offset:3072\n"
      ".Lwe%=:\n"
      "s_mov_b64 s[20:21], %10\n"
      "s_add_u32 s20, s20, 8192\n"
      "s_addc_u32 s21, s21, 0\n"
      "global_load_dwordx4 a[176:179], %5, s[20:21] offset:0\n"
      "global_load_dwordx4 a[180:183], %5, s[20:21] offset:1024\n"
      "global_load_dwordx4 a[184:187], %5, s[20:21] offset:2048\n"
      "global_load_dwordx4 a[188:191], %5, s[20:21] offset:3072\n"
      "s_add_u32 s20, s20, 4096\n"
      "s_addc_u32 s21, s21, 0\n"
      "global_load_dwordx4 a[192:195], %5, s[20:21] offset:0\n"
      "global_load_dwordx4 a[196:199], %5, s[20:21] offset:1024\n"
      "global_load_dwordx4 a[200:203], %5, s[20:21] offset:2048\n"
      "global_load_dwordx4 a[204:207], %5, s[20:21] offset:3072\n"
      "s_add_u32 s20, s20, 4096\n"
      "s_addc_u32 s21, s21, 0\n"
      "global_load_dwordx4 a[208:211], %5, s[20:21] offset:0\n"
      "global_load_dwordx4 a[212:215], %5, s[20:21] offset:1024\n"
      "global_load_dwordx4 a[216:219], %5, s[20:21] offset:2048\n"
      "global_load_dwordx4 a[220:223], %5, s[20:21] offset:3072\n"
      "s_add_u32 s20, s20, 4096\n"
      "s_addc_u32 s21, s21, 0\n"
      "global_load_dwordx4 a[224:227], %5, s[20:21] offset:0\n"
      "global_load_dwordx4 a[228:231], %5, s[20:21] offset:1024\n"
      "global_load_dwordx4 a[232:235], %5, s[20:21] offset:2048\n"
      "global_load_dwordx4 a[236:239], %5, s[20:21] offset:3072\n"
      "s_add_u32 s20, s20, 4096\n"
      "s_addc_u32 s21, s21, 0\n"
      "global_load_dwordx4 a[240:243], %5, s[20:21] offset:0\n"
      "global_load_dwordx4 a[244:247], %5, s[20:21] offset:1024\n"
      "global_load_dwordx4 a[248:251], %5, s[20:21] offset:2048\n"
      "global_load_dwordx4 a[252:255], %5, s[20:21] offset:3072\n"
      "s_mov_b32 m0, %15\n"
      "s_add_u32 s20, s20, 196608\n"
      "s_addc_u32 s21, s21, 0\n"
      "global_load_lds_dwordx4 %5, s[20:21]\n"
      "s_add_u32 m0, m0, 1024\n"
      "s_add_u32 s20, s20, 8192\n"
      "s_addc_u32 s21, s21, 0\n"
      "global_load_lds_dwordx4 %5, s[20:21]\n"
      "s_add_u32 m0, m0, 1024\n"
      "s_add_u32 s20, s20, 8192\n"
      "s_addc_u32 s21, s21, 0\n"
      "global_load_lds_dwordx4 %5, s[20:21]\n"
      "s_add_u32 m0, m0, 1024\n"
      "s_add_u32 s20, s20, 8192\n"
      "s_addc_u32 s21, s21, 0\n"
      "global_load_lds_dwordx4 %5, s[20:21]\n"
      "s_add_u32 m0, m0, 1024\n"
      "s_add_u32 s20, s20, 8192\n"
      "s_addc_u32 s21, s21, 0\n"
      "global_load_lds_dwordx4 %5, s[20:21]\n"
      "s_add_u32 m0, m0, 1024\n"
      "s_add_u32 s20, s20, 8192\n"
      "s_addc_u32 s21, s21, 0\n"
      "global_load_lds_dwordx4 %5, s[20:21]\n"
      "s_add_u32 m0, m0, 1024\n"
      "s_add_u32 s20, s20, 8192\n"
      "s_addc_u32 s21, s21, 0\n"
      "global_load_lds_dwordx4 %5, s[20:21]\n"
      "s_add_u32 m0, m0, 1024\n"
      "s_add_u32 s20, s20, 8192\n"
      "s_addc_u32 s21, s21, 0\n"
      "global_load_lds_dwordx4 %5, s[20:21]\n"
      "s_add_u32 m0, m0, 1024\n"
      "s_add_u32 s20, s20, 8192\n"
      "s_addc_u32 s21, s21, 0\n"
      "global_load_lds_dwordx4 %5, s[20:21]\n"
      "s_add_u32 m0, m0, 1024\n"
      "s_add_u32 s20, s20, 8192\n"
      "s_addc_u32 s21, s21, 0\n"
      "global_load_lds_dwordx4 %5, s[20:21]\n"
      "s_add_u32 m0, m0, 1024\n"
      "s_add_u32 s20, s20, 8192\n"
      "s_addc_u32 s21, s21, 0\n"
      "global_load_lds_dwordx4 %5, s[20:21]\n"
      "s_add_u32 m0, m0, 1024\n"
      "s_add_u32 s20, s20, 8192\n"
      "s_addc_u32 s21, s21, 0\n"
      "global_load_lds_dwordx4 %5, s[20:21]\n"
      "s_add_u32 m0, m0, 1024\n"
      "s_branch .Lxg%=\n"
      ".Lw3%=:\n"
      "s_bitcmp1_b32 %14, 0\n"
      "s_cbranch_scc1 .Lw4%=\n"
      "s_sleep 60\n"
      ".Lw4%=:\n"
      "s_mov_b32 s22, 1048576\n"
      "v_mov_b32 v32, 0\n"
      ".Lxp%=:\n"
      "global_load_dword v33, v32, %12 offset:0 sc1\n"
      "s_waitcnt vmcnt(0)\n"
      "v_readfirstlane_b32 s23, v33\n"
      "s_sub_u32 s22, s22, 1\n"
      "s_cbranch_scc1 .Lxg%=\n"
      "s_cmp_ge_u32 s23, 6\n"
      "s_cbranch_scc1 .Lxg%=\n"
      "s_sleep 2\n"
      "s_branch .Lxp%=\n"
      ".Lxg%=:\n"
      "s_barrier\n"
      "global_load_dwordx2 %0, %6, %8 offset:-4096 sc1\n"
      "global_load_dwordx2 %1, %6, %8 offset:-2048 sc1\n"
      "global_load_dwordx2 %2, %6, %8 offset:0 sc1\n"
      "global_load_dwordx2 %3, %6, %8 offset:2048 sc1\n"
      "global_load_dwordx2 %4, %7, %8 sc1\n"
      "s_mov_b32 m0, %11\n"
      "s_mov_b64 s[20:21], %9\n"
      "s_nop 0\n"
      "global_load_lds_dwordx4 %5, s[20:21] offset:0 sc1\n"
      "global_load_lds_dwordx4 %5, s[20:21] offset:1024 sc1\n"
      "global_load_lds_dwordx4 %5, s[20:21] offset:2048 sc1\n"
      "s_add_u32 s20, s20, 12288\n"
      "s_addc_u32 s21, s21, 0\n"
      "s_add_u32 m0, m0, 12288\n"
      "s_nop 0\n"
      "global_load_lds_dwordx4 %5, s[20:21] offset:0 sc1\n"
      "global_load_lds_dwordx4 %5, s[20:21] offset:1024 sc1\n"
      "global_load_lds_dwordx4 %5, s[20:21] offset:2048 sc1\n"
      "s_add_u32 s20, s20, 12288\n"
      "s_addc_u32 s21, s21, 0\n"
      "s_add_u32 m0, m0, 12288\n"
      "s_nop 0\n"
      "global_load_lds_dwordx4 %5, s[20:21] offset:0 sc1\n"
      "global_load_lds_dwordx4 %5, s[20:21] offset:1024 sc1\n"
      "global_load_lds_dwordx4 %5, s[20:21] offset:2048 sc1\n"
      "s_waitcnt vmcnt(9)\n"
      : "=&v"(s0), "=&v"(s1), "=&v"(s2), "=&v"(s3), "=&v"(s4)
      : "v"(lane16), "v"((u32)(g * 512 + t16 * 8)), "v"((u32)((8 + (g & 1)) * 512 + t16 * 8)), "s"(stp + 4096 + w * 128),
        "s"(hf + w * 3072), "s"(wstream), "s"(lds0 + w * 3072), "s"(cnt), "s"(ws + CNT_OFF + pg * 512),
        "s"(__builtin_amdgcn_readfirstlane((pn < 6) + 2 * (w == 3))), "s"(lds0 + K4_PF + w * 13312)
      : "memory", "scc", "s20", "s21", "s22", "s23", "v32", "v33", "a0","a1","a2","a3","a4","a5","a6","a7","a8","a9","a10","a11","a12","a13","a14","a15","a16","a17","a18","a19","a20","a21","a22","a23","a24","a25","a26","a27","a28","a29","a30","a31","a32","a33","a34","a35","a36","a37","a38","a39","a40","a41","a42","a43","a44","a45","a46","a47","a48","a49","a50","a51","a52","a53","a54","a55","a56","a57","a58","a59","a60","a61","a62","a63","a64","a65","a66","a67","a68","a69","a70","a71","a72","a73","a74","a75","a76","a77","a78","a79","a80","a81","a82","a83","a84","a85","a86","a87","a88","a89","a90","a91","a92","a93","a94","a95","a96","a97","a98","a99","a100","a101","a102","a103","a104","a105","a106","a107","a108","a109","a110","a111","a112","a113","a114","a115","a116","a117","a118","a119","a120","a121","a122","a123","a124","a125","a126","a127","a128","a129","a130","a131","a132","a133","a134","a135","a136","a137","a138","a139","a140","a141","a142","a143","a144","a145","a146","a147","a148","a149","a150","a151","a152","a153","a154","a155","a156","a157","a158","a159","a160","a161","a162","a163","a164","a165","a166","a167","a168","a169","a170","a171","a172","a173","a174","a175","a176","a177","a178","a179","a180","a181","a182","a183","a184","a185","a186","a187","a188","a189","a190","a191","a192","a193","a194","a195","a196","a197","a198","a199","a200","a201","a202","a203","a204","a205","a206","a207","a208","a209","a210","a211","a212","a213","a214","a215","a216","a217","a218","a219","a220","a221","a222","a223","a224","a225","a226","a227","a228","a229","a230","a231","a232","a233","a234","a235","a236","a237","a238","a239","a240","a241","a242","a243","a244","a245","a246","a247","a248","a249","a250","a251","a252","a253","a254","a255");
  {
    float su = (F(s0[0]) + F(s1[0])) + (F(s2[0]) + F(s3[0])) + (g < 2 ? F(s4[0]) : 0.f), sq = (F(s0[1]) + F(s1[1])) + (F(s2[1]) + F(s3[1])) + (g < 2 ? F(s4[1]) : 0.f);
    su += __shfl_xor(su, 16, 64); sq += __shfl_xor(sq, 16, 64);
    f32x2 ms = ln_scalars(xadd(su), xadd(sq));
    ms[0] = -ms[0];
    if (g == 0) *(f32x2*)(smem + K4_MS + w * 128 + t16 * 8) = ms;
  }
  const char* part = ws + PART_OFF + (size_t)(fs * 32 + mg) * 73728;
  const u32 la = lds0 + lane16;
  asm volatile(
      "s_lshr_b32 s28, %20, 1\n"
      "s_and_b32 s29, %20, 1\n"
      "s_cmp_eq_u32 s28, %14\n"
      "s_cselect_b32 s28, s29, 2\n"
      "s_mov_b64 s[22:23], %11\n"
      "v_readfirstlane_b32 s31, %1\n"
      "s_mul_i32 s25, %14, 3072\n"
      "s_add_u32 s31, s31, s25\n"
      "s_mov_b64 s[26:27], %21\n"
      "s_add_u32 s25, s25, 36864\n"
      "s_add_u32 s26, s26, s25\n"
      "s_addc_u32 s27, s27, 0\n"
      "s_add_u32 m0, s31, 36864\n"
      "s_nop 0\n"
      "global_load_lds_dwordx4 %0, s[26:27] offset:0 sc1\n"
      "global_load_lds_dwordx4 %0, s[26:27] offset:1024 sc1\n"
      "global_load_lds_dwordx4 %0, s[26:27] offset:2048 sc1\n"
      "s_waitcnt vmcnt(9)\n"
      "s_waitcnt lgkmcnt(0)\n"
      "s_barrier\n"
      "ds_read_b64 v[24:25], %8 offset:0\n"
      "ds_read_b64 v[26:27], %8 offset:128\n"
      "ds_read_b64 v[28:29], %8 offset:256\n"
      "ds_read_b64 v[30:31], %8 offset:384\n"
      "ds_read_b128 v[240:243], %7 offset:128\n"
      "ds_read_b128 v[244:247], %7 offset:144\n"
      "ds_read_b128 v[32:35], %1 offset:0\n"
      "ds_read_b128 v[36:39], %1 offset:1024\n"
      "ds_read_b128 a[0:3], %1 offset:2048\n"
      "ds_read_b128 a[4:7], %1 offset:3072\n"
      "ds_read_b128 v[40:43], %1 offset:4096\n"
      "ds_read_b128 v[44:47], %1 offset:5120\n"
      "ds_read_b128 a[8:11], %1 offset:6144\n"
      "ds_read_b128 a[12:15], %1 offset:7168\n"
      "s_waitcnt lgkmcnt(8)\n"
      "v_mul_f32 v176, v24, v240\n"
      "v_mul_f32 v177, v24, v241\n"
      "v_mul_f32 v178, v24, v242\n"
      "v_mul_f32 v179, v24, v243\n"
      "v_mul_f32 v180, v26, v240\n"
      "v_mul_f32 v181, v26, v241\n"
      "v_mul_f32 v182, v26, v242\n"
      "v_mul_f32 v183, v26, v243\n"
      "v_mul_f32 v184, v28, v240\n"
      "v_mul_f32 v185, v28, v241\n"
      "v_mul_f32 v186, v28, v242\n"
      "v_mul_f32 v187, v28, v243\n"
      "v_mul_f32 v188, v30, v240\n"
      "v_mul_f32 v189, v30, v241\n"
      "v_mul_f32 v190, v30, v242\n"
      "v_mul_f32 v191, v30, v243\n"
      "v_mul_f32 v192, v24, v244\n"
      "v_mul_f32 v193, v24, v245\n"
      "v_mul_f32 v194, v24, v246\n"
      "v_mul_f32 v195, v24, v247\n"
      "v_mul_f32 v196, v26, v244\n"
      "v_mul_f32 v197, v26, v245\n"
      "v_mul_f32 v198, v26, v246\n"
      "v_mul_f32 v199, v26, v247\n"
      "v_mul_f32 v200, v28, v244\n"
      "v_mul_f32 v201, v28, v245\n"
      "v_mul_f32 v202, v28, v246\n"
      "v_mul_f32 v203, v28, v247\n"
      "v_mul_f32 v204, v30, v244\n"
      "v_mul_f32 v205, v30, v245\n"
      "v_mul_f32 v206, v30, v246\n"
      "v_mul_f32 v207, v30, v247\n"
      "s_nop 1\n"
      "s_waitcnt lgkmcnt(7)\n"
      "v_mfma_f32_16x16x32_bf16 v[176:179], a[144:147], v[32:35], v[176:179]\n"
      "ds_read_b128 v[240:243], %7 offset:256\n"
      "ds_read_b128 v[48:51], %1 offset:8192\n"
      "ds_read_b128 v[52:55], %1 offset:9216\n"
      "ds_read_b128 a[16:19], %1 offset:10240\n"
      "ds_read_b128 a[20:23], %1 offset:11264\n"
      "s_waitcnt lgkmcnt(11)\n"
      "v_mfma_f32_16x16x32_bf16 v[180:183], a[144:147], v[36:39], v[180:183]\n"
      "ds_read_b128 v[244:247], %7 offset:272\n"
      "s_waitcnt lgkmcnt(11)\n"
      "v_mfma_f32_16x16x32_bf16 v[184:187], a[144:147], a[0:3], v[184:187]\n"
      "s_waitcnt lgkmcnt(10)\n"
      "v_mfma_f32_16x16x32_bf16 v[188:191], a[144:147], a[4:7], v[188:191]\n"
      "s_mov_b64 s[20:21], %10\n"
      "s_add_u32 s20, s20, 28672\n"
      "s_addc_u32 s21, s21, 0\n"
      "global_load_dwordx4 a[144:147], %0, s[20:21] offset:0\n"
      "v_mfma_f32_16x16x32_bf16 v[192:195], a[148:151], v[32:35], v[192:195]\n"
      "v_mfma_f32_16x16x32_bf16 v[196:199], a[148:151], v[36:39], v[196:199]\n"
      "v_mfma_f32_16x16x32_bf16 v[200:203], a[148:151], a[0:3], v[200:203]\n"
      "s_waitcnt lgkmcnt(0)\n"
      "v_mfma_f32_16x16x32_bf16 v[204:207], a[148:151], a[4:7], v[204:207]\n"
      "global_load_dwordx4 a[148:151], %0, s[20:21] offset:1024\n"
      "v_mul_f32 v208, v24, v240\n"
      "s_waitcnt vmcnt(8)\n"
      "s_barrier\n"
      "s_add_u32 s26, s26, 12288\n"
      "s_addc_u32 s27, s27, 0\n"
      "s_add_u32 m0, s31, 49152\n"
      "s_nop 0\n"
      "global_load_lds_dwordx4 %0, s[26:27] offset:0 sc1\n"
      "global_load_lds_dwordx4 %0, s[26:27] offset:1024 sc1\n"
      "global_load_lds_dwordx4 %0, s[26:27] offset:2048 sc1\n"
      "v_mfma_f32_16x16x32_bf16 v[176:179], a[152:155], v[40:43], v[176:179]\n"
      "v_mul_f32 v209, v24, v241\n"
      "ds_read_b128 v[56:59], %1 offset:12288\n"
      "ds_read_b128 v[60:63], %1 offset:13312\n"
      "ds_read_b128 a[24:27], %1 offset:14336\n"
      "ds_read_b128 a[28:31], %1 offset:15360\n"
      "v_mfma_f32_16x16x32_bf16 v[180:183], a[152:155], v[44:47], v[180:183]\n"
      "v_mul_f32 v210, v24, v242\n"
      "v_mfma_f32_16x16x32_bf16 v[184:187], a[152:155], a[8:11], v[184:187]\n"
      "v_mul_f32 v211, v24, v243\n"
      "v_mfma_f32_16x16x32_bf16 v[188:191], a[152:155], a[12:15], v[188:191]\n"
      "global_load_dwordx4 a[152:155], %0, s[20:21] offset:2048\n"
      "v_mul_f32 v212, v26, v240\n"
      "v_mfma_f32_16x16x32_bf16 v[192:195], a[156:159], v[40:43], v[192:195]\n"
      "v_mul_f32 v213, v26, v241\n"
      "v_mfma_f32_16x16x32_bf16 v[196:199], a[156:159], v[44:47], v[196:199]\n"
      "v_mul_f32 v214, v26, v242\n"
      "v_mfma_f32_16x16x32_bf16 v[200:203], a[156:159], a[8:11], v[200:203]\n"
      "v_mul_f32 v215, v26, v243\n"
      "v_mfma_f32_16x16x32_bf16 v[204:207], a[156:159], a[12:15], v[204:207]\n"
      "global_load_dwordx4 a[156:159], %0, s[20:21] offset:3072\n"
      "v_mul_f32 v216, v28, v240\n"
      "v_mfma_f32_16x16x32_bf16 v[176:179], a[160:163], v[48:51], v[176:179]\n"
      "v_mul_f32 v217, v28, v241\n"
      "ds_read_b128 v[64:67], %1 offset:16384\n"
      "ds_read_b128 v[68:71], %1 offset:17408\n"
      "ds_read_b128 a[32:35], %1 offset:18432\n"
      "ds_read_b128 a[36:39], %1 offset:19456\n"
      "v_mfma_f32_16x16x32_bf16 v[180:183], a[160:163], v[52:55], v[180:183]\n"
      "v_mul_f32 v218, v28, v242\n"
      "v_mfma_f32_16x16x32_bf16 v[184:187], a[160:163], a[16:19], v[184:187]\n"
      "v_mul_f32 v219, v28, v243\n"
      "v_mfma_f32_16x16x32_bf16 v[188:191], a[160:163], a[20:23], v[188:191]\n"
      "s_add_u32 s20, s20, 8192\n"
      "s_addc_u32 s21, s21, 0\n"
      "global_load_dwordx4 a[160:163], %0, s[20:21] offset:-4096\n"
      "v_mul_f32 v220, v30, v240\n"
      "v_mfma_f32_16x16x32_bf16 v[192:195], a[164:167], v[48:51], v[192:195]\n"
      "v_mul_f32 v221, v30, v241\n"
      "v_mfma_f32_16x16x32_bf16 v[196:199], a[164:167], v[52:55], v[196:199]\n"
      "v_mul_f32 v222, v30, v242\n"
      "v_mfma_f32_16x16x32_bf16 v[200:203], a[164:167], a[16:19], v[200:203]\n"
      "v_mul_f32 v223, v30, v243\n"
      "v_mfma_f32_16x16x32_bf16 v[204:207], a[164:167], a[20:23], v[204:207]\n"
      "global_load_dwordx4 a[164:167], %0, s[20:21] offset:-3072\n"
      "v_mul_f32 v224, v24, v244\n"
      "s_waitcnt lgkmcnt(7)\n"
      "v_mfma_f32_16x16x32_bf16 v[176:179], a[168:171], v[56:59], v[176:179]\n"
      "v_mul_f32 v225, v24, v245\n"
      "ds_read_b128 v[72:75], %1 offset:20480\n"
      "ds_read_b128 v[76:79], %1 offset:21504\n"
      "ds_read_b128 a[40:43], %1 offset:22528\n"
      "ds_read_b128 a[44:47], %1 offset:23552\n"
      "s_waitcnt lgkmcnt(10)\n"
      "v_mfma_f32_16x16x32_bf16 v[180:183], a[168:171], v[60:63], v[180:183]\n"
      "v_mul_f32 v226, v24, v246\n"
      "s_waitcnt lgkmcnt(9)\n"
      "v_mfma_f32_16x16x32_bf16 v[184:187], a[168:171], a[24:27], v[184:187]\n"
      "v_mul_f32 v227, v24, v247\n"
      "s_waitcnt lgkmcnt(8)\n"
      "v_mfma_f32_16x16x32_bf16 v[188:191], a[168:171], a[28:31], v[188:191]\n"
      "global_load_dwordx4 a[168:171], %0, s[20:21] offset:-2048\n"
      "v_mul_f32 v228, v26, v244\n"
      "v_mfma_f32_16x16x32_bf16 v[192:195], a[172:175], v[56:59], v[192:195]\n"
      "v_mul_f32 v229, v26, v245\n"
      "v_mfma_f32_16x16x32_bf16 v[196:199], a[172:175], v[60:63], v[196:199]\n"
      "v_mul_f32 v230, v26, v246\n"
      "v_mfma_f32_16x16x32_bf16 v[200:203], a[172:175], a[24:27], v[200:203]\n"
      "v_mul_f32 v231, v26, v247\n"
      "v_mfma_f32_16x16x32_bf16 v[204:207], a[172:175], a[28:31], v[204:207]\n"
      "global_load_dwordx4 a[172:175], %0, s[20:21] offset:-1024\n"
      "v_mul_f32 v232, v28, v244\n"
      "s_waitcnt vmcnt(14)\n"
      "s_barrier\n"
      "s_add_u32 s26, s26, 12288\n"
      "s_addc_u32 s27, s27, 0\n"
      "s_add_u32 m0, s31, 61440\n"
      "s_nop 0\n"
      "global_load_lds_dwordx4 %0, s[26:27] offset:0 sc1\n"
      "global_load_lds_dwordx4 %0, s[26:27] offset:1024 sc1\n"
      "global_load_lds_dwordx4 %0, s[26:27] offset:2048 sc1\n"
      "s_waitcnt lgkmcnt(7)\n"
      "v_mfma_f32_16x16x32_bf16 v[176:179], a[176:179], v[64:67], v[176:179]\n"
      "v_mul_f32 v233, v28, v245\n"
      "ds_read_b128 v[80:83], %1 offset:24576\n"
      "ds_read_b128 v[84:87], %1 offset:25600\n"
      "ds_read_b128 a[48:51], %1 offset:26624\n"
      "ds_read_b128 a[52:55], %1 offset:27648\n"
      "s_waitcnt lgkmcnt(10)\n"
      "v_mfma_f32_16x16x32_bf16 v[180:183], a[176:179], v[68:71], v[180:183]\n"
      "v_mul_f32 v234, v28, v246\n"
      "s_waitcnt lgkmcnt(9)\n"
      "v_mfma_f32_16x16x32_bf16 v[184:187], a[176:179], a[32:35], v[184:187]\n"
      "v_mul_f32 v235, v28, v247\n"
      "s_waitcnt lgkmcnt(8)\n"
      "v_mfma_f32_16x16x32_bf16 v[188:191], a[176:179], a[36:39], v[188:191]\n"
      "global_load_dwordx4 a[176:179], %0, s[20:21] offset:0\n"
      "v_mul_f32 v236, v30, v244\n"
      "v_mfma_f32_16x16x32_bf16 v[192:195], a[180:183], v[64:67], v[192:195]\n"
      "v_mul_f32 v237, v30, v245\n"
      "v_mfma_f32_16x16x32_bf16 v[196:199], a[180:183], v[68:71], v[196:199]\n"
      "v_mul_f32 v238, v30, v246\n"
      "v_mfma_f32_16x16x32_bf16 v[200:203], a[180:183], a[32:35], v[200:203]\n"
      "v_mul_f32 v239, v30, v247\n"
      "v_mfma_f32_16x16x32_bf16 v[204:207], a[180:183], a[36:39], v[204:207]\n"
      "global_load_dwordx4 a[180:183], %0, s[20:21] offset:1024\n"
      "s_waitcnt lgkmcnt(7)\n"
      "v_mfma_f32_16x16x32_bf16 v[176:179], a[184:187], v[72:75], v[176:179]\n"
      "ds_read_b128 v[88:91], %1 offset:28672\n"
      "ds_read_b128 v[92:95], %1 offset:29696\n"
      "ds_read_b128 a[56:59], %1 offset:30720\n"
      "ds_read_b128 a[60:63], %1 offset:31744\n"
      "s_waitcnt lgkmcnt(10)\n"
      "v_mfma_f32_16x16x32_bf16 v[180:183], a[184:187], v[76:79], v[180:183]\n"
      "s_waitcnt lgkmcnt(9)\n"
      "v_mfma_f32_16x16x32_bf16 v[184:187], a[184:187], a[40:43], v[184:187]\n"
      "s_waitcnt lgkmcnt(8)\n"
      "v_mfma_f32_16x16x32_bf16 v[188:191], a[184:187], a[44:47], v[188:191]\n"
      "global_load_dwordx4 a[184:187], %0, s[20:21] offset:2048\n"
      "v_mfma_f32_16x16x32_bf16 v[192:195], a[188:191], v[72:75], v[192:195]\n"
      "v_mfma_f32_16x16x32_bf16 v[196:199], a[188:191], v[76:79], v[196:199]\n"
      "v_mfma_f32_16x16x32_bf16 v[200:203], a[188:191], a[40:43], v[200:203]\n"
      "v_mfma_f32_16x16x32_bf16 v[204:207], a[188:191], a[44:47], v[204:207]\n"
      "global_load_dwordx4 a[188:191], %0, s[20:21] offset:3072\n"
      "s_waitcnt lgkmcnt(7)\n"
      "v_mfma_f32_16x16x32_bf16 v[176:179], a[192:195], v[80:83], v[176:179]\n"
      "ds_read_b128 v[96:99], %1 offset:32768\n"
      "ds_read_b128 v[100:103], %1 offset:33792\n"
      "ds_read_b128 a[64:67], %1 offset:34816\n"
      "ds_read_b128 a[68:71], %1 offset:35840\n"
      "s_waitcnt lgkmcnt(10)\n"
      "v_mfma_f32_16x16x32_bf16 v[180:183], a[192:195], v[84:87], v[180:183]\n"
      "s_waitcnt lgkmcnt(9)\n"
      "v_mfma_f32_16x16x32_bf16 v[184:187], a[192:195], a[48:51], v[184:187]\n"
      "s_waitcnt lgkmcnt(8)\n"
      "v_mfma_f32_16x16x32_bf16 v[188:191], a[192:195], a[52:55], v[188:191]\n"
      "s_add_u32 s20, s20, 8192\n"
      "s_addc_u32 s21, s21, 0\n"
      "global_load_dwordx4 a[192:195], %0, s[20:21] offset:-4096\n"
      "v_mfma_f32_16x16x32_bf16 v[192:195], a[196:199], v[80:83], v[192:195]\n"
      "v_mfma_f32_16x16x32_bf16 v[196:199], a[196:199], v[84:87], v[196:199]\n"
      "v_mfma_f32_16x16x32_bf16 v[200:203], a[196:199], a[48:51], v[200:203]\n"
      "v_mfma_f32_16x16x32_bf16 v[204:207], a[196:199], a[52:55], v[204:207]\n"
      "global_load_dwordx4 a[196:199], %0, s[20:21] offset:-3072\n"
      "s_waitcnt vmcnt(20)\n"
      "s_barrier\n"
      "v_readfirstlane_b32 s31, %9\n"
      "s_mul_i32 s25, %14, 3072\n"
      "s_add_u32 s31, s31, s25\n"
      "s_add_u32 s25, %16, 0\n"
      "s_lshl_b32 s25, s25, 10\n"
      "s_mov_b64 s[26:27], %21\n"
      "s_add_u32 s26, s26, s25\n"
      "s_addc_u32 s27, s27, 0\n"
      "s_add_u32 m0, s31, 9216\n"
      "s_nop 0\n"
      "global_load_lds_dwordx4 %0, s[26:27] sc1\n"
      "s_add_u32 s25, %16, 8\n"
      "s_lshl_b32 s25, s25, 10\n"
      "s_mov_b64 s[26:27], %21\n"
      "s_add_u32 s26, s26, s25\n"
      "s_addc_u32 s27, s27, 0\n"
      "s_add_u32 m0, s31, 10240\n"
      "s_nop 0\n"
      "global_load_lds_dwordx4 %0, s[26:27] sc1\n"
      "s_add_u32 s25, %17, 0\n"
      "s_lshl_b32 s25, s25, 10\n"
      "s_mov_b64 s[26:27], %21\n"
      "s_add_u32 s26, s26, s25\n"
      "s_addc_u32 s27, s27, 0\n"
      "s_add_u32 m0, s31, 11264\n"
      "s_nop 0\n"
      "global_load_lds_dwordx4 %0, s[26:27] sc1\n"
      "s_waitcnt lgkmcnt(7)\n"
      "v_mfma_f32_16x16x32_bf16 v[176:179], a[200:203], v[88:91], v[176:179]\n"
      "ds_read_b128 v[104:107], %2 offset:0\n"
      "ds_read_b128 v[108:111], %2 offset:1024\n"
      "ds_read_b128 a[72:75], %2 offset:2048\n"
      "ds_read_b128 a[76:79], %2 offset:3072\n"
      "s_waitcnt lgkmcnt(10)\n"
      "v_mfma_f32_16x16x32_bf16 v[180:183], a[200:203], v[92:95], v[180:183]\n"
      "s_waitcnt lgkmcnt(9)\n"
      "v_mfma_f32_16x16x32_bf16 v[184:187], a[200:203], a[56:59], v[184:187]\n"
      "s_waitcnt lgkmcnt(8)\n"
      "v_mfma_f32_16x16x32_bf16 v[188:191], a[200:203], a[60:63], v[188:191]\n"
      "global_load_dwordx4 a[200:203], %0, s[20:21] offset:-2048\n"
      "v_mfma_f32_16x16x32_bf16 v[192:195], a[204:207], v[88:91], v[192:195]\n"
      "v_mfma_f32_16x16x32_bf16 v[196:199], a[204:207], v[92:95], v[196:199]\n"
      "v_mfma_f32_16x16x32_bf16 v[200:203], a[204:207], a[56:59], v[200:203]\n"
      "v_mfma_f32_16x16x32_bf16 v[204:207], a[204:207], a[60:63], v[204:207]\n"
      "global_load_dwordx4 a[204:207], %0, s[20:21] offset:-1024\n"
      "s_waitcnt lgkmcnt(7)\n"
      "v_mfma_f32_16x16x32_bf16 v[176:179], a[208:211], v[96:99], v[176:179]\n"
      "ds_read_b128 v[112:115], %2 offset:4096\n"
      "ds_read_b128 v[116:119], %2 offset:5120\n"
      "ds_read_b128 a[80:83], %2 offset:6144\n"
      "ds_read_b128 a[84:87], %2 offset:7168\n"
      "s_waitcnt lgkmcnt(10)\n"
      "v_mfma_f32_16x16x32_bf16 v[180:183], a[208:211], v[100:103], v[180:183]\n"
      "s_waitcnt lgkmcnt(9)\n"
      "v_mfma_f32_16x16x32_bf16 v[184:187], a[208:211], a[64:67], v[184:187]\n"
      "s_waitcnt lgkmcnt(8)\n"
      "v_mfma_f32_16x16x32_bf16 v[188:191], a[208:211], a[68:71], v[188:191]\n"
      "global_load_dwordx4 a[208:211], %0, s[20:21] offset:0\n"
      "v_mfma_f32_16x16x32_bf16 v[192:195], a[212:215], v[96:99], v[192:195]\n"
      "v_mfma_f32_16x16x32_bf16 v[196:199], a[212:215], v[100:103], v[196:199]\n"
      "v_mfma_f32_16x16x32_bf16 v[200:203], a[212:215], a[64:67], v[200:203]\n"
      "v_mfma_f32_16x16x32_bf16 v[204:207], a[212:215], a[68:71], v[204:207]\n"
      "global_load_dwordx4 a[212:215], %0, s[20:21] offset:1024\n"
      "s_waitcnt lgkmcnt(7)\n"
      "v_mfma_f32_16x16x32_bf16 v[176:179], a[216:219], v[104:107], v[176:179]\n"
      "ds_read_b128 v[120:123], %2 offset:8192\n"
      "ds_read_b128 v[124:127], %2 offset:9216\n"
      "ds_read_b128 a[88:91], %2 offset:10240\n"
      "ds_read_b128 a[92:95], %2 offset:11264\n"
      "s_waitcnt lgkmcnt(10)\n"
      "v_mfma_f32_16x16x32_bf16 v[180:183], a[216:219], v[108:111], v[180:183]\n"
      "s_waitcnt lgkmcnt(9)\n"
      "v_mfma_f32_16x16x32_bf16 v[184:187], a[216:219], a[72:75], v[184:187]\n"
      "s_waitcnt lgkmcnt(8)\n"
      "v_mfma_f32_16x16x32_bf16 v[188:191], a[216:219], a[76:79], v[188:191]\n"
      "global_load_dwordx4 a[216:219], %0, s[20:21] offset:2048\n"
      "v_mfma_f32_16x16x32_bf16 v[192:195], a[220:223], v[104:107], v[192:195]\n"
      "v_mfma_f32_16x16x32_bf16 v[196:199], a[220:223], v[108:111], v[196:199]\n"
      "v_mfma_f32_16x16x32_bf16 v[200:203], a[220:223], a[72:75], v[200:203]\n"
      "v_mfma_f32_16x16x32_bf16 v[204:207], a[220:223], a[76:79], v[204:207]\n"
      "global_load_dwordx4 a[220:223], %0, s[20:21] offset:3072\n"
      "s_waitcnt vmcnt(24)\n"
      "s_barrier\n"
      "s_waitcnt lgkmcnt(7)\n"
      "v_mfma_f32_16x16x32_bf16 v[176:179], a[224:227], v[112:115], v[176:179]\n"
      "ds_read_b128 v[128:131], %2 offset:12288\n"
      "ds_read_b128 v[132:135], %2 offset:13312\n"
      "ds_read_b128 a[96:99], %2 offset:14336\n"
      "ds_read_b128 a[100:103], %2 offset:15360\n"
      "s_waitcnt lgkmcnt(10)\n"
      "v_mfma_f32_16x16x32_bf16 v[180:183], a[224:227], v[116:119], v[180:183]\n"
      "s_waitcnt lgkmcnt(9)\n"
      "v_mfma_f32_16x16x32_bf16 v[184:187], a[224:227], a[80:83], v[184:187]\n"
      "s_waitcnt lgkmcnt(8)\n"
      "v_mfma_f32_16x16x32_bf16 v[188:191], a[224:227], a[84:87], v[188:191]\n"
      "s_add_u32 s20, s20, 8192\n"
      "s_addc_u32 s21, s21, 0\n"
      "global_load_dwordx4 a[224:227], %0, s[20:21] offset:-4096\n"
      "v_mfma_f32_16x16x32_bf16 v[192:195], a[228:231], v[112:115], v[192:195]\n"
      "v_mfma_f32_16x16x32_bf16 v[196:199], a[228:231], v[116:119], v[196:199]\n"
      "v_mfma_f32_16x16x32_bf16 v[200:203], a[228:231], a[80:83], v[200:203]\n"
      "v_mfma_f32_16x16x32_bf16 v[204:207], a[228:231], a[84:87], v[204:207]\n"
      "global_load_dwordx4 a[228:231], %0, s[20:21] offset:-3072\n"
      "s_waitcnt lgkmcnt(7)\n"
      "v_mfma_f32_16x16x32_bf16 v[176:179], a[232:235], v[120:123], v[176:179]\n"
      "ds_read_b128 v[136:139], %2 offset:16384\n"
      "ds_read_b128 v[140:143], %2 offset:17408\n"
      "ds_read_b128 a[104:107], %2 offset:18432\n"
      "ds_read_b128 a[108:111], %2 offset:19456\n"
      "s_waitcnt lgkmcnt(10)\n"
      "v_mfma_f32_16x16x32_bf16 v[180:183], a[232:235], v[124:127], v[180:183]\n"
      "s_waitcnt lgkmcnt(9)\n"
      "v_mfma_f32_16x16x32_bf16 v[184:187], a[232:235], a[88:91], v[184:187]\n"
      "s_waitcnt lgkmcnt(8)\n"
      "v_mfma_f32_16x16x32_bf16 v[188:191], a[232:235], a[92:95], v[188:191]\n"
      "global_load_dwordx4 a[232:235], %0, s[20:21] offset:-2048\n"
      "v_mfma_f32_16x16x32_bf16 v[192:195], a[236:239], v[120:123], v[192:195]\n"
      "v_mfma_f32_16x16x32_bf16 v[196:199], a[236:239], v[124:127], v[196:199]\n"
      "v_mfma_f32_16x16x32_bf16 v[200:203], a[236:239], a[88:91], v[200:203]\n"
      "v_mfma_f32_16x16x32_bf16 v[204:207], a[236:239], a[92:95], v[204:207]\n"
      "global_load_dwordx4 a[236:239], %0, s[20:21] offset:-1024\n"
      "s_waitcnt lgkmcnt(7)\n"
      "v_mfma_f32_16x16x32_bf16 v[176:179], a[240:243], v[128:131], v[176:179]\n"
      "ds_read_b128 v[144:147], %2 offset:20480\n"
      "ds_read_b128 v[148:151], %2 offset:21504\n"
      "ds_read_b128 a[112:115], %2 offset:22528\n"
      "ds_read_b128 a[116:119], %2 offset:23552\n"
      "s_waitcnt lgkmcnt(10)\n"
      "v_mfma_f32_16x16x32_bf16 v[180:183], a[240:243], v[132:135], v[180:183]\n"
      "s_waitcnt lgkmcnt(9)\n"
      "v_mfma_f32_16x16x32_bf16 v[184:187], a[240:243], a[96:99], v[184:187]\n"
      "s_waitcnt lgkmcnt(8)\n"
      "v_mfma_f32_16x16x32_bf16 v[188:191], a[240:243], a[100:103], v[188:191]\n"
      "global_load_dwordx4 a[240:243], %0, s[20:21] offset:0\n"
      "v_mfma_f32_16x16x32_bf16 v[192:195], a[244:247], v[128:131], v[192:195]\n"
      "v_mfma_f32_16x16x32_bf16 v[196:199], a[244:247], v[132:135], v[196:199]\n"
      "v_mfma_f32_16x16x32_bf16 v[200:203], a[244:247], a[96:99], v[200:203]\n"
      "v_mfma_f32_16x16x32_bf16 v[204:207], a[244:247], a[100:103], v[204:207]\n"
      "global_load_dwordx4 a[244:247], %0, s[20:21] offset:1024\n"
      "s_waitcnt vmcnt(21)\n"
      "s_barrier\n"
      "s_waitcnt lgkmcnt(7)\n"
      "v_mfma_f32_16x16x32_bf16 v[176:179], a[248:251], v[136:139], v[176:179]\n"
      "ds_read_b128 v[152:155], %2 offset:24576\n"
      "ds_read_b128 v[156:159], %2 offset:25600\n"
      "ds_read_b128 a[120:123], %2 offset:26624\n"
      "ds_read_b128 a[124:127], %2 offset:27648\n"
      "s_waitcnt lgkmcnt(10)\n"
      "v_mfma_f32_16x16x32_bf16 v[180:183], a[248:251], v[140:143], v[180:183]\n"
      "s_waitcnt lgkmcnt(9)\n"
      "v_mfma_f32_16x16x32_bf16 v[184:187], a[248:251], a[104:107], v[184:187]\n"
      "s_waitcnt lgkmcnt(8)\n"
      "v_mfma_f32_16x16x32_bf16 v[188:191], a[248:251], a[108:111], v[188:191]\n"
      "global_load_dwordx4 a[248:251], %0, s[20:21] offset:2048\n"
      "v_mfma_f32_16x16x32_bf16 v[192:195], a[252:255], v[136:139], v[192:195]\n"
      "v_mfma_f32_16x16x32_bf16 v[196:199], a[252:255], v[140:143], v[196:199]\n"
      "v_mfma_f32_16x16x32_bf16 v[200:203], a[252:255], a[104:107], v[200:203]\n"
      "v_mfma_f32_16x16x32_bf16 v[204:207], a[252:255], a[108:111], v[204:207]\n"
      "global_load_dwordx4 a[252:255], %0, s[20:21] offset:3072\n"
      "s_waitcnt lgkmcnt(7)\n"
      "v_mfma_f32_16x16x32_bf16 v[176:179], a[144:147], v[144:147], v[176:179]\n"
      "ds_read_b128 v[160:163], %2 offset:28672\n"
      "ds_read_b128 v[164:167], %2 offset:29696\n"
      "ds_read_b128 a[128:131], %2 offset:30720\n"
      "ds_read_b128 a[132:135], %2 offset:31744\n"
      "s_waitcnt lgkmcnt(10)\n"
      "v_mfma_f32_16x16x32_bf16 v[180:183], a[144:147], v[148:151], v[180:183]\n"
      "s_waitcnt lgkmcnt(9)\n"
      "v_mfma_f32_16x16x32_bf16 v[184:187], a[144:147], a[112:115], v[184:187]\n"
      "s_waitcnt lgkmcnt(8)\n"
      "v_mfma_f32_16x16x32_bf16 v[188:191], a[144:147], a[116:119], v[188:191]\n"
      "s_add_u32 s20, s20, 8192\n"
      "s_addc_u32 s21, s21, 0\n"
      "global_load_dwordx4 a[144:147], %0, s[20:21] offset:-4096\n"
      "v_mfma_f32_16x16x32_bf16 v[192:195], a[148:151], v[144:147], v[192:195]\n"
      "v_mfma_f32_16x16x32_bf16 v[196:199], a[148:151], v[148:151], v[196:199]\n"
      "v_mfma_f32_16x16x32_bf16 v[200:203], a[148:151], a[112:115], v[200:203]\n"
      "v_mfma_f32_16x16x32_bf16 v[204:207], a[148:151], a[116:119], v[204:207]\n"
      "global_load_dwordx4 a[148:151], %0, s[20:21] offset:-3072\n"
      "s_waitcnt lgkmcnt(7)\n"
      "v_mfma_f32_16x16x32_bf16 v[176:179], a[152:155], v[152:155], v[176:179]\n"
      "ds_read_b128 v[168:171], %2 offset:32768\n"
      "ds_read_b128 v[172:175], %2 offset:33792\n"
      "ds_read_b128 a[136:139], %2 offset:34816\n"
      "ds_read_b128 a[140:143], %2 offset:35840\n"
      "s_waitcnt lgkmcnt(10)\n"
      "v_mfma_f32_16x16x32_bf16 v[180:183], a[152:155], v[156:159], v[180:183]\n"
      "s_waitcnt lgkmcnt(9)\n"
      "v_mfma_f32_16x16x32_bf16 v[184:187], a[152:155], a[120:123], v[184:187]\n"
      "s_waitcnt lgkmcnt(8)\n"
      "v_mfma_f32_16x16x32_bf16 v[188:191], a[152:155], a[124:127], v[188:191]\n"
      "global_load_dwordx4 a[152:155], %0, s[20:21] offset:-2048\n"
      "v_mfma_f32_16x16x32_bf16 v[192:195], a[156:159], v[152:155], v[192:195]\n"
      "v_mfma_f32_16x16x32_bf16 v[196:199], a[156:159], v[156:159], v[196:199]\n"
      "v_mfma_f32_16x16x32_bf16 v[200:203], a[156:159], a[120:123], v[200:203]\n"
      "v_mfma_f32_16x16x32_bf16 v[204:207], a[156:159], a[124:127], v[204:207]\n"
      "global_load_dwordx4 a[156:159], %0, s[20:21] offset:-1024\n"
      "s_waitcnt lgkmcnt(7)\n"
      "v_mfma_f32_16x16x32_bf16 v[176:179], a[160:163], v[160:163], v[176:179]\n"
      "s_waitcnt lgkmcnt(6)\n"
      "v_mfma_f32_16x16x32_bf16 v[180:183], a[160:163], v[164:167], v[180:183]\n"
      "s_waitcnt lgkmcnt(5)\n"
      "v_mfma_f32_16x16x32_bf16 v[184:187], a[160:163], a[128:131], v[184:187]\n"
      "s_waitcnt lgkmcnt(4)\n"
      "v_mfma_f32_16x16x32_bf16 v[188:191], a[160:163], a[132:135], v[188:191]\n"
      "global_load_dwordx4 a[160:163], %0, s[20:21] offset:0\n"
      "v_mfma_f32_16x16x32_bf16 v[192:195], a[164:167], v[160:163], v[192:195]\n"
      "v_mfma_f32_16x16x32_bf16 v[196:199], a[164:167], v[164:167], v[196:199]\n"
      "v_mfma_f32_16x16x32_bf16 v[200:203], a[164:167], a[128:131], v[200:203]\n"
      "v_mfma_f32_16x16x32_bf16 v[204:207], a[164:167], a[132:135], v[204:207]\n"
      "global_load_dwordx4 a[164:167], %0, s[20:21] offset:1024\n"
      "s_waitcnt lgkmcnt(3)\n"
      "v_mfma_f32_16x16x32_bf16 v[176:179], a[168:171], v[168:171], v[176:179]\n"
      "s_waitcnt lgkmcnt(2)\n"
      "v_mfma_f32_16x16x32_bf16 v[180:183], a[168:171], v[172:175], v[180:183]\n"
      "s_waitcnt lgkmcnt(1)\n"
      "v_mfma_f32_16x16x32_bf16 v[184:187], a[168:171], a[136:139], v[184:187]\n"
      "s_waitcnt lgkmcnt(0)\n"
      "v_mfma_f32_16x16x32_bf16 v[188:191], a[168:171], a[140:143], v[188:191]\n"
      "global_load_dwordx4 a[168:171], %0, s[20:21] offset:2048\n"
      "v_mfma_f32_16x16x32_bf16 v[192:195], a[172:175], v[168:171], v[192:195]\n"
      "v_mfma_f32_16x16x32_bf16 v[196:199], a[172:175], v[172:175], v[196:199]\n"
      "v_mfma_f32_16x16x32_bf16 v[200:203], a[172:175], a[136:139], v[200:203]\n"
      "v_mfma_f32_16x16x32_bf16 v[204:207], a[172:175], a[140:143], v[204:207]\n"
      "global_load_dwordx4 a[172:175], %0, s[20:21] offset:3072\n"
      "s_nop 1\n"
      "s_waitcnt lgkmcnt(0)\n"
      "s_barrier\n"
      "s_waitcnt vmcnt(29)\n"
      "v_mfma_f32_16x16x32_bf16 v[208:211], a[176:179], v[32:35], v[208:211]\n"
      "v_mfma_f32_16x16x32_bf16 v[212:215], a[176:179], v[36:39], v[212:215]\n"
      "v_mfma_f32_16x16x32_bf16 v[216:219], a[176:179], a[0:3], v[216:219]\n"
      "v_mfma_f32_16x16x32_bf16 v[220:223], a[176:179], a[4:7], v[220:223]\n"
      "s_add_u32 s20, s20, 8192\n"
      "s_addc_u32 s21, s21, 0\n"
      "global_load_dwordx4 a[176:179], %0, s[20:21] offset:-4096\n"
      "v_mfma_f32_16x16x32_bf16 v[224:227], a[180:183], v[32:35], v[224:227]\n"
      "v_mfma_f32_16x16x32_bf16 v[228:231], a[180:183], v[36:39], v[228:231]\n"
      "v_mfma_f32_16x16x32_bf16 v[232:235], a[180:183], a[0:3], v[232:235]\n"
      "v_mfma_f32_16x16x32_bf16 v[236:239], a[180:183], a[4:7], v[236:239]\n"
      "global_load_dwordx4 a[180:183], %0, s[20:21] offset:-3072\n"
      "s_waitcnt vmcnt(29)\n"
      "v_mfma_f32_16x16x32_bf16 v[208:211], a[184:187], v[40:43], v[208:211]\n"
      "v_cvt_pk_bf16_f32 v240, v176, v177\n"
      "v_mfma_f32_16x16x32_bf16 v[212:215], a[184:187], v[44:47], v[212:215]\n"
      "v_cvt_pk_bf16_f32 v241, v178, v179\n"
      "v_mfma_f32_16x16x32_bf16 v[216:219], a[184:187], a[8:11], v[216:219]\n"
      "v_cvt_pk_bf16_f32 v242, v192, v193\n"
      "v_mfma_f32_16x16x32_bf16 v[220:223], a[184:187], a[12:15], v[220:223]\n"
      "global_load_dwordx4 a[184:187], %0, s[20:21] offset:-2048\n"
      "v_cvt_pk_bf16_f32 v243, v194, v195\n"
      "v_mfma_f32_16x16x32_bf16 v[224:227], a[188:191], v[40:43], v[224:227]\n"
      "v_pk_max_i16 v240, v240, 0\n"
      "v_mfma_f32_16x16x32_bf16 v[228:231], a[188:191], v[44:47], v[228:231]\n"
      "v_pk_max_i16 v241, v241, 0\n"
      "v_mfma_f32_16x16x32_bf16 v[232:235], a[188:191], a[8:11], v[232:235]\n"
      "v_pk_max_i16 v242, v242, 0\n"
      "v_mfma_f32_16x16x32_bf16 v[236:239], a[188:191], a[12:15], v[236:239]\n"
      "global_load_dwordx4 a[188:191], %0, s[20:21] offset:-1024\n"
      "v_pk_max_i16 v243, v243, 0\n"
      "s_waitcnt vmcnt(29)\n"
      "v_mfma_f32_16x16x32_bf16 v[208:211], a[192:195], v[48:51], v[208:211]\n"
      "ds_write_b128 %3, v[240:243] offset:0\n"
      "v_mfma_f32_16x16x32_bf16 v[212:215], a[192:195], v[52:55], v[212:215]\n"
      "v_cvt_pk_bf16_f32 v244, v180, v181\n"
      "v_mfma_f32_16x16x32_bf16 v[216:219], a[192:195], a[16:19], v[216:219]\n"
      "v_cvt_pk_bf16_f32 v245, v182, v183\n"
      "v_mfma_f32_16x16x32_bf16 v[220:223], a[192:195], a[20:23], v[220:223]\n"
      "global_load_dwordx4 a[192:195], %0, s[20:21] offset:0\n"
      "v_cvt_pk_bf16_f32 v246, v196, v197\n"
      "v_mfma_f32_16x16x32_bf16 v[224:227], a[196:199], v[48:51], v[224:227]\n"
      "v_cvt_pk_bf16_f32 v247, v198, v199\n"
      "v_mfma_f32_16x16x32_bf16 v[228:231], a[196:199], v[52:55], v[228:231]\n"
      "v_pk_max_i16 v244, v244, 0\n"
      "v_mfma_f32_16x16x32_bf16 v[232:235], a[196:199], a[16:19], v[232:235]\n"
      "v_pk_max_i16 v245, v245, 0\n"
      "v_mfma_f32_16x16x32_bf16 v[236:239], a[196:199], a[20:23], v[236:239]\n"
      "global_load_dwordx4 a[196:199], %0, s[20:21] offset:1024\n"
      "v_pk_max_i16 v246, v246, 0\n"
      "s_waitcnt vmcnt(26)\n"
      "v_mfma_f32_16x16x32_bf16 v[208:211], a[200:203], v[56:59], v[208:211]\n"
      "v_pk_max_i16 v247, v247, 0\n"
      "v_mfma_f32_16x16x32_bf16 v[212:215], a[200:203], v[60:63], v[212:215]\n"
      "ds_write_b128 %3, v[244:247] offset:1024\n"
      "v_mfma_f32_16x16x32_bf16 v[216:219], a[200:203], a[24:27], v[216:219]\n"
      "v_cvt_pk_bf16_f32 v248, v184, v185\n"
      "v_mfma_f32_16x16x32_bf16 v[220:223], a[200:203], a[28:31], v[220:223]\n"
      "global_load_dwordx4 a[200:203], %0, s[20:21] offset:2048\n"
      "v_cvt_pk_bf16_f32 v249, v186, v187\n"
      "v_mfma_f32_16x16x32_bf16 v[224:227], a[204:207], v[56:59], v[224:227]\n"
      "v_cvt_pk_bf16_f32 v250, v200, v201\n"
      "v_mfma_f32_16x16x32_bf16 v[228:231], a[204:207], v[60:63], v[228:231]\n"
      "v_cvt_pk_bf16_f32 v251, v202, v203\n"
      "v_mfma_f32_16x16x32_bf16 v[232:235], a[204:207], a[24:27], v[232:235]\n"
      "v_pk_max_i16 v248, v248, 0\n"
      "v_mfma_f32_16x16x32_bf16 v[236:239], a[204:207], a[28:31], v[236:239]\n"
      "global_load_dwordx4 a[204:207], %0, s[20:21] offset:3072\n"
      "v_pk_max_i16 v249, v249, 0\n"
      "s_waitcnt vmcnt(26)\n"
      "v_mfma_f32_16x16x32_bf16 v[208:211], a[208:211], v[64:67], v[208:211]\n"
      "v_pk_max_i16 v250, v250, 0\n"
      "v_mfma_f32_16x16x32_bf16 v[212:215], a[208:211], v[68:71], v[212:215]\n"
      "v_pk_max_i16 v251, v251, 0\n"
      "v_mfma_f32_16x16x32_bf16 v[216:219], a[208:211], a[32:35], v[216:219]\n"
      "ds_write_b128 %3, v[248:251] offset:2048\n"
      "v_mfma_f32_16x16x32_bf16 v[220:223], a[208:211], a[36:39], v[220:223]\n"
      "s_add_u32 s20, s20, 8192\n"
      "s_addc_u32 s21, s21, 0\n"
      "global_load_dwordx4 a[208:211], %0, s[20:21] offset:-4096\n"
      "v_cvt_pk_bf16_f32 v252, v188, v189\n"
      "v_mfma_f32_16x16x32_bf16 v[224:227], a[212:215], v[64:67], v[224:227]\n"
      "v_cvt_pk_bf16_f32 v253, v190, v191\n"
      "v_mfma_f32_16x16x32_bf16 v[228:231], a[212:215], v[68:71], v[228:231]\n"
      "v_cvt_pk_bf16_f32 v254, v204, v205\n"
      "v_mfma_f32_16x16x32_bf16 v[232:235], a[212:215], a[32:35], v[232:235]\n"
      "v_cvt_pk_bf16_f32 v255, v206, v207\n"
      "v_mfma_f32_16x16x32_bf16 v[236:239], a[212:215], a[36:39], v[236:239]\n"
      "global_load_dwordx4 a[212:215], %0, s[20:21] offset:-3072\n"
      "v_pk_max_i16 v252, v252, 0\n"
      "s_waitcnt vmcnt(26)\n"
      "v_mfma_f32_16x16x32_bf16 v[208:211], a[216:219], v[72:75], v[208:211]\n"
      "v_pk_max_i16 v253, v253, 0\n"
      "v_mfma_f32_16x16x32_bf16 v[212:215], a[216:219], v[76:79], v[212:215]\n"
      "v_pk_max_i16 v254, v254, 0\n"
      "v_mfma_f32_16x16x32_bf16 v[216:219], a[216:219], a[40:43], v[216:219]\n"
      "v_pk_max_i16 v255, v255, 0\n"
      "v_mfma_f32_16x16x32_bf16 v[220:223], a[216:219], a[44:47], v[220:223]\n"
      "global_load_dwordx4 a[216:219], %0, s[20:21] offset:-2048\n"
      "ds_write_b128 %3, v[252:255] offset:3072\n"
      "v_mfma_f32_16x16x32_bf16 v[224:227], a[220:223], v[72:75], v[224:227]\n"
      "ds_read_b128 v[240:243], %7 offset:384\n"
      "v_mfma_f32_16x16x32_bf16 v[228:231], a[220:223], v[76:79], v[228:231]\n"
      "ds_read_b128 v[244:247], %7 offset:400\n"
      "v_mfma_f32_16x16x32_bf16 v[232:235], a[220:223], a[40:43], v[232:235]\n"
      "v_mfma_f32_16x16x32_bf16 v[236:239], a[220:223], a[44:47], v[236:239]\n"
      "global_load_dwordx4 a[220:223], %0, s[20:21] offset:-1024\n"
      "s_waitcnt vmcnt(26)\n"
      "v_mfma_f32_16x16x32_bf16 v[208:211], a[224:227], v[80:83], v[208:211]\n"
      "v_mfma_f32_16x16x32_bf16 v[212:215], a[224:227], v[84:87], v[212:215]\n"
      "v_mfma_f32_16x16x32_bf16 v[216:219], a[224:227], a[48:51], v[216:219]\n"
      "s_waitcnt lgkmcnt(0)\n"
      "v_mfma_f32_16x16x32_bf16 v[220:223], a[224:227], a[52:55], v[220:223]\n"
      "global_load_dwordx4 a[224:227], %0, s[20:21] offset:0\n"
      "v_mul_f32 v176, v24, v240\n"
      "v_mfma_f32_16x16x32_bf16 v[224:227], a[228:231], v[80:83], v[224:227]\n"
      "v_mul_f32 v177, v24, v241\n"
      "v_mfma_f32_16x16x32_bf16 v[228:231], a[228:231], v[84:87], v[228:231]\n"
      "v_mul_f32 v178, v24, v242\n"
      "v_mfma_f32_16x16x32_bf16 v[232:235], a[228:231], a[48:51], v[232:235]\n"
      "v_mul_f32 v179, v24, v243\n"
      "v_mfma_f32_16x16x32_bf16 v[236:239], a[228:231], a[52:55], v[236:239]\n"
      "global_load_dwordx4 a[228:231], %0, s[20:21] offset:1024\n"
      "v_mul_f32 v180, v26, v240\n"
      "s_waitcnt vmcnt(26)\n"
      "v_mfma_f32_16x16x32_bf16 v[208:211], a[232:235], v[88:91], v[208:211]\n"
      "v_mul_f32 v181, v26, v241\n"
      "v_mfma_f32_16x16x32_bf16 v[212:215], a[232:235], v[92:95], v[212:215]\n"
      "v_mul_f32 v182, v26, v242\n"
      "v_mfma_f32_16x16x32_bf16 v[216:219], a[232:235], a[56:59], v[216:219]\n"
      "v_mul_f32 v183, v26, v243\n"
      "v_mfma_f32_16x16x32_bf16 v[220:223], a[232:235], a[60:63], v[220:223]\n"
      "global_load_dwordx4 a[232:235], %0, s[20:21] offset:2048\n"
      "v_mul_f32 v184, v28, v240\n"
      "v_mfma_f32_16x16x32_bf16 v[224:227], a[236:239], v[88:91], v[224:227]\n"
      "v_mul_f32 v185, v28, v241\n"
      "v_mfma_f32_16x16x32_bf16 v[228:231], a[236:239], v[92:95], v[228:231]\n"
      "v_mul_f32 v186, v28, v242\n"
      "v_mfma_f32_16x16x32_bf16 v[232:235], a[236:239], a[56:59], v[232:235]\n"
      "v_mul_f32 v187, v28, v243\n"
      "v_mfma_f32_16x16x32_bf16 v[236:239], a[236:239], a[60:63], v[236:239]\n"
      "global_load_dwordx4 a[236:239], %0, s[20:21] offset:3072\n"
      "v_mul_f32 v188, v30, v240\n"
      "s_waitcnt vmcnt(26)\n"
      "v_mfma_f32_16x16x32_bf16 v[208:211], a[240:243], v[96:99], v[208:211]\n"
      "v_mul_f32 v189, v30, v241\n"
      "v_mfma_f32_16x16x32_bf16 v[212:215], a[240:243], v[100:103], v[212:215]\n"
      "v_mul_f32 v190, v30, v242\n"
      "v_mfma_f32_16x16x32_bf16 v[216:219], a[240:243], a[64:67], v[216:219]\n"
      "v_mul_f32 v191, v30, v243\n"
      "v_mfma_f32_16x16x32_bf16 v[220:223], a[240:243], a[68:71], v[220:223]\n"
      "s_add_u32 s20, s20, 8192\n"
      "s_addc_u32 s21, s21, 0\n"
      "global_load_dwordx4 a[240:243], %0, s[20:21] offset:-4096\n"
      "v_mul_f32 v192, v24, v244\n"
      "v_mfma_f32_16x16x32_bf16 v[224:227], a[244:247], v[96:99], v[224:227]\n"
      "v_mul_f32 v193, v24, v245\n"
      "v_mfma_f32_16x16x32_bf16 v[228:231], a[244:247], v[100:103], v[228:231]\n"
      "v_mul_f32 v194, v24, v246\n"
      "v_mfma_f32_16x16x32_bf16 v[232:235], a[244:247], a[64:67], v[232:235]\n"
      "v_mul_f32 v195, v24, v247\n"
      "v_mfma_f32_16x16x32_bf16 v[236:239], a[244:247], a[68:71], v[236:239]\n"
      "global_load_dwordx4 a[244:247], %0, s[20:21] offset:-3072\n"
      "v_mul_f32 v196, v26, v244\n"
      "s_waitcnt vmcnt(26)\n"
      "v_mfma_f32_16x16x32_bf16 v[208:211], a[248:251], v[104:107], v[208:211]\n"
      "v_mul_f32 v197, v26, v245\n"
      "v_mfma_f32_16x16x32_bf16 v[212:215], a[248:251], v[108:111], v[212:215]\n"
      "v_mul_f32 v198, v26, v246\n"
      "v_mfma_f32_16x16x32_bf16 v[216:219], a[248:251], a[72:75], v[216:219]\n"
      "v_mul_f32 v199, v26, v247\n"
      "v_mfma_f32_16x16x32_bf16 v[220:223], a[248:251], a[76:79], v[220:223]\n"
      "global_load_dwordx4 a[248:251], %0, s[20:21] offset:-2048\n"
      "v_mul_f32 v200, v28, v244\n"
      "v_mfma_f32_16x16x32_bf16 v[224:227], a[252:255], v[104:107], v[224:227]\n"
      "v_mul_f32 v201, v28, v245\n"
      "v_mfma_f32_16x16x32_bf16 v[228:231], a[252:255], v[108:111], v[228:231]\n"
      "v_mul_f32 v202, v28, v246\n"
      "v_mfma_f32_16x16x32_bf16 v[232:235], a[252:255], a[72:75], v[232:235]\n"
      "v_mul_f32 v203, v28, v247\n"
      "v_mfma_f32_16x16x32_bf16 v[236:239], a[252:255], a[76:79], v[236:239]\n"
      "global_load_dwordx4 a[252:255], %0, s[20:21] offset:-1024\n"
      "v_mul_f32 v204, v30, v244\n"
      "s_waitcnt vmcnt(26)\n"
      "v_mfma_f32_16x16x32_bf16 v[208:211], a[144:147], v[112:115], v[208:211]\n"
      "v_mul_f32 v205, v30, v245\n"
      "v_mfma_f32_16x16x32_bf16 v[212:215], a[144:147], v[116:119], v[212:215]\n"
      "v_mul_f32 v206, v30, v246\n"
      "v_mfma_f32_16x16x32_bf16 v[216:219], a[144:147], a[80:83], v[216:219]\n"
      "v_mul_f32 v207, v30, v247\n"
      "v_mfma_f32_16x16x32_bf16 v[220:223], a[144:147], a[84:87], v[220:223]\n"
      "global_load_dwordx4 a[144:147], %0, s[20:21] offset:0\n"
      "v_mfma_f32_16x16x32_bf16 v[224:227], a[148:151], v[112:115], v[224:227]\n"
      "v_mfma_f32_16x16x32_bf16 v[228:231], a[148:151], v[116:119], v[228:231]\n"
      "v_mfma_f32_16x16x32_bf16 v[232:235], a[148:151], a[80:83], v[232:235]\n"
      "v_mfma_f32_16x16x32_bf16 v[236:239], a[148:151], a[84:87], v[236:239]\n"
      "global_load_dwordx4 a[148:151], %0, s[20:21] offset:1024\n"
      "s_waitcnt vmcnt(26)\n"
      "v_mfma_f32_16x16x32_bf16 v[208:211], a[152:155], v[120:123], v[208:211]\n"
      "v_mfma_f32_16x16x32_bf16 v[212:215], a[152:155], v[124:127], v[212:215]\n"
      "v_mfma_f32_16x16x32_bf16 v[216:219], a[152:155], a[88:91], v[216:219]\n"
      "v_mfma_f32_16x16x32_bf16 v[220:223], a[152:155], a[92:95], v[220:223]\n"
      "global_load_dwordx4 a[152:155], %0, s[20:21] offset:2048\n"
      "v_mfma_f32_16x16x32_bf16 v[224:227], a[156:159], v[120:123], v[224:227]\n"
      "v_mfma_f32_16x16x32_bf16 v[228:231], a[156:159], v[124:127], v[228:231]\n"
      "v_mfma_f32_16x16x32_bf16 v[232:235], a[156:159], a[88:91], v[232:235]\n"
      "v_mfma_f32_16x16x32_bf16 v[236:239], a[156:159], a[92:95], v[236:239]\n"
      "global_load_dwordx4 a[156:159], %0, s[20:21] offset:3072\n"
      "s_waitcnt vmcnt(26)\n"
      "v_mfma_f32_16x16x32_bf16 v[208:211], a[160:163], v[128:131], v[208:211]\n"
      "v_mfma_f32_16x16x32_bf16 v[212:215], a[160:163], v[132:135], v[212:215]\n"
      "v_mfma_f32_16x16x32_bf16 v[216:219], a[160:163], a[96:99], v[216:219]\n"
      "v_mfma_f32_16x16x32_bf16 v[220:223], a[160:163], a[100:103], v[220:223]\n"
      "s_add_u32 s20, s20, 8192\n"
      "s_addc_u32 s21, s21, 0\n"
      "global_load_dwordx4 a[160:163], %0, s[20:21] offset:-4096\n"
      "v_mfma_f32_16x16x32_bf16 v[224:227], a[164:167], v[128:131], v[224:227]\n"
      "v_mfma_f32_16x16x32_bf16 v[228:231], a[164:167], v[132:135], v[228:231]\n"
      "v_mfma_f32_16x16x32_bf16 v[232:235], a[164:167], a[96:99], v[232:235]\n"
      "v_mfma_f32_16x16x32_bf16 v[236:239], a[164:167], a[100:103], v[236:239]\n"
      "global_load_dwordx4 a[164:167], %0, s[20:21] offset:-3072\n"
      "s_waitcnt vmcnt(26)\n"
      "v_mfma_f32_16x16x32_bf16 v[208:211], a[168:171], v[136:139], v[208:211]\n"
      "v_mfma_f32_16x16x32_bf16 v[212:215], a[168:171], v[140:143], v[212:215]\n"
      "v_mfma_f32_16x16x32_bf16 v[216:219], a[168:171], a[104:107], v[216:219]\n"
      "v_mfma_f32_16x16x32_bf16 v[220:223], a[168:171], a[108:111], v[220:223]\n"
      "global_load_dwordx4 a[168:171], %0, s[20:21] offset:-2048\n"
      "v_mfma_f32_16x16x32_bf16 v[224:227], a[172:175], v[136:139], v[224:227]\n"
      "v_mfma_f32_16x16x32_bf16 v[228:231], a[172:175], v[140:143], v[228:231]\n"
      "v_mfma_f32_16x16x32_bf16 v[232:235], a[172:175], a[104:107], v[232:235]\n"
      "v_mfma_f32_16x16x32_bf16 v[236:239], a[172:175], a[108:111], v[236:239]\n"
      "global_load_dwordx4 a[172:175], %0, s[20:21] offset:-1024\n"
      "s_waitcnt vmcnt(26)\n"
      "v_mfma_f32_16x16x32_bf16 v[208:211], a[176:179], v[144:147], v[208:211]\n"
      "v_mfma_f32_16x16x32_bf16 v[212:215], a[176:179], v[148:151], v[212:215]\n"
      "v_mfma_f32_16x16x32_bf16 v[216:219], a[176:179], a[112:115], v[216:219]\n"
      "v_mfma_f32_16x16x32_bf16 v[220:223], a[176:179], a[116:119], v[220:223]\n"
      "global_load_dwordx4 a[176:179], %0, s[20:21] offset:0\n"
      "v_mfma_f32_16x16x32_bf16 v[224:227], a[180:183], v[144:147], v[224:227]\n"
      "v_mfma_f32_16x16x32_bf16 v[228:231], a[180:183], v[148:151], v[228:231]\n"
      "v_mfma_f32_16x16x32_bf16 v[232:235], a[180:183], a[112:115], v[232:235]\n"
      "v_mfma_f32_16x16x32_bf16 v[236:239], a[180:183], a[116:119], v[236:239]\n"
      "global_load_dwordx4 a[180:183], %0, s[20:21] offset:1024\n"
      "s_waitcnt vmcnt(26)\n"
      "v_mfma_f32_16x16x32_bf16 v[208:211], a[184:187], v[152:155], v[208:211]\n"
      "v_mfma_f32_16x16x32_bf16 v[212:215], a[184:187], v[156:159], v[212:215]\n"
      "v_mfma_f32_16x16x32_bf16 v[216:219], a[184:187], a[120:123], v[216:219]\n"
      "v_mfma_f32_16x16x32_bf16 v[220:223], a[184:187], a[124:127], v[220:223]\n"
      "global_load_dwordx4 a[184:187], %0, s[20:21] offset:2048\n"
      "v_mfma_f32_16x16x32_bf16 v[224:227], a[188:191], v[152:155], v[224:227]\n"
      "v_mfma_f32_16x16x32_bf16 v[228:231], a[188:191], v[156:159], v[228:231]\n"
      "v_mfma_f32_16x16x32_bf16 v[232:235], a[188:191], a[120:123], v[232:235]\n"
      "v_mfma_f32_16x16x32_bf16 v[236:239], a[188:191], a[124:127], v[236:239]\n"
      "global_load_dwordx4 a[188:191], %0, s[20:21] offset:3072\n"
      "s_waitcnt vmcnt(26)\n"
      "v_mfma_f32_16x16x32_bf16 v[208:211], a[192:195], v[160:163], v[208:211]\n"
      "v_mfma_f32_16x16x32_bf16 v[212:215], a[192:195], v[164:167], v[212:215]\n"
      "v_mfma_f32_16x16x32_bf16 v[216:219], a[192:195], a[128:131], v[216:219]\n"
      "v_mfma_f32_16x16x32_bf16 v[220:223], a[192:195], a[132:135], v[220:223]\n"
      "s_add_u32 s20, s20, 8192\n"
      "s_addc_u32 s21, s21, 0\n"
      "global_load_dwordx4 a[192:195], %0, s[20:21] offset:-4096\n"
      "v_mfma_f32_16x16x32_bf16 v[224:227], a[196:199], v[160:163], v[224:227]\n"
      "v_mfma_f32_16x16x32_bf16 v[228:231], a[196:199], v[164:167], v[228:231]\n"
      "v_mfma_f32_16x16x32_bf16 v[232:235], a[196:199], a[128:131], v[232:235]\n"
      "v_mfma_f32_16x16x32_bf16 v[236:239], a[196:199], a[132:135], v[236:239]\n"
      "global_load_dwordx4 a[196:199], %0, s[20:21] offset:-3072\n"
      "s_waitcnt vmcnt(26)\n"
      "v_mfma_f32_16x16x32_bf16 v[208:211], a[200:203], v[168:171], v[208:211]\n"
      "v_mfma_f32_16x16x32_bf16 v[212:215], a[200:203], v[172:175], v[212:215]\n"
      "v_mfma_f32_16x16x32_bf16 v[216:219], a[200:203], a[136:139], v[216:219]\n"
      "v_mfma_f32_16x16x32_bf16 v[220:223], a[200:203], a[140:143], v[220:223]\n"
      "global_load_dwordx4 a[200:203], %0, s[20:21] offset:-2048\n"
      "v_mfma_f32_16x16x32_bf16 v[224:227], a[204:207], v[168:171], v[224:227]\n"
      "v_mfma_f32_16x16x32_bf16 v[228:231], a[204:207], v[172:175], v[228:231]\n"
      "v_mfma_f32_16x16x32_bf16 v[232:235], a[204:207], a[136:139], v[232:235]\n"
      "v_mfma_f32_16x16x32_bf16 v[236:239], a[204:207], a[140:143], v[236:239]\n"
      "global_load_dwordx4 a[204:207], %0, s[20:21] offset:-1024\n"
      "s_nop 1\n"
      "s_waitcnt vmcnt(26)\n"
      "v_mfma_f32_16x16x32_bf16 v[176:179], a[208:211], v[32:35], v[176:179]\n"
      "v_mfma_f32_16x16x32_bf16 v[180:183], a[208:211], v[36:39], v[180:183]\n"
      "v_mfma_f32_16x16x32_bf16 v[184:187], a[208:211], a[0:3], v[184:187]\n"
      "v_mfma_f32_16x16x32_bf16 v[188:191], a[208:211], a[4:7], v[188:191]\n"
      "global_load_dwordx4 a[208:211], %0, s[20:21] offset:0\n"
      "v_mfma_f32_16x16x32_bf16 v[192:195], a[212:215], v[32:35], v[192:195]\n"
      "v_mfma_f32_16x16x32_bf16 v[196:199], a[212:215], v[36:39], v[196:199]\n"
      "v_mfma_f32_16x16x32_bf16 v[200:203], a[212:215], a[0:3], v[200:203]\n"
      "v_mfma_f32_16x16x32_bf16 v[204:207], a[212:215], a[4:7], v[204:207]\n"
      "global_load_dwordx4 a[212:215], %0, s[20:21] offset:1024\n"
      "s_waitcnt vmcnt(26)\n"
      "v_mfma_f32_16x16x32_bf16 v[176:179], a[216:219], v[40:43], v[176:179]\n"
      "v_cvt_pk_bf16_f32 v240, v208, v209\n"
      "v_mfma_f32_16x16x32_bf16 v[180:183], a[216:219], v[44:47], v[180:183]\n"
      "v_cvt_pk_bf16_f32 v241, v210, v211\n"
      "v_mfma_f32_16x16x32_bf16 v[184:187], a[216:219], a[8:11], v[184:187]\n"
      "v_cvt_pk_bf16_f32 v242, v224, v225\n"
      "v_mfma_f32_16x16x32_bf16 v[188:191], a[216:219], a[12:15], v[188:191]\n"
      "global_load_dwordx4 a[216:219], %0, s[20:21] offset:2048\n"
      "v_cvt_pk_bf16_f32 v243, v226, v227\n"
      "v_mfma_f32_16x16x32_bf16 v[192:195], a[220:223], v[40:43], v[192:195]\n"
      "v_pk_max_i16 v240, v240, 0\n"
      "v_mfma_f32_16x16x32_bf16 v[196:199], a[220:223], v[44:47], v[196:199]\n"
      "v_pk_max_i16 v241, v241, 0\n"
      "v_mfma_f32_16x16x32_bf16 v[200:203], a[220:223], a[8:11], v[200:203]\n"
      "v_pk_max_i16 v242, v242, 0\n"
      "v_mfma_f32_16x16x32_bf16 v[204:207], a[220:223], a[12:15], v[204:207]\n"
      "global_load_dwordx4 a[220:223], %0, s[20:21] offset:3072\n"
      "v_pk_max_i16 v243, v243, 0\n"
      "s_waitcnt vmcnt(26)\n"
      "v_mfma_f32_16x16x32_bf16 v[176:179], a[224:227], v[48:51], v[176:179]\n"
      "ds_write_b128 %3, v[240:243] offset:4096\n"
      "v_mfma_f32_16x16x32_bf16 v[180:183], a[224:227], v[52:55], v[180:183]\n"
      "v_cvt_pk_bf16_f32 v244, v212, v213\n"
      "v_mfma_f32_16x16x32_bf16 v[184:187], a[224:227], a[16:19], v[184:187]\n"
      "v_cvt_pk_bf16_f32 v245, v214, v215\n"
      "v_mfma_f32_16x16x32_bf16 v[188:191], a[224:227], a[20:23], v[188:191]\n"
      "s_add_u32 s20, s20, 8192\n"
      "s_addc_u32 s21, s21, 0\n"
      "global_load_dwordx4 a[224:227], %0, s[20:21] offset:-4096\n"
      "v_cvt_pk_bf16_f32 v246, v228, v229\n"
      "v_mfma_f32_16x16x32_bf16 v[192:195], a[228:231], v[48:51], v[192:195]\n"
      "v_cvt_pk_bf16_f32 v247, v230, v231\n"
      "v_mfma_f32_16x16x32_bf16 v[196:199], a[228:231], v[52:55], v[196:199]\n"
      "v_pk_max_i16 v244, v244, 0\n"
      "v_mfma_f32_16x16x32_bf16 v[200:203], a[228:231], a[16:19], v[200:203]\n"
      "v_pk_max_i16 v245, v245, 0\n"
      "v_mfma_f32_16x16x32_bf16 v[204:207], a[228:231], a[20:23], v[204:207]\n"
      "global_load_dwordx4 a[228:231], %0, s[20:21] offset:-3072\n"
      "v_pk_max_i16 v246, v246, 0\n"
      "s_waitcnt vmcnt(26)\n"
      "v_mfma_f32_16x16x32_bf16 v[176:179], a[232:235], v[56:59], v[176:179]\n"
      "v_pk_max_i16 v247, v247, 0\n"
      "v_mfma_f32_16x16x32_bf16 v[180:183], a[232:235], v[60:63], v[180:183]\n"
      "ds_write_b128 %3, v[244:247] offset:5120\n"
      "v_mfma_f32_16x16x32_bf16 v[184:187], a[232:235], a[24:27], v[184:187]\n"
      "v_cvt_pk_bf16_f32 v248, v216, v217\n"
      "v_mfma_f32_16x16x32_bf16 v[188:191], a[232:235], a[28:31], v[188:191]\n"
      "global_load_dwordx4 a[232:235], %0, s[20:21] offset:-2048\n"
      "v_cvt_pk_bf16_f32 v249, v218, v219\n"
      "v_mfma_f32_16x16x32_bf16 v[192:195], a[236:239], v[56:59], v[192:195]\n"
      "v_cvt_pk_bf16_f32 v250, v232, v233\n"
      "v_mfma_f32_16x16x32_bf16 v[196:199], a[236:239], v[60:63], v[196:199]\n"
      "v_cvt_pk_bf16_f32 v251, v234, v235\n"
      "v_mfma_f32_16x16x32_bf16 v[200:203], a[236:239], a[24:27], v[200:203]\n"
      "v_pk_max_i16 v248, v248, 0\n"
      "v_mfma_f32_16x16x32_bf16 v[204:207], a[236:239], a[28:31], v[204:207]\n"
      "global_load_dwordx4 a[236:239], %0, s[20:21] offset:-1024\n"
      "v_pk_max_i16 v249, v249, 0\n"
      "s_waitcnt vmcnt(26)\n"
      "v_mfma_f32_16x16x32_bf16 v[176:179], a[240:243], v[64:67], v[176:179]\n"
      "v_pk_max_i16 v250, v250, 0\n"
      "v_mfma_f32_16x16x32_bf16 v[180:183], a[240:243], v[68:71], v[180:183]\n"
      "v_pk_max_i16 v251, v251, 0\n"
      "v_mfma_f32_16x16x32_bf16 v[184:187], a[240:243], a[32:35], v[184:187]\n"
      "ds_write_b128 %3, v[248:251] offset:6144\n"
      "v_mfma_f32_16x16x32_bf16 v[188:191], a[240:243], a[36:39], v[188:191]\n"
      "global_load_dwordx4 a[240:243], %0, s[20:21] offset:0\n"
      "v_cvt_pk_bf16_f32 v252, v220, v221\n"
      "v_mfma_f32_16x16x32_bf16 v[192:195], a[244:247], v[64:67], v[192:195]\n"
      "v_cvt_pk_bf16_f32 v253, v222, v223\n"
      "v_mfma_f32_16x16x32_bf16 v[196:199], a[244:247], v[68:71], v[196:199]\n"
      "v_cvt_pk_bf16_f32 v254, v236, v237\n"
      "v_mfma_f32_16x16x32_bf16 v[200:203], a[244:247], a[32:35], v[200:203]\n"
      "v_cvt_pk_bf16_f32 v255, v238, v239\n"
      "v_mfma_f32_16x16x32_bf16 v[204:207], a[244:247], a[36:39], v[204:207]\n"
      "global_load_dwordx4 a[244:247], %0, s[20:21] offset:1024\n"
      "v_pk_max_i16 v252, v252, 0\n"
      "s_waitcnt vmcnt(26)\n"
      "v_mfma_f32_16x16x32_bf16 v[176:179], a[248:251], v[72:75], v[176:179]\n"
      "v_pk_max_i16 v253, v253, 0\n"
      "v_mfma_f32_16x16x32_bf16 v[180:183], a[248:251], v[76:79], v[180:183]\n"
      "v_pk_max_i16 v254, v254, 0\n"
      "v_mfma_f32_16x16x32_bf16 v[184:187], a[248:251], a[40:43], v[184:187]\n"
      "v_pk_max_i16 v255, v255, 0\n"
      "v_mfma_f32_16x16x32_bf16 v[188:191], a[248:251], a[44:47], v[188:191]\n"
      "global_load_dwordx4 a[248:251], %0, s[20:21] offset:2048\n"
      "ds_write_b128 %3, v[252:255] offset:7168\n"
      "v_mfma_f32_16x16x32_bf16 v[192:195], a[252:255], v[72:75], v[192:195]\n"
      "ds_read_b128 v[240:243], %7 offset:512\n"
      "v_mfma_f32_16x16x32_bf16 v[196:199], a[252:255], v[76:79], v[196:199]\n"
      "ds_read_b128 v[244:247], %7 offset:528\n"
      "v_mfma_f32_16x16x32_bf16 v[200:203], a[252:255], a[40:43], v[200:203]\n"
      "v_mfma_f32_16x16x32_bf16 v[204:207], a[252:255], a[44:47], v[204:207]\n"
      "global_load_dwordx4 a[252:255], %0, s[20:21] offset:3072\n"
      "s_waitcnt vmcnt(26)\n"
      "v_mfma_f32_16x16x32_bf16 v[176:179], a[144:147], v[80:83], v[176:179]\n"
      "v_mfma_f32_16x16x32_bf16 v[180:183], a[144:147], v[84:87], v[180:183]\n"
      "v_mfma_f32_16x16x32_bf16 v[184:187], a[144:147], a[48:51], v[184:187]\n"
      "s_waitcnt lgkmcnt(0)\n"
      "v_mfma_f32_16x16x32_bf16 v[188:191], a[144:147], a[52:55], v[188:191]\n"
      "s_add_u32 s20, s20, 8192\n"
      "s_addc_u32 s21, s21, 0\n"
      "global_load_dwordx4 a[144:147], %0, s[20:21] offset:-4096\n"
      "v_mul_f32 v208, v24, v240\n"
      "v_mfma_f32_16x16x32_bf16 v[192:195], a[148:151], v[80:83], v[192:195]\n"
      "v_mul_f32 v209, v24, v241\n"
      "v_mfma_f32_16x16x32_bf16 v[196:199], a[148:151], v[84:87], v[196:199]\n"
      "v_mul_f32 v210, v24, v242\n"
      "v_mfma_f32_16x16x32_bf16 v[200:203], a[148:151], a[48:51], v[200:203]\n"
      "v_mul_f32 v211, v24, v243\n"
      "v_mfma_f32_16x16x32_bf16 v[204:207], a[148:151], a[52:55], v[204:207]\n"
      "global_load_dwordx4 a[148:151], %0, s[20:21] offset:-3072\n"
      "v_mul_f32 v212, v26, v240\n"
      "s_waitcnt vmcnt(26)\n"
      "v_mfma_f32_16x16x32_bf16 v[176:179], a[152:155], v[88:91], v[176:179]\n"
      "v_mul_f32 v213, v26, v241\n"
      "v_mfma_f32_16x16x32_bf16 v[180:183], a[152:155], v[92:95], v[180:183]\n"
      "v_mul_f32 v214, v26, v242\n"
      "v_mfma_f32_16x16x32_bf16 v[184:187], a[152:155], a[56:59], v[184:187]\n"
      "v_mul_f32 v215, v26, v243\n"
      "v_mfma_f32_16x16x32_bf16 v[188:191], a[152:155], a[60:63], v[188:191]\n"
      "global_load_dwordx4 a[152:155], %0, s[20:21] offset:-2048\n"
      "v_mul_f32 v216, v28, v240\n"
      "v_mfma_f32_16x16x32_bf16 v[192:195], a[156:159], v[88:91], v[192:195]\n"
      "v_mul_f32 v217, v28, v241\n"
      "v_mfma_f32_16x16x32_bf16 v[196:199], a[156:159], v[92:95], v[196:199]\n"
      "v_mul_f32 v218, v28, v242\n"
      "v_mfma_f32_16x16x32_bf16 v[200:203], a[156:159], a[56:59], v[200:203]\n"
      "v_mul_f32 v219, v28, v243\n"
      "v_mfma_f32_16x16x32_bf16 v[204:207], a[156:159], a[60:63], v[204:207]\n"
      "global_load_dwordx4 a[156:159], %0, s[20:21] offset:-1024\n"
      "v_mul_f32 v220, v30, v240\n"
      "s_waitcnt vmcnt(26)\n"
      "v_mfma_f32_16x16x32_bf16 v[176:179], a[160:163], v[96:99], v[176:179]\n"
      "v_mul_f32 v221, v30, v241\n"
      "v_mfma_f32_16x16x32_bf16 v[180:183], a[160:163], v[100:103], v[180:183]\n"
      "v_mul_f32 v222, v30, v242\n"
      "v_mfma_f32_16x16x32_bf16 v[184:187], a[160:163], a[64:67], v[184:187]\n"
      "v_mul_f32 v223, v30, v243\n"
      "v_mfma_f32_16x16x32_bf16 v[188:191], a[160:163], a[68:71], v[188:191]\n"
      "global_load_dwordx4 a[160:163], %0, s[20:21] offset:0\n"
      "v_mul_f32 v224, v24, v244\n"
      "v_mfma_f32_16x16x32_bf16 v[192:195], a[164:167], v[96:99], v[192:195]\n"
      "v_mul_f32 v225, v24, v245\n"
      "v_mfma_f32_16x16x32_bf16 v[196:199], a[164:167], v[100:103], v[196:199]\n"
      "v_mul_f32 v226, v24, v246\n"
      "v_mfma_f32_16x16x32_bf16 v[200:203], a[164:167], a[64:67], v[200:203]\n"
      "v_mul_f32 v227, v24, v247\n"
      "v_mfma_f32_16x16x32_bf16 v[204:207], a[164:167], a[68:71], v[204:207]\n"
      "global_load_dwordx4 a[164:167], %0, s[20:21] offset:1024\n"
      "v_mul_f32 v228, v26, v244\n"
      "s_waitcnt vmcnt(26)\n"
      "v_mfma_f32_16x16x32_bf16 v[176:179], a[168:171], v[104:107], v[176:179]\n"
      "v_mul_f32 v229, v26, v245\n"
      "v_mfma_f32_16x16x32_bf16 v[180:183], a[168:171], v[108:111], v[180:183]\n"
      "v_mul_f32 v230, v26, v246\n"
      "v_mfma_f32_16x16x32_bf16 v[184:187], a[168:171], a[72:75], v[184:187]\n"
      "v_mul_f32 v231, v26, v247\n"
      "v_mfma_f32_16x16x32_bf16 v[188:191], a[168:171], a[76:79], v[188:191]\n"
      "global_load_dwordx4 a[168:171], %0, s[20:21] offset:2048\n"
      "v_mul_f32 v232, v28, v244\n"
      "v_mfma_f32_16x16x32_bf16 v[192:195], a[172:175], v[104:107], v[192:195]\n"
      "v_mul_f32 v233, v28, v245\n"
      "v_mfma_f32_16x16x32_bf16 v[196:199], a[172:175], v[108:111], v[196:199]\n"
      "v_mul_f32 v234, v28, v246\n"
      "v_mfma_f32_16x16x32_bf16 v[200:203], a[172:175], a[72:75], v[200:203]\n"
      "v_mul_f32 v235, v28, v247\n"
      "v_mfma_f32_16x16x32_bf16 v[204:207], a[172:175], a[76:79], v[204:207]\n"
      "global_load_dwordx4 a[172:175], %0, s[20:21] offset:3072\n"
      "v_mul_f32 v236, v30, v244\n"
      "s_waitcnt vmcnt(26)\n"
      "v_mfma_f32_16x16x32_bf16 v[176:179], a[176:179], v[112:115], v[176:179]\n"
      "v_mul_f32 v237, v30, v245\n"
      "v_mfma_f32_16x16x32_bf16 v[180:183], a[176:179], v[116:119], v[180:183]\n"
      "v_mul_f32 v238, v30, v246\n"
      "v_mfma_f32_16x16x32_bf16 v[184:187], a[176:179], a[80:83], v[184:187]\n"
      "v_mul_f32 v239, v30, v247\n"
      "v_mfma_f32_16x16x32_bf16 v[188:191], a[176:179], a[84:87], v[188:191]\n"
      "s_add_u32 s20, s20, 8192\n"
      "s_addc_u32 s21, s21, 0\n"
      "global_load_dwordx4 a[176:179], %0, s[20:21] offset:-4096\n"
      "v_mfma_f32_16x16x32_bf16 v[192:195], a[180:183], v[112:115], v[192:195]\n"
      "v_mfma_f32_16x16x32_bf16 v[196:199], a[180:183], v[116:119], v[196:199]\n"
      "v_mfma_f32_16x16x32_bf16 v[200:203], a[180:183], a[80:83], v[200:203]\n"
      "v_mfma_f32_16x16x32_bf16 v[204:207], a[180:183], a[84:87], v[204:207]\n"
      "global_load_dwordx4 a[180:183], %0, s[20:21] offset:-3072\n"
      "s_waitcnt vmcnt(26)\n"
      "v_mfma_f32_16x16x32_bf16 v[176:179], a[184:187], v[120:123], v[176:179]\n"
      "v_mfma_f32_16x16x32_bf16 v[180:183], a[184:187], v[124:127], v[180:183]\n"
      "v_mfma_f32_16x16x32_bf16 v[184:187], a[184:187], a[88:91], v[184:187]\n"
      "v_mfma_f32_16x16x32_bf16 v[188:191], a[184:187], a[92:95], v[188:191]\n"
      "global_load_dwordx4 a[184:187], %0, s[20:21] offset:-2048\n"
      "v_mfma_f32_16x16x32_bf16 v[192:195], a[188:191], v[120:123], v[192:195]\n"
      "v_mfma_f32_16x16x32_bf16 v[196:199], a[188:191], v[124:127], v[196:199]\n"
      "v_mfma_f32_16x16x32_bf16 v[200:203], a[188:191], a[88:91], v[200:203]\n"
      "v_mfma_f32_16x16x32_bf16 v[204:207], a[188:191], a[92:95], v[204:207]\n"
      "global_load_dwordx4 a[188:191], %0, s[20:21] offset:-1024\n"
      "s_waitcnt vmcnt(26)\n"
      "v_mfma_f32_16x16x32_bf16 v[176:179], a[192:195], v[128:131], v[176:179]\n"
      "v_mfma_f32_16x16x32_bf16 v[180:183], a[192:195], v[132:135], v[180:183]\n"
      "v_mfma_f32_16x16x32_bf16 v[184:187], a[192:195], a[96:99], v[184:187]\n"
      "v_mfma_f32_16x16x32_bf16 v[188:191], a[192:195], a[100:103], v[188:191]\n"
      "global_load_dwordx4 a[192:195], %0, s[20:21] offset:0\n"
      "v_mfma_f32_16x16x32_bf16 v[192:195], a[196:199], v[128:131], v[192:195]\n"
      "v_mfma_f32_16x16x32_bf16 v[196:199], a[196:199], v[132:135], v[196:199]\n"
      "v_mfma_f32_16x16x32_bf16 v[200:203], a[196:199], a[96:99], v[200:203]\n"
      "v_mfma_f32_16x16x32_bf16 v[204:207], a[196:199], a[100:103], v[204:207]\n"
      "global_load_dwordx4 a[196:199], %0, s[20:21] offset:1024\n"
      "s_waitcnt vmcnt(26)\n"
      "v_mfma_f32_16x16x32_bf16 v[176:179], a[200:203], v[136:139], v[176:179]\n"
      "v_mfma_f32_16x16x32_bf16 v[180:183], a[200:203], v[140:143], v[180:183]\n"
      "v_mfma_f32_16x16x32_bf16 v[184:187], a[200:203], a[104:107], v[184:187]\n"
      "v_mfma_f32_16x16x32_bf16 v[188:191], a[200:203], a[108:111], v[188:191]\n"
      "global_load_dwordx4 a[200:203], %0, s[20:21] offset:2048\n"
      "v_mfma_f32_16x16x32_bf16 v[192:195], a[204:207], v[136:139], v[192:195]\n"
      "v_mfma_f32_16x16x32_bf16 v[196:199], a[204:207], v[140:143], v[196:199]\n"
      "v_mfma_f32_16x16x32_bf16 v[200:203], a[204:207], a[104:107], v[200:203]\n"
      "v_mfma_f32_16x16x32_bf16 v[204:207], a[204:207], a[108:111], v[204:207]\n"
      "global_load_dwordx4 a[204:207], %0, s[20:21] offset:3072\n"
      "s_waitcnt vmcnt(26)\n"
      "v_mfma_f32_16x16x32_bf16 v[176:179], a[208:211], v[144:147], v[176:179]\n"
      "v_mfma_f32_16x16x32_bf16 v[180:183], a[208:211], v[148:151], v[180:183]\n"
      "v_mfma_f32_16x16x32_bf16 v[184:187], a[208:211], a[112:115], v[184:187]\n"
      "v_mfma_f32_16x16x32_bf16 v[188:191], a[208:211], a[116:119], v[188:191]\n"
      "s_add_u32 s20, s20, 8192\n"
      "s_addc_u32 s21, s21, 0\n"
      "global_load_dwordx4 a[208:211], %0, s[20:21] offset:-4096\n"
      "v_mfma_f32_16x16x32_bf16 v[192:195], a[212:215], v[144:147], v[192:195]\n"
      "v_mfma_f32_16x16x32_bf16 v[196:199], a[212:215], v[148:151], v[196:199]\n"
      "v_mfma_f32_16x16x32_bf16 v[200:203], a[212:215], a[112:115], v[200:203]\n"
      "v_mfma_f32_16x16x32_bf16 v[204:207], a[212:215], a[116:119], v[204:207]\n"
      "global_load_dwordx4 a[212:215], %0, s[20:21] offset:-3072\n"
      "s_waitcnt vmcnt(26)\n"
      "v_mfma_f32_16x16x32_bf16 v[176:179], a[216:219], v[152:155], v[176:179]\n"
      "v_mfma_f32_16x16x32_bf16 v[180:183], a[216:219], v[156:159], v[180:183]\n"
      "v_mfma_f32_16x16x32_bf16 v[184:187], a[216:219], a[120:123], v[184:187]\n"
      "v_mfma_f32_16x16x32_bf16 v[188:191], a[216:219], a[124:127], v[188:191]\n"
      "global_load_dwordx4 a[216:219], %0, s[20:21] offset:-2048\n"
      "v_mfma_f32_16x16x32_bf16 v[192:195], a[220:223], v[152:155], v[192:195]\n"
      "v_mfma_f32_16x16x32_bf16 v[196:199], a[220:223], v[156:159], v[196:199]\n"
      "v_mfma_f32_16x16x32_bf16 v[200:203], a[220:223], a[120:123], v[200:203]\n"
      "v_mfma_f32_16x16x32_bf16 v[204:207], a[220:223], a[124:127], v[204:207]\n"
      "global_load_dwordx4 a[220:223], %0, s[20:21] offset:-1024\n"
      "s_waitcnt vmcnt(26)\n"
      "v_mfma_f32_16x16x32_bf16 v[176:179], a[224:227], v[160:163], v[176:179]\n"
      "v_mfma_f32_16x16x32_bf16 v[180:183], a[224:227], v[164:167], v[180:183]\n"
      "v_mfma_f32_16x16x32_bf16 v[184:187], a[224:227], a[128:131], v[184:187]\n"
      "v_mfma_f32_16x16x32_bf16 v[188:191], a[224:227], a[132:135], v[188:191]\n"
      "global_load_dwordx4 a[224:227], %0, s[20:21] offset:0\n"
      "v_mfma_f32_16x16x32_bf16 v[192:195], a[228:231], v[160:163], v[192:195]\n"
      "v_mfma_f32_16x16x32_bf16 v[196:199], a[228:231], v[164:167], v[196:199]\n"
      "v_mfma_f32_16x16x32_bf16 v[200:203], a[228:231], a[128:131], v[200:203]\n"
      "v_mfma_f32_16x16x32_bf16 v[204:207], a[228:231], a[132:135], v[204:207]\n"
      "global_load_dwordx4 a[228:231], %0, s[20:21] offset:1024\n"
      "s_waitcnt vmcnt(26)\n"
      "v_mfma_f32_16x16x32_bf16 v[176:179], a[232:235], v[168:171], v[176:179]\n"
      "v_mfma_f32_16x16x32_bf16 v[180:183], a[232:235], v[172:175], v[180:183]\n"
      "v_mfma_f32_16x16x32_bf16 v[184:187], a[232:235], a[136:139], v[184:187]\n"
      "v_mfma_f32_16x16x32_bf16 v[188:191], a[232:235], a[140:143], v[188:191]\n"
      "global_load_dwordx4 a[232:235], %0, s[20:21] offset:2048\n"
      "v_mfma_f32_16x16x32_bf16 v[192:195], a[236:239], v[168:171], v[192:195]\n"
      "v_mfma_f32_16x16x32_bf16 v[196:199], a[236:239], v[172:175], v[196:199]\n"
      "v_mfma_f32_16x16x32_bf16 v[200:203], a[236:239], a[136:139], v[200:203]\n"
      "v_mfma_f32_16x16x32_bf16 v[204:207], a[236:239], a[140:143], v[204:207]\n"
      "global_load_dwordx4 a[236:239], %0, s[20:21] offset:3072\n"
      "s_nop 1\n"
      "s_waitcnt vmcnt(26)\n"
      "v_mfma_f32_16x16x32_bf16 v[208:211], a[240:243], v[32:35], v[208:211]\n"
      "v_mfma_f32_16x16x32_bf16 v[212:215], a[240:243], v[36:39], v[212:215]\n"
      "v_mfma_f32_16x16x32_bf16 v[216:219], a[240:243], a[0:3], v[216:219]\n"
      "v_mfma_f32_16x16x32_bf16 v[220:223], a[240:243], a[4:7], v[220:223]\n"
      "s_add_u32 s20, s20, 8192\n"
      "s_addc_u32 s21, s21, 0\n"
      "global_load_dwordx4 a[240:243], %0, s[20:21] offset:-4096\n"
      "v_mfma_f32_16x16x32_bf16 v[224:227], a[244:247], v[32:35], v[224:227]\n"
      "v_mfma_f32_16x16x32_bf16 v[228:231], a[244:247], v[36:39], v[228:231]\n"
      "v_mfma_f32_16x16x32_bf16 v[232:235], a[244:247], a[0:3], v[232:235]\n"
      "v_mfma_f32_16x16x32_bf16 v[236:239], a[244:247], a[4:7], v[236:239]\n"
      "global_load_dwordx4 a[244:247], %0, s[20:21] offset:-3072\n"
      "s_waitcnt vmcnt(26)\n"
      "v_mfma_f32_16x16x32_bf16 v[208:211], a[248:251], v[40:43], v[208:211]\n"
      "v_cvt_pk_bf16_f32 v240, v176, v177\n"
      "v_mfma_f32_16x16x32_bf16 v[212:215], a[248:251], v[44:47], v[212:215]\n"
      "v_cvt_pk_bf16_f32 v241, v178, v179\n"
      "v_mfma_f32_16x16x32_bf16 v[216:219], a[248:251], a[8:11], v[216:219]\n"
      "v_cvt_pk_bf16_f32 v242, v192, v193\n"
      "v_mfma_f32_16x16x32_bf16 v[220:223], a[248:251], a[12:15], v[220:223]\n"
      "global_load_dwordx4 a[248:251], %0, s[20:21] offset:-2048\n"
      "v_cvt_pk_bf16_f32 v243, v194, v195\n"
      "v_mfma_f32_16x16x32_bf16 v[224:227], a[252:255], v[40:43], v[224:227]\n"
      "v_pk_max_i16 v240, v240, 0\n"
      "v_mfma_f32_16x16x32_bf16 v[228:231], a[252:255], v[44:47], v[228:231]\n"
      "v_pk_max_i16 v241, v241, 0\n"
      "v_mfma_f32_16x16x32_bf16 v[232:235], a[252:255], a[8:11], v[232:235]\n"
      "v_pk_max_i16 v242, v242, 0\n"
      "v_mfma_f32_16x16x32_bf16 v[236:239], a[252:255], a[12:15], v[236:239]\n"
      "global_load_dwordx4 a[252:255], %0, s[20:21] offset:-1024\n"
      "v_pk_max_i16 v243, v243, 0\n"
      "s_waitcnt vmcnt(26)\n"
      "v_mfma_f32_16x16x32_bf16 v[208:211], a[144:147], v[48:51], v[208:211]\n"
      "ds_write_b128 %3, v[240:243] offset:8192\n"
      "v_mfma_f32_16x16x32_bf16 v[212:215], a[144:147], v[52:55], v[212:215]\n"
      "v_cvt_pk_bf16_f32 v244, v180, v181\n"
      "v_mfma_f32_16x16x32_bf16 v[216:219], a[144:147], a[16:19], v[216:219]\n"
      "v_cvt_pk_bf16_f32 v245, v182, v183\n"
      "v_mfma_f32_16x16x32_bf16 v[220:223], a[144:147], a[20:23], v[220:223]\n"
      "global_load_dwordx4 a[144:147], %0, s[20:21] offset:0\n"
      "v_cvt_pk_bf16_f32 v246, v196, v197\n"
      "v_mfma_f32_16x16x32_bf16 v[224:227], a[148:151], v[48:51], v[224:227]\n"
      "v_cvt_pk_bf16_f32 v247, v198, v199\n"
      "v_mfma_f32_16x16x32_bf16 v[228:231], a[148:151], v[52:55], v[228:231]\n"
      "v_pk_max_i16 v244, v244, 0\n"
      "v_mfma_f32_16x16x32_bf16 v[232:235], a[148:151], a[16:19], v[232:235]\n"
      "v_pk_max_i16 v245, v245, 0\n"
      "v_mfma_f32_16x16x32_bf16 v[236:239], a[148:151], a[20:23], v[236:239]\n"
      "global_load_dwordx4 a[148:151], %0, s[20:21] offset:1024\n"
      "v_pk_max_i16 v246, v246, 0\n"
      "s_waitcnt vmcnt(26)\n"
      "v_mfma_f32_16x16x32_bf16 v[208:211], a[152:155], v[56:59], v[208:211]\n"
      "v_pk_max_i16 v247, v247, 0\n"
      "v_mfma_f32_16x16x32_bf16 v[212:215], a[152:155], v[60:63], v[212:215]\n"
      "ds_write_b128 %3, v[244:247] offset:9216\n"
      "v_mfma_f32_16x16x32_bf16 v[216:219], a[152:155], a[24:27], v[216:219]\n"
      "v_cvt_pk_bf16_f32 v248, v184, v185\n"
      "v_mfma_f32_16x16x32_bf16 v[220:223], a[152:155], a[28:31], v[220:223]\n"
      "global_load_dwordx4 a[152:155], %0, s[20:21] offset:2048\n"
      "v_cvt_pk_bf16_f32 v249, v186, v187\n"
      "v_mfma_f32_16x16x32_bf16 v[224:227], a[156:159], v[56:59], v[224:227]\n"
      "v_cvt_pk_bf16_f32 v250, v200, v201\n"
      "v_mfma_f32_16x16x32_bf16 v[228:231], a[156:159], v[60:63], v[228:231]\n"
      "v_cvt_pk_bf16_f32 v251, v202, v203\n"
      "v_mfma_f32_16x16x32_bf16 v[232:235], a[156:159], a[24:27], v[232:235]\n"
      "v_pk_max_i16 v248, v248, 0\n"
      "v_mfma_f32_16x16x32_bf16 v[236:239], a[156:159], a[28:31], v[236:239]\n"
      "global_load_dwordx4 a[156:159], %0, s[20:21] offset:3072\n"
      "v_pk_max_i16 v249, v249, 0\n"
      "s_waitcnt vmcnt(26)\n"
      "v_mfma_f32_16x16x32_bf16 v[208:211], a[160:163], v[64:67], v[208:211]\n"
      "v_pk_max_i16 v250, v250, 0\n"
      "v_mfma_f32_16x16x32_bf16 v[212:215], a[160:163], v[68:71], v[212:215]\n"
      "v_pk_max_i16 v251, v251, 0\n"
      "v_mfma_f32_16x16x32_bf16 v[216:219], a[160:163], a[32:35], v[216:219]\n"
      "ds_write_b128 %3, v[248:251] offset:10240\n"
      "v_mfma_f32_16x16x32_bf16 v[220:223], a[160:163], a[36:39], v[220:223]\n"
      "s_add_u32 s20, s20, 8192\n"
      "s_addc_u32 s21, s21, 0\n"
      "global_load_dwordx4 a[160:163], %0, s[20:21] offset:-4096\n"
      "v_cvt_pk_bf16_f32 v252, v188, v189\n"
      "v_mfma_f32_16x16x32_bf16 v[224:227], a[164:167], v[64:67], v[224:227]\n"
      "v_cvt_pk_bf16_f32 v253, v190, v191\n"
      "v_mfma_f32_16x16x32_bf16 v[228:231], a[164:167], v[68:71], v[228:231]\n"
      "v_cvt_pk_bf16_f32 v254, v204, v205\n"
      "v_mfma_f32_16x16x32_bf16 v[232:235], a[164:167], a[32:35], v[232:235]\n"
      "v_cvt_pk_bf16_f32 v255, v206, v207\n"
      "v_mfma_f32_16x16x32_bf16 v[236:239], a[164:167], a[36:39], v[236:239]\n"
      "global_load_dwordx4 a[164:167], %0, s[20:21] offset:-3072\n"
      "v_pk_max_i16 v252, v252, 0\n"
      "s_waitcnt vmcnt(26)\n"
      "v_mfma_f32_16x16x32_bf16 v[208:211], a[168:171], v[72:75], v[208:211]\n"
      "v_pk_max_i16 v253, v253, 0\n"
      "v_mfma_f32_16x16x32_bf16 v[212:215], a[168:171], v[76:79], v[212:215]\n"
      "v_pk_max_i16 v254, v254, 0\n"
      "v_mfma_f32_16x16x32_bf16 v[216:219], a[168:171], a[40:43], v[216:219]\n"
      "v_pk_max_i16 v255, v255, 0\n"
      "v_mfma_f32_16x16x32_bf16 v[220:223], a[168:171], a[44:47], v[220:223]\n"
      "global_load_dwordx4 a[168:171], %0, s[20:21] offset:-2048\n"
      "ds_write_b128 %3, v[252:255] offset:11264\n"
      "v_mfma_f32_16x16x32_bf16 v[224:227], a[172:175], v[72:75], v[224:227]\n"
      "ds_read_b128 v[240:243], %7 offset:0\n"
      "v_mfma_f32_16x16x32_bf16 v[228:231], a[172:175], v[76:79], v[228:231]\n"
      "v_mfma_f32_16x16x32_bf16 v[232:235], a[172:175], a[40:43], v[232:235]\n"
      "v_mfma_f32_16x16x32_bf16 v[236:239], a[172:175], a[44:47], v[236:239]\n"
      "global_load_dwordx4 a[172:175], %0, s[20:21] offset:-1024\n"
      "s_waitcnt vmcnt(26)\n"
      "v_mfma_f32_16x16x32_bf16 v[208:211], a[176:179], v[80:83], v[208:211]\n"
      "v_mfma_f32_16x16x32_bf16 v[212:215], a[176:179], v[84:87], v[212:215]\n"
      "s_waitcnt lgkmcnt(0)\n"
      "v_mfma_f32_16x16x32_bf16 v[216:219], a[176:179], a[48:51], v[216:219]\n"
      "v_mul_f32 v176, v24, v240\n"
      "v_mfma_f32_16x16x32_bf16 v[220:223], a[176:179], a[52:55], v[220:223]\n"
      "global_load_dwordx4 a[176:179], %0, s[20:21] offset:0\n"
      "v_mul_f32 v177, v24, v241\n"
      "v_mfma_f32_16x16x32_bf16 v[224:227], a[180:183], v[80:83], v[224:227]\n"
      "v_mul_f32 v178, v24, v242\n"
      "v_mfma_f32_16x16x32_bf16 v[228:231], a[180:183], v[84:87], v[228:231]\n"
      "v_mul_f32 v179, v24, v243\n"
      "v_mfma_f32_16x16x32_bf16 v[232:235], a[180:183], a[48:51], v[232:235]\n"
      "v_mul_f32 v180, v26, v240\n"
      "v_mfma_f32_16x16x32_bf16 v[236:239], a[180:183], a[52:55], v[236:239]\n"
      "global_load_dwordx4 a[180:183], %0, s[20:21] offset:1024\n"
      "v_mul_f32 v181, v26, v241\n"
      "s_waitcnt vmcnt(26)\n"
      "v_mfma_f32_16x16x32_bf16 v[208:211], a[184:187], v[88:91], v[208:211]\n"
      "v_mul_f32 v182, v26, v242\n"
      "v_mfma_f32_16x16x32_bf16 v[212:215], a[184:187], v[92:95], v[212:215]\n"
      "v_mul_f32 v183, v26, v243\n"
      "v_mfma_f32_16x16x32_bf16 v[216:219], a[184:187], a[56:59], v[216:219]\n"
      "v_mul_f32 v184, v28, v240\n"
      "v_mfma_f32_16x16x32_bf16 v[220:223], a[184:187], a[60:63], v[220:223]\n"
      "global_load_dwordx4 a[184:187], %0, s[20:21] offset:2048\n"
      "v_mul_f32 v185, v28, v241\n"
      "v_mfma_f32_16x16x32_bf16 v[224:227], a[188:191], v[88:91], v[224:227]\n"
      "v_mul_f32 v186, v28, v242\n"
      "v_mfma_f32_16x16x32_bf16 v[228:231], a[188:191], v[92:95], v[228:231]\n"
      "v_mul_f32 v187, v28, v243\n"
      "v_mfma_f32_16x16x32_bf16 v[232:235], a[188:191], a[56:59], v[232:235]\n"
      "v_mul_f32 v188, v30, v240\n"
      "v_mfma_f32_16x16x32_bf16 v[236:239], a[188:191], a[60:63], v[236:239]\n"
      "global_load_dwordx4 a[188:191], %0, s[20:21] offset:3072\n"
      "v_mul_f32 v189, v30, v241\n"
      "s_waitcnt vmcnt(26)\n"
      "v_mfma_f32_16x16x32_bf16 v[208:211], a[192:195], v[96:99], v[208:211]\n"
      "v_mul_f32 v190, v30, v242\n"
      "v_mfma_f32_16x16x32_bf16 v[212:215], a[192:195], v[100:103], v[212:215]\n"
      "v_mul_f32 v191, v30, v243\n"
      "v_mfma_f32_16x16x32_bf16 v[216:219], a[192:195], a[64:67], v[216:219]\n"
      "v_mfma_f32_16x16x32_bf16 v[220:223], a[192:195], a[68:71], v[220:223]\n"
      "s_add_u32 s20, s20, 8192\n"
      "s_addc_u32 s21, s21, 0\n"
      "global_load_dwordx4 a[192:195], %0, s[20:21] offset:-4096\n"
      "v_mfma_f32_16x16x32_bf16 v[224:227], a[196:199], v[96:99], v[224:227]\n"
      "v_mfma_f32_16x16x32_bf16 v[228:231], a[196:199], v[100:103], v[228:231]\n"
      "v_mfma_f32_16x16x32_bf16 v[232:235], a[196:199], a[64:67], v[232:235]\n"
      "v_mfma_f32_16x16x32_bf16 v[236:239], a[196:199], a[68:71], v[236:239]\n"
      "global_load_dwordx4 a[196:199], %0, s[20:21] offset:-3072\n"
      "s_waitcnt vmcnt(26)\n"
      "v_mfma_f32_16x16x32_bf16 v[208:211], a[200:203], v[104:107], v[208:211]\n"
      "v_mfma_f32_16x16x32_bf16 v[212:215], a[200:203], v[108:111], v[212:215]\n"
      "v_mfma_f32_16x16x32_bf16 v[216:219], a[200:203], a[72:75], v[216:219]\n"
      "v_mfma_f32_16x16x32_bf16 v[220:223], a[200:203], a[76:79], v[220:223]\n"
      "global_load_dwordx4 a[200:203], %0, s[20:21] offset:-2048\n"
      "v_mfma_f32_16x16x32_bf16 v[224:227], a[204:207], v[104:107], v[224:227]\n"
      "v_mfma_f32_16x16x32_bf16 v[228:231], a[204:207], v[108:111], v[228:231]\n"
      "v_mfma_f32_16x16x32_bf16 v[232:235], a[204:207], a[72:75], v[232:235]\n"
      "v_mfma_f32_16x16x32_bf16 v[236:239], a[204:207], a[76:79], v[236:239]\n"
      "global_load_dwordx4 a[204:207], %0, s[20:21] offset:-1024\n"
      "s_waitcnt vmcnt(26)\n"
      "v_mfma_f32_16x16x32_bf16 v[208:211], a[208:211], v[112:115], v[208:211]\n"
      "v_mfma_f32_16x16x32_bf16 v[212:215], a[208:211], v[116:119], v[212:215]\n"
      "v_mfma_f32_16x16x32_bf16 v[216:219], a[208:211], a[80:83], v[216:219]\n"
      "v_mfma_f32_16x16x32_bf16 v[220:223], a[208:211], a[84:87], v[220:223]\n"
      "global_load_dwordx4 a[208:211], %0, s[20:21] offset:0\n"
      "v_mfma_f32_16x16x32_bf16 v[224:227], a[212:215], v[112:115], v[224:227]\n"
      "v_mfma_f32_16x16x32_bf16 v[228:231], a[212:215], v[116:119], v[228:231]\n"
      "v_mfma_f32_16x16x32_bf16 v[232:235], a[212:215], a[80:83], v[232:235]\n"
      "v_mfma_f32_16x16x32_bf16 v[236:239], a[212:215], a[84:87], v[236:239]\n"
      "global_load_dwordx4 a[212:215], %0, s[20:21] offset:1024\n"
      "s_waitcnt vmcnt(26)\n"
      "v_mfma_f32_16x16x32_bf16 v[208:211], a[216:219], v[120:123], v[208:211]\n"
      "v_mfma_f32_16x16x32_bf16 v[212:215], a[216:219], v[124:127], v[212:215]\n"
      "v_mfma_f32_16x16x32_bf16 v[216:219], a[216:219], a[88:91], v[216:219]\n"
      "v_mfma_f32_16x16x32_bf16 v[220:223], a[216:219], a[92:95], v[220:223]\n"
      "global_load_dwordx4 a[216:219], %0, s[20:21] offset:2048\n"
      "v_mfma_f32_16x16x32_bf16 v[224:227], a[220:223], v[120:123], v[224:227]\n"
      "v_mfma_f32_16x16x32_bf16 v[228:231], a[220:223], v[124:127], v[228:231]\n"
      "v_mfma_f32_16x16x32_bf16 v[232:235], a[220:223], a[88:91], v[232:235]\n"
      "v_mfma_f32_16x16x32_bf16 v[236:239], a[220:223], a[92:95], v[236:239]\n"
      "global_load_dwordx4 a[220:223], %0, s[20:21] offset:3072\n"
      "s_waitcnt vmcnt(26)\n"
      "v_mfma_f32_16x16x32_bf16 v[208:211], a[224:227], v[128:131], v[208:211]\n"
      "v_mfma_f32_16x16x32_bf16 v[212:215], a[224:227], v[132:135], v[212:215]\n"
      "v_mfma_f32_16x16x32_bf16 v[216:219], a[224:227], a[96:99], v[216:219]\n"
      "v_mfma_f32_16x16x32_bf16 v[220:223], a[224:227], a[100:103], v[220:223]\n"
      "s_add_u32 s20, s20, 8192\n"
      "s_addc_u32 s21, s21, 0\n"
      "global_load_dwordx4 a[224:227], %0, s[20:21] offset:-4096\n"
      "v_mfma_f32_16x16x32_bf16 v[224:227], a[228:231], v[128:131], v[224:227]\n"
      "v_mfma_f32_16x16x32_bf16 v[228:231], a[228:231], v[132:135], v[228:231]\n"
      "v_mfma_f32_16x16x32_bf16 v[232:235], a[228:231], a[96:99], v[232:235]\n"
      "v_mfma_f32_16x16x32_bf16 v[236:239], a[228:231], a[100:103], v[236:239]\n"
      "global_load_dwordx4 a[228:231], %0, s[20:21] offset:-3072\n"
      "s_waitcnt vmcnt(26)\n"
      "v_mfma_f32_16x16x32_bf16 v[208:211], a[232:235], v[136:139], v[208:211]\n"
      "v_mfma_f32_16x16x32_bf16 v[212:215], a[232:235], v[140:143], v[212:215]\n"
      "v_mfma_f32_16x16x32_bf16 v[216:219], a[232:235], a[104:107], v[216:219]\n"
      "v_mfma_f32_16x16x32_bf16 v[220:223], a[232:235], a[108:111], v[220:223]\n"
      "global_load_dwordx4 a[232:235], %0, s[20:21] offset:-2048\n"
      "v_mfma_f32_16x16x32_bf16 v[224:227], a[236:239], v[136:139], v[224:227]\n"
      "v_mfma_f32_16x16x32_bf16 v[228:231], a[236:239], v[140:143], v[228:231]\n"
      "v_mfma_f32_16x16x32_bf16 v[232:235], a[236:239], a[104:107], v[232:235]\n"
      "v_mfma_f32_16x16x32_bf16 v[236:239], a[236:239], a[108:111], v[236:239]\n"
      "global_load_dwordx4 a[236:239], %0, s[20:21] offset:-1024\n"
      "s_waitcnt vmcnt(26)\n"
      "v_mfma_f32_16x16x32_bf16 v[208:211], a[240:243], v[144:147], v[208:211]\n"
      "v_mfma_f32_16x16x32_bf16 v[212:215], a[240:243], v[148:151], v[212:215]\n"
      "v_mfma_f32_16x16x32_bf16 v[216:219], a[240:243], a[112:115], v[216:219]\n"
      "v_mfma_f32_16x16x32_bf16 v[220:223], a[240:243], a[116:119], v[220:223]\n"
      "global_load_dwordx4 a[240:243], %0, s[20:21] offset:0\n"
      "v_mfma_f32_16x16x32_bf16 v[224:227], a[244:247], v[144:147], v[224:227]\n"
      "v_mfma_f32_16x16x32_bf16 v[228:231], a[244:247], v[148:151], v[228:231]\n"
      "v_mfma_f32_16x16x32_bf16 v[232:235], a[244:247], a[112:115], v[232:235]\n"
      "v_mfma_f32_16x16x32_bf16 v[236:239], a[244:247], a[116:119], v[236:239]\n"
      "global_load_dwordx4 a[244:247], %0, s[20:21] offset:1024\n"
      "s_waitcnt vmcnt(26)\n"
      "v_mfma_f32_16x16x32_bf16 v[208:211], a[248:251], v[152:155], v[208:211]\n"
      "v_mfma_f32_16x16x32_bf16 v[212:215], a[248:251], v[156:159], v[212:215]\n"
      "v_mfma_f32_16x16x32_bf16 v[216:219], a[248:251], a[120:123], v[216:219]\n"
      "v_mfma_f32_16x16x32_bf16 v[220:223], a[248:251], a[124:127], v[220:223]\n"
      "global_load_dwordx4 a[248:251], %0, s[20:21] offset:2048\n"
      "v_mfma_f32_16x16x32_bf16 v[224:227], a[252:255], v[152:155], v[224:227]\n"
      "v_mfma_f32_16x16x32_bf16 v[228:231], a[252:255], v[156:159], v[228:231]\n"
      "v_mfma_f32_16x16x32_bf16 v[232:235], a[252:255], a[120:123], v[232:235]\n"
      "v_mfma_f32_16x16x32_bf16 v[236:239], a[252:255], a[124:127], v[236:239]\n"
      "global_load_dwordx4 a[252:255], %0, s[20:21] offset:3072\n"
      "s_waitcnt vmcnt(26)\n"
      "v_mfma_f32_16x16x32_bf16 v[208:211], a[144:147], v[160:163], v[208:211]\n"
      "v_mfma_f32_16x16x32_bf16 v[212:215], a[144:147], v[164:167], v[212:215]\n"
      "v_mfma_f32_16x16x32_bf16 v[216:219], a[144:147], a[128:131], v[216:219]\n"
      "v_mfma_f32_16x16x32_bf16 v[220:223], a[144:147], a[132:135], v[220:223]\n"
      "s_add_u32 s20, s20, 8192\n"
      "s_addc_u32 s21, s21, 0\n"
      "global_load_dwordx4 a[144:147], %0, s[20:21] offset:-4096\n"
      "v_mfma_f32_16x16x32_bf16 v[224:227], a[148:151], v[160:163], v[224:227]\n"
      "v_mfma_f32_16x16x32_bf16 v[228:231], a[148:151], v[164:167], v[228:231]\n"
      "v_mfma_f32_16x16x32_bf16 v[232:235], a[148:151], a[128:131], v[232:235]\n"
      "v_mfma_f32_16x16x32_bf16 v[236:239], a[148:151], a[132:135], v[236:239]\n"
      "global_load_dwordx4 a[148:151], %0, s[20:21] offset:-3072\n"
      "s_waitcnt vmcnt(26)\n"
      "v_mfma_f32_16x16x32_bf16 v[208:211], a[152:155], v[168:171], v[208:211]\n"
      "v_mfma_f32_16x16x32_bf16 v[212:215], a[152:155], v[172:175], v[212:215]\n"
      "v_mfma_f32_16x16x32_bf16 v[216:219], a[152:155], a[136:139], v[216:219]\n"
      "v_mfma_f32_16x16x32_bf16 v[220:223], a[152:155], a[140:143], v[220:223]\n"
      "global_load_dwordx4 a[152:155], %0, s[20:21] offset:-2048\n"
      "v_mfma_f32_16x16x32_bf16 v[224:227], a[156:159], v[168:171], v[224:227]\n"
      "v_mfma_f32_16x16x32_bf16 v[228:231], a[156:159], v[172:175], v[228:231]\n"
      "v_mfma_f32_16x16x32_bf16 v[232:235], a[156:159], a[136:139], v[232:235]\n"
      "v_mfma_f32_16x16x32_bf16 v[236:239], a[156:159], a[140:143], v[236:239]\n"
      "global_load_dwordx4 a[156:159], %0, s[20:21] offset:-1024\n"
      "s_nop 1\n"
      "s_waitcnt vmcnt(27)\n"
      "v_mfma_f32_16x16x32_bf16 v[176:179], a[160:163], v[32:35], v[176:179]\n"
      "v_mfma_f32_16x16x32_bf16 v[180:183], a[160:163], v[36:39], v[180:183]\n"
      "v_mfma_f32_16x16x32_bf16 v[184:187], a[160:163], a[0:3], v[184:187]\n"
      "v_mfma_f32_16x16x32_bf16 v[188:191], a[160:163], a[4:7], v[188:191]\n"
      "global_load_dwordx4 a[160:163], %0, s[20:21] offset:0\n"
      "s_waitcnt vmcnt(27)\n"
      "v_mfma_f32_16x16x32_bf16 v[176:179], a[164:167], v[40:43], v[176:179]\n"
      "v_mfma_f32_16x16x32_bf16 v[180:183], a[164:167], v[44:47], v[180:183]\n"
      "v_mfma_f32_16x16x32_bf16 v[184:187], a[164:167], a[8:11], v[184:187]\n"
      "v_mfma_f32_16x16x32_bf16 v[188:191], a[164:167], a[12:15], v[188:191]\n"
      "global_load_dwordx4 a[164:167], %0, s[20:21] offset:1024\n"
      "s_waitcnt vmcnt(27)\n"
      "v_mfma_f32_16x16x32_bf16 v[176:179], a[168:171], v[48:51], v[176:179]\n"
      "v_cvt_pk_bf16_f32 v240, v208, v209\n"
      "v_mfma_f32_16x16x32_bf16 v[180:183], a[168:171], v[52:55], v[180:183]\n"
      "v_cvt_pk_bf16_f32 v241, v210, v211\n"
      "v_mfma_f32_16x16x32_bf16 v[184:187], a[168:171], a[16:19], v[184:187]\n"
      "v_cvt_pk_bf16_f32 v242, v224, v225\n"
      "v_mfma_f32_16x16x32_bf16 v[188:191], a[168:171], a[20:23], v[188:191]\n"
      "global_load_dwordx4 a[168:171], %0, s[20:21] offset:2048\n"
      "v_cvt_pk_bf16_f32 v243, v226, v227\n"
      "s_waitcnt vmcnt(27)\n"
      "v_mfma_f32_16x16x32_bf16 v[176:179], a[172:175], v[56:59], v[176:179]\n"
      "v_pk_max_i16 v240, v240, 0\n"
      "v_mfma_f32_16x16x32_bf16 v[180:183], a[172:175], v[60:63], v[180:183]\n"
      "v_pk_max_i16 v241, v241, 0\n"
      "v_mfma_f32_16x16x32_bf16 v[184:187], a[172:175], a[24:27], v[184:187]\n"
      "v_pk_max_i16 v242, v242, 0\n"
      "v_mfma_f32_16x16x32_bf16 v[188:191], a[172:175], a[28:31], v[188:191]\n"
      "global_load_dwordx4 a[172:175], %0, s[20:21] offset:3072\n"
      "v_pk_max_i16 v243, v243, 0\n"
      "s_waitcnt vmcnt(27)\n"
      "v_mfma_f32_16x16x32_bf16 v[176:179], a[176:179], v[64:67], v[176:179]\n"
      "ds_write_b128 %3, v[240:243] offset:12288\n"
      "v_mfma_f32_16x16x32_bf16 v[180:183], a[176:179], v[68:71], v[180:183]\n"
      "v_cvt_pk_bf16_f32 v244, v212, v213\n"
      "v_mfma_f32_16x16x32_bf16 v[184:187], a[176:179], a[32:35], v[184:187]\n"
      "v_cvt_pk_bf16_f32 v245, v214, v215\n"
      "v_mfma_f32_16x16x32_bf16 v[188:191], a[176:179], a[36:39], v[188:191]\n"
      "s_add_u32 s20, s20, 8192\n"
      "s_addc_u32 s21, s21, 0\n"
      "global_load_dwordx4 a[176:179], %0, s[20:21] offset:-4096\n"
      "v_cvt_pk_bf16_f32 v246, v228, v229\n"
      "s_waitcnt vmcnt(27)\n"
      "v_mfma_f32_16x16x32_bf16 v[176:179], a[180:183], v[72:75], v[176:179]\n"
      "v_cvt_pk_bf16_f32 v247, v230, v231\n"
      "v_mfma_f32_16x16x32_bf16 v[180:183], a[180:183], v[76:79], v[180:183]\n"
      "v_pk_max_i16 v244, v244, 0\n"
      "v_mfma_f32_16x16x32_bf16 v[184:187], a[180:183], a[40:43], v[184:187]\n"
      "v_pk_max_i16 v245, v245, 0\n"
      "v_mfma_f32_16x16x32_bf16 v[188:191], a[180:183], a[44:47], v[188:191]\n"
      "global_load_dwordx4 a[180:183], %0, s[20:21] offset:-3072\n"
      "v_pk_max_i16 v246, v246, 0\n"
      "s_waitcnt vmcnt(27)\n"
      "v_mfma_f32_16x16x32_bf16 v[176:179], a[184:187], v[80:83], v[176:179]\n"
      "v_pk_max_i16 v247, v247, 0\n"
      "v_mfma_f32_16x16x32_bf16 v[180:183], a[184:187], v[84:87], v[180:183]\n"
      "ds_write_b128 %3, v[244:247] offset:13312\n"
      "v_mfma_f32_16x16x32_bf16 v[184:187], a[184:187], a[48:51], v[184:187]\n"
      "v_cvt_pk_bf16_f32 v248, v216, v217\n"
      "v_mfma_f32_16x16x32_bf16 v[188:191], a[184:187], a[52:55], v[188:191]\n"
      "global_load_dwordx4 a[184:187], %0, s[20:21] offset:-2048\n"
      "v_cvt_pk_bf16_f32 v249, v218, v219\n"
      "s_waitcnt vmcnt(27)\n"
      "v_mfma_f32_16x16x32_bf16 v[176:179], a[188:191], v[88:91], v[176:179]\n"
      "v_cvt_pk_bf16_f32 v250, v232, v233\n"
      "v_mfma_f32_16x16x32_bf16 v[180:183], a[188:191], v[92:95], v[180:183]\n"
      "v_cvt_pk_bf16_f32 v251, v234, v235\n"
      "v_mfma_f32_16x16x32_bf16 v[184:187], a[188:191], a[56:59], v[184:187]\n"
      "v_pk_max_i16 v248, v248, 0\n"
      "v_mfma_f32_16x16x32_bf16 v[188:191], a[188:191], a[60:63], v[188:191]\n"
      "global_load_dwordx4 a[188:191], %0, s[20:21] offset:-1024\n"
      "v_pk_max_i16 v249, v249, 0\n"
      "s_waitcnt vmcnt(27)\n"
      "v_mfma_f32_16x16x32_bf16 v[176:179], a[192:195], v[96:99], v[176:179]\n"
      "v_pk_max_i16 v250, v250, 0\n"
      "v_mfma_f32_16x16x32_bf16 v[180:183], a[192:195], v[100:103], v[180:183]\n"
      "v_pk_max_i16 v251, v251, 0\n"
      "v_mfma_f32_16x16x32_bf16 v[184:187], a[192:195], a[64:67], v[184:187]\n"
      "ds_write_b128 %3, v[248:251] offset:14336\n"
      "v_mfma_f32_16x16x32_bf16 v[188:191], a[192:195], a[68:71], v[188:191]\n"
      "global_load_dwordx4 a[192:195], %0, s[20:21] offset:0\n"
      "v_cvt_pk_bf16_f32 v252, v220, v221\n"
      "s_waitcnt vmcnt(27)\n"
      "v_mfma_f32_16x16x32_bf16 v[176:179], a[196:199], v[104:107], v[176:179]\n"
      "v_cvt_pk_bf16_f32 v253, v222, v223\n"
      "v_mfma_f32_16x16x32_bf16 v[180:183], a[196:199], v[108:111], v[180:183]\n"
      "v_cvt_pk_bf16_f32 v254, v236, v237\n"
      "v_mfma_f32_16x16x32_bf16 v[184:187], a[196:199], a[72:75], v[184:187]\n"
      "v_cvt_pk_bf16_f32 v255, v238, v239\n"
      "v_mfma_f32_16x16x32_bf16 v[188:191], a[196:199], a[76:79], v[188:191]\n"
      "global_load_dwordx4 a[196:199], %0, s[20:21] offset:1024\n"
      "v_pk_max_i16 v252, v252, 0\n"
      "s_waitcnt vmcnt(27)\n"
      "v_mfma_f32_16x16x32_bf16 v[176:179], a[200:203], v[112:115], v[176:179]\n"
      "v_pk_max_i16 v253, v253, 0\n"
      "v_mfma_f32_16x16x32_bf16 v[180:183], a[200:203], v[116:119], v[180:183]\n"
      "v_pk_max_i16 v254, v254, 0\n"
      "v_mfma_f32_16x16x32_bf16 v[184:187], a[200:203], a[80:83], v[184:187]\n"
      "v_pk_max_i16 v255, v255, 0\n"
      "v_mfma_f32_16x16x32_bf16 v[188:191], a[200:203], a[84:87], v[188:191]\n"
      "global_load_dwordx4 a[200:203], %0, s[20:21] offset:2048\n"
      "ds_write_b128 %3, v[252:255] offset:15360\n"
      "s_waitcnt vmcnt(27)\n"
      "v_mfma_f32_16x16x32_bf16 v[176:179], a[204:207], v[120:123], v[176:179]\n"
      "v_mfma_f32_16x16x32_bf16 v[180:183], a[204:207], v[124:127], v[180:183]\n"
      "v_mfma_f32_16x16x32_bf16 v[184:187], a[204:207], a[88:91], v[184:187]\n"
      "v_mfma_f32_16x16x32_bf16 v[188:191], a[204:207], a[92:95], v[188:191]\n"
      "global_load_dwordx4 a[204:207], %0, s[20:21] offset:3072\n"
      "s_waitcnt vmcnt(27)\n"
      "v_mfma_f32_16x16x32_bf16 v[176:179], a[208:211], v[128:131], v[176:179]\n"
      "v_mfma_f32_16x16x32_bf16 v[180:183], a[208:211], v[132:135], v[180:183]\n"
      "v_mfma_f32_16x16x32_bf16 v[184:187], a[208:211], a[96:99], v[184:187]\n"
      "v_mfma_f32_16x16x32_bf16 v[188:191], a[208:211], a[100:103], v[188:191]\n"
      "s_add_u32 s20, s20, 8192\n"
      "s_addc_u32 s21, s21, 0\n"
      "global_load_dwordx4 a[208:211], %0, s[20:21] offset:-4096\n"
      "s_waitcnt vmcnt(27)\n"
      "v_mfma_f32_16x16x32_bf16 v[176:179], a[212:215], v[136:139], v[176:179]\n"
      "v_mfma_f32_16x16x32_bf16 v[180:183], a[212:215], v[140:143], v[180:183]\n"
      "v_mfma_f32_16x16x32_bf16 v[184:187], a[212:215], a[104:107], v[184:187]\n"
      "v_mfma_f32_16x16x32_bf16 v[188:191], a[212:215], a[108:111], v[188:191]\n"
      "global_load_dwordx4 a[212:215], %0, s[20:21] offset:-3072\n"
      "s_waitcnt vmcnt(27)\n"
      "v_mfma_f32_16x16x32_bf16 v[176:179], a[216:219], v[144:147], v[176:179]\n"
      "v_mfma_f32_16x16x32_bf16 v[180:183], a[216:219], v[148:151], v[180:183]\n"
      "v_mfma_f32_16x16x32_bf16 v[184:187], a[216:219], a[112:115], v[184:187]\n"
      "v_mfma_f32_16x16x32_bf16 v[188:191], a[216:219], a[116:119], v[188:191]\n"
      "global_load_dwordx4 a[216:219], %0, s[20:21] offset:-2048\n"
      "s_waitcnt vmcnt(27)\n"
      "v_mfma_f32_16x16x32_bf16 v[176:179], a[220:223], v[152:155], v[176:179]\n"
      "v_mfma_f32_16x16x32_bf16 v[180:183], a[220:223], v[156:159], v[180:183]\n"
      "v_mfma_f32_16x16x32_bf16 v[184:187], a[220:223], a[120:123], v[184:187]\n"
      "v_mfma_f32_16x16x32_bf16 v[188:191], a[220:223], a[124:127], v[188:191]\n"
      "global_load_dwordx4 a[220:223], %0, s[20:21] offset:-1024\n"
      "s_waitcnt vmcnt(27)\n"
      "v_mfma_f32_16x16x32_bf16 v[176:179], a[224:227], v[160:163], v[176:179]\n"
      "v_mfma_f32_16x16x32_bf16 v[180:183], a[224:227], v[164:167], v[180:183]\n"
      "v_mfma_f32_16x16x32_bf16 v[184:187], a[224:227], a[128:131], v[184:187]\n"
      "v_mfma_f32_16x16x32_bf16 v[188:191], a[224:227], a[132:135], v[188:191]\n"
      "global_load_dwordx4 a[224:227], %0, s[20:21] offset:0\n"
      "s_waitcnt vmcnt(27)\n"
      "v_mfma_f32_16x16x32_bf16 v[176:179], a[228:231], v[168:171], v[176:179]\n"
      "v_mfma_f32_16x16x32_bf16 v[180:183], a[228:231], v[172:175], v[180:183]\n"
      "v_mfma_f32_16x16x32_bf16 v[184:187], a[228:231], a[136:139], v[184:187]\n"
      "v_mfma_f32_16x16x32_bf16 v[188:191], a[228:231], a[140:143], v[188:191]\n"
      "global_load_dwordx4 a[228:231], %0, s[20:21] offset:1024\n"
      "s_nop 1\n"
      "s_nop 7\n"
      "v_cvt_pk_bf16_f32 v240, v176, v177\n"
      "v_cvt_pk_bf16_f32 v241, v178, v179\n"
      "v_pk_max_i16 v240, v240, 0\n"
      "v_pk_max_i16 v241, v241, 0\n"
      "ds_write_b64 %4, v[240:241] offset:0\n"
      "v_cvt_pk_bf16_f32 v242, v180, v181\n"
      "v_cvt_pk_bf16_f32 v243, v182, v183\n"
      "v_pk_max_i16 v242, v242, 0\n"
      "v_pk_max_i16 v243, v243, 0\n"
      "ds_write_b64 %4, v[242:243] offset:1024\n"
      "v_cvt_pk_bf16_f32 v244, v184, v185\n"
      "v_cvt_pk_bf16_f32 v245, v186, v187\n"
      "v_pk_max_i16 v244, v244, 0\n"
      "v_pk_max_i16 v245, v245, 0\n"
      "ds_write_b64 %4, v[244:245] offset:2048\n"
      "v_cvt_pk_bf16_f32 v246, v188, v189\n"
      "v_cvt_pk_bf16_f32 v247, v190, v191\n"
      "v_pk_max_i16 v246, v246, 0\n"
      "v_pk_max_i16 v247, v247, 0\n"
      "ds_write_b64 %4, v[246:247] offset:3072\n"
      "s_waitcnt lgkmcnt(0)\n"
      "s_barrier\n"
      "ds_read_b128 v[32:35], %1 offset:0\n"
      "ds_read_b128 v[36:39], %1 offset:1024\n"
      "ds_read_b128 a[0:3], %1 offset:2048\n"
      "ds_read_b128 a[4:7], %1 offset:3072\n"
      "ds_read_b128 v[40:43], %1 offset:4096\n"
      "ds_read_b128 v[44:47], %1 offset:5120\n"
      "ds_read_b128 a[8:11], %1 offset:6144\n"
      "ds_read_b128 a[12:15], %1 offset:7168\n"
      "s_waitcnt vmcnt(26)\n"
      "s_waitcnt lgkmcnt(7)\n"
      "v_mfma_f32_16x16x32_bf16 v[176:179], a[232:235], v[32:35], 0\n"
      "ds_read_b128 v[48:51], %1 offset:8192\n"
      "ds_read_b128 v[52:55], %1 offset:9216\n"
      "ds_read_b128 a[16:19], %1 offset:10240\n"
      "ds_read_b128 a[20:23], %1 offset:11264\n"
      "s_waitcnt lgkmcnt(10)\n"
      "v_mfma_f32_16x16x32_bf16 v[180:183], a[232:235], v[36:39], 0\n"
      "s_waitcnt lgkmcnt(9)\n"
      "v_mfma_f32_16x16x32_bf16 v[184:187], a[232:235], a[0:3], 0\n"
      "s_waitcnt lgkmcnt(8)\n"
      "v_mfma_f32_16x16x32_bf16 v[188:191], a[232:235], a[4:7], 0\n"
      "global_load_dwordx4 a[232:235], %0, s[20:21] offset:2048\n"
      "v_mfma_f32_16x16x32_bf16 v[192:195], a[236:239], v[32:35], 0\n"
      "v_mfma_f32_16x16x32_bf16 v[196:199], a[236:239], v[36:39], 0\n"
      "v_mfma_f32_16x16x32_bf16 v[200:203], a[236:239], a[0:3], 0\n"
      "v_mfma_f32_16x16x32_bf16 v[204:207], a[236:239], a[4:7], 0\n"
      "global_load_dwordx4 a[236:239], %0, s[20:21] offset:3072\n"
      "s_waitcnt vmcnt(26)\n"
      "s_waitcnt lgkmcnt(7)\n"
      "v_mfma_f32_16x16x32_bf16 v[176:179], a[240:243], v[40:43], v[176:179]\n"
      "ds_read_b128 v[56:59], %1 offset:12288\n"
      "ds_read_b128 v[60:63], %1 offset:13312\n"
      "ds_read_b128 a[24:27], %1 offset:14336\n"
      "ds_read_b128 a[28:31], %1 offset:15360\n"
      "s_waitcnt lgkmcnt(10)\n"
      "v_mfma_f32_16x16x32_bf16 v[180:183], a[240:243], v[44:47], v[180:183]\n"
      "s_waitcnt lgkmcnt(9)\n"
      "v_mfma_f32_16x16x32_bf16 v[184:187], a[240:243], a[8:11], v[184:187]\n"
      "s_waitcnt lgkmcnt(8)\n"
      "v_mfma_f32_16x16x32_bf16 v[188:191], a[240:243], a[12:15], v[188:191]\n"
      "s_add_u32 s20, s20, 8192\n"
      "s_addc_u32 s21, s21, 0\n"
      "global_load_dwordx4 a[240:243], %0, s[20:21] offset:-4096\n"
      "v_mfma_f32_16x16x32_bf16 v[192:195], a[244:247], v[40:43], v[192:195]\n"
      "v_mfma_f32_16x16x32_bf16 v[196:199], a[244:247], v[44:47], v[196:199]\n"
      "v_mfma_f32_16x16x32_bf16 v[200:203], a[244:247], a[8:11], v[200:203]\n"
      "v_mfma_f32_16x16x32_bf16 v[204:207], a[244:247], a[12:15], v[204:207]\n"
      "global_load_dwordx4 a[244:247], %0, s[20:21] offset:-3072\n"
      "s_waitcnt vmcnt(26)\n"
      "s_waitcnt lgkmcnt(7)\n"
      "v_mfma_f32_16x16x32_bf16 v[176:179], a[248:251], v[48:51], v[176:179]\n"
      "ds_read_b128 v[64:67], %1 offset:16384\n"
      "ds_read_b128 v[68:71], %1 offset:17408\n"
      "ds_read_b128 a[32:35], %1 offset:18432\n"
      "ds_read_b128 a[36:39], %1 offset:19456\n"
      "s_waitcnt lgkmcnt(10)\n"
      "v_mfma_f32_16x16x32_bf16 v[180:183], a[248:251], v[52:55], v[180:183]\n"
      "s_waitcnt lgkmcnt(9)\n"
      "v_mfma_f32_16x16x32_bf16 v[184:187], a[248:251], a[16:19], v[184:187]\n"
      "s_waitcnt lgkmcnt(8)\n"
      "v_mfma_f32_16x16x32_bf16 v[188:191], a[248:251], a[20:23], v[188:191]\n"
      "global_load_dwordx4 a[248:251], %0, s[20:21] offset:-2048\n"
      "v_mfma_f32_16x16x32_bf16 v[192:195], a[252:255], v[48:51], v[192:195]\n"
      "v_mfma_f32_16x16x32_bf16 v[196:199], a[252:255], v[52:55], v[196:199]\n"
      "v_mfma_f32_16x16x32_bf16 v[200:203], a[252:255], a[16:19], v[200:203]\n"
      "v_mfma_f32_16x16x32_bf16 v[204:207], a[252:255], a[20:23], v[204:207]\n"
      "global_load_dwordx4 a[252:255], %0, s[20:21] offset:-1024\n"
      "s_waitcnt vmcnt(26)\n"
      "s_waitcnt lgkmcnt(7)\n"
      "v_mfma_f32_16x16x32_bf16 v[176:179], a[144:147], v[56:59], v[176:179]\n"
      "ds_read_b128 v[72:75], %1 offset:20480\n"
      "ds_read_b128 v[76:79], %1 offset:21504\n"
      "ds_read_b128 a[40:43], %1 offset:22528\n"
      "ds_read_b128 a[44:47], %1 offset:23552\n"
      "s_waitcnt lgkmcnt(10)\n"
      "v_mfma_f32_16x16x32_bf16 v[180:183], a[144:147], v[60:63], v[180:183]\n"
      "s_waitcnt lgkmcnt(9)\n"
      "v_mfma_f32_16x16x32_bf16 v[184:187], a[144:147], a[24:27], v[184:187]\n"
      "s_waitcnt lgkmcnt(8)\n"
      "v_mfma_f32_16x16x32_bf16 v[188:191], a[144:147], a[28:31], v[188:191]\n"
      "global_load_dwordx4 a[144:147], %0, s[20:21] offset:0\n"
      "v_mfma_f32_16x16x32_bf16 v[192:195], a[148:151], v[56:59], v[192:195]\n"
      "v_mfma_f32_16x16x32_bf16 v[196:199], a[148:151], v[60:63], v[196:199]\n"
      "v_mfma_f32_16x16x32_bf16 v[200:203], a[148:151], a[24:27], v[200:203]\n"
      "v_mfma_f32_16x16x32_bf16 v[204:207], a[148:151], a[28:31], v[204:207]\n"
      "global_load_dwordx4 a[148:151], %0, s[20:21] offset:1024\n"
      "s_waitcnt vmcnt(26)\n"
      "s_waitcnt lgkmcnt(7)\n"
      "v_mfma_f32_16x16x32_bf16 v[176:179], a[152:155], v[64:67], v[176:179]\n"
      "ds_read_b128 v[80:83], %1 offset:24576\n"
      "ds_read_b128 v[84:87], %1 offset:25600\n"
      "ds_read_b128 a[48:51], %1 offset:26624\n"
      "ds_read_b128 a[52:55], %1 offset:27648\n"
      "s_waitcnt lgkmcnt(10)\n"
      "v_mfma_f32_16x16x32_bf16 v[180:183], a[152:155], v[68:71], v[180:183]\n"
      "s_waitcnt lgkmcnt(9)\n"
      "v_mfma_f32_16x16x32_bf16 v[184:187], a[152:155], a[32:35], v[184:187]\n"
      "s_waitcnt lgkmcnt(8)\n"
      "v_mfma_f32_16x16x32_bf16 v[188:191], a[152:155], a[36:39], v[188:191]\n"
      "global_load_dwordx4 a[152:155], %0, s[20:21] offset:2048\n"
      "v_mfma_f32_16x16x32_bf16 v[192:195], a[156:159], v[64:67], v[192:195]\n"
      "v_mfma_f32_16x16x32_bf16 v[196:199], a[156:159], v[68:71], v[196:199]\n"
      "v_mfma_f32_16x16x32_bf16 v[200:203], a[156:159], a[32:35], v[200:203]\n"
      "v_mfma_f32_16x16x32_bf16 v[204:207], a[156:159], a[36:39], v[204:207]\n"
      "global_load_dwordx4 a[156:159], %0, s[20:21] offset:3072\n"
      "s_waitcnt vmcnt(26)\n"
      "s_waitcnt lgkmcnt(7)\n"
      "v_mfma_f32_16x16x32_bf16 v[176:179], a[160:163], v[72:75], v[176:179]\n"
      "ds_read_b128 v[88:91], %1 offset:28672\n"
      "ds_read_b128 v[92:95], %1 offset:29696\n"
      "ds_read_b128 a[56:59], %1 offset:30720\n"
      "ds_read_b128 a[60:63], %1 offset:31744\n"
      "s_waitcnt lgkmcnt(10)\n"
      "v_mfma_f32_16x16x32_bf16 v[180:183], a[160:163], v[76:79], v[180:183]\n"
      "s_waitcnt lgkmcnt(9)\n"
      "v_mfma_f32_16x16x32_bf16 v[184:187], a[160:163], a[40:43], v[184:187]\n"
      "s_waitcnt lgkmcnt(8)\n"
      "v_mfma_f32_16x16x32_bf16 v[188:191], a[160:163], a[44:47], v[188:191]\n"
      "s_add_u32 s20, s20, 8192\n"
      "s_addc_u32 s21, s21, 0\n"
      "global_load_dwordx4 a[160:163], %0, s[20:21] offset:-4096\n"
      "v_mfma_f32_16x16x32_bf16 v[192:195], a[164:167], v[72:75], v[192:195]\n"
      "v_mfma_f32_16x16x32_bf16 v[196:199], a[164:167], v[76:79], v[196:199]\n"
      "v_mfma_f32_16x16x32_bf16 v[200:203], a[164:167], a[40:43], v[200:203]\n"
      "v_mfma_f32_16x16x32_bf16 v[204:207], a[164:167], a[44:47], v[204:207]\n"
      "global_load_dwordx4 a[164:167], %0, s[20:21] offset:-3072\n"
      "s_waitcnt vmcnt(26)\n"
      "s_waitcnt lgkmcnt(7)\n"
      "v_mfma_f32_16x16x32_bf16 v[176:179], a[168:171], v[80:83], v[176:179]\n"
      "ds_read_b128 v[96:99], %1 offset:32768\n"
      "ds_read_b128 v[100:103], %1 offset:33792\n"
      "ds_read_b128 a[64:67], %1 offset:34816\n"
      "ds_read_b128 a[68:71], %1 offset:35840\n"
      "s_waitcnt lgkmcnt(10)\n"
      "v_mfma_f32_16x16x32_bf16 v[180:183], a[168:171], v[84:87], v[180:183]\n"
      "s_waitcnt lgkmcnt(9)\n"
      "v_mfma_f32_16x16x32_bf16 v[184:187], a[168:171], a[48:51], v[184:187]\n"
      "s_waitcnt lgkmcnt(8)\n"
      "v_mfma_f32_16x16x32_bf16 v[188:191], a[168:171], a[52:55], v[188:191]\n"
      "global_load_dwordx4 a[168:171], %0, s[20:21] offset:-2048\n"
      "v_mfma_f32_16x16x32_bf16 v[192:195], a[172:175], v[80:83], v[192:195]\n"
      "v_mfma_f32_16x16x32_bf16 v[196:199], a[172:175], v[84:87], v[196:199]\n"
      "v_mfma_f32_16x16x32_bf16 v[200:203], a[172:175], a[48:51], v[200:203]\n"
      "v_mfma_f32_16x16x32_bf16 v[204:207], a[172:175], a[52:55], v[204:207]\n"
      "global_load_dwordx4 a[172:175], %0, s[20:21] offset:-1024\n"
      "s_waitcnt vmcnt(26)\n"
      "s_waitcnt lgkmcnt(7)\n"
      "v_mfma_f32_16x16x32_bf16 v[176:179], a[176:179], v[88:91], v[176:179]\n"
      "ds_read_b128 v[104:107], %2 offset:0\n"
      "ds_read_b128 v[108:111], %2 offset:1024\n"
      "ds_read_b128 a[72:75], %2 offset:2048\n"
      "ds_read_b128 a[76:79], %2 offset:3072\n"
      "s_waitcnt lgkmcnt(10)\n"
      "v_mfma_f32_16x16x32_bf16 v[180:183], a[176:179], v[92:95], v[180:183]\n"
      "s_waitcnt lgkmcnt(9)\n"
      "v_mfma_f32_16x16x32_bf16 v[184:187], a[176:179], a[56:59], v[184:187]\n"
      "s_waitcnt lgkmcnt(8)\n"
      "v_mfma_f32_16x16x32_bf16 v[188:191], a[176:179], a[60:63], v[188:191]\n"
      "global_load_dwordx4 a[176:179], %0, s[20:21] offset:0\n"
      "v_mfma_f32_16x16x32_bf16 v[192:195], a[180:183], v[88:91], v[192:195]\n"
      "v_mfma_f32_16x16x32_bf16 v[196:199], a[180:183], v[92:95], v[196:199]\n"
      "v_mfma_f32_16x16x32_bf16 v[200:203], a[180:183], a[56:59], v[200:203]\n"
      "v_mfma_f32_16x16x32_bf16 v[204:207], a[180:183], a[60:63], v[204:207]\n"
      "global_load_dwordx4 a[180:183], %0, s[20:21] offset:1024\n"
      "s_waitcnt vmcnt(26)\n"
      "s_waitcnt lgkmcnt(7)\n"
      "v_mfma_f32_16x16x32_bf16 v[176:179], a[184:187], v[96:99], v[176:179]\n"
      "ds_read_b128 v[112:115], %2 offset:4096\n"
      "ds_read_b128 v[116:119], %2 offset:5120\n"
      "ds_read_b128 a[80:83], %2 offset:6144\n"
      "ds_read_b128 a[84:87], %2 offset:7168\n"
      "s_waitcnt lgkmcnt(10)\n"
      "v_mfma_f32_16x16x32_bf16 v[180:183], a[184:187], v[100:103], v[180:183]\n"
      "s_waitcnt lgkmcnt(9)\n"
      "v_mfma_f32_16x16x32_bf16 v[184:187], a[184:187], a[64:67], v[184:187]\n"
      "s_waitcnt lgkmcnt(8)\n"
      "v_mfma_f32_16x16x32_bf16 v[188:191], a[184:187], a[68:71], v[188:191]\n"
      "global_load_dwordx4 a[184:187], %0, s[20:21] offset:2048\n"
      "v_mfma_f32_16x16x32_bf16 v[192:195], a[188:191], v[96:99], v[192:195]\n"
      "v_mfma_f32_16x16x32_bf16 v[196:199], a[188:191], v[100:103], v[196:199]\n"
      "v_mfma_f32_16x16x32_bf16 v[200:203], a[188:191], a[64:67], v[200:203]\n"
      "v_mfma_f32_16x16x32_bf16 v[204:207], a[188:191], a[68:71], v[204:207]\n"
      "global_load_dwordx4 a[188:191], %0, s[20:21] offset:3072\n"
      "s_waitcnt vmcnt(26)\n"
      "s_waitcnt lgkmcnt(7)\n"
      "v_mfma_f32_16x16x32_bf16 v[176:179], a[192:195], v[104:107], v[176:179]\n"
      "ds_read_b128 v[120:123], %2 offset:8192\n"
      "ds_read_b128 v[124:127], %2 offset:9216\n"
      "ds_read_b128 a[88:91], %2 offset:10240\n"
      "ds_read_b128 a[92:95], %2 offset:11264\n"
      "s_waitcnt lgkmcnt(10)\n"
      "v_mfma_f32_16x16x32_bf16 v[180:183], a[192:195], v[108:111], v[180:183]\n"
      "s_waitcnt lgkmcnt(9)\n"
      "v_mfma_f32_16x16x32_bf16 v[184:187], a[192:195], a[72:75], v[184:187]\n"
      "s_waitcnt lgkmcnt(8)\n"
      "v_mfma_f32_16x16x32_bf16 v[188:191], a[192:195], a[76:79], v[188:191]\n"
      "s_add_u32 s20, s20, 8192\n"
      "s_addc_u32 s21, s21, 0\n"
      "global_load_dwordx4 a[192:195], %0, s[20:21] offset:-4096\n"
      "v_mfma_f32_16x16x32_bf16 v[192:195], a[196:199], v[104:107], v[192:195]\n"
      "v_mfma_f32_16x16x32_bf16 v[196:199], a[196:199], v[108:111], v[196:199]\n"
      "v_mfma_f32_16x16x32_bf16 v[200:203], a[196:199], a[72:75], v[200:203]\n"
      "v_mfma_f32_16x16x32_bf16 v[204:207], a[196:199], a[76:79], v[204:207]\n"
      "global_load_dwordx4 a[196:199], %0, s[20:21] offset:-3072\n"
      "s_waitcnt vmcnt(26)\n"
      "s_waitcnt lgkmcnt(7)\n"
      "v_mfma_f32_16x16x32_bf16 v[176:179], a[200:203], v[112:115], v[176:179]\n"
      "ds_read_b128 v[128:131], %2 offset:12288\n"
      "ds_read_b128 v[132:135], %2 offset:13312\n"
      "ds_read_b128 a[96:99], %2 offset:14336\n"
      "ds_read_b128 a[100:103], %2 offset:15360\n"
      "s_waitcnt lgkmcnt(10)\n"
      "v_mfma_f32_16x16x32_bf16 v[180:183], a[200:203], v[116:119], v[180:183]\n"
      "s_waitcnt lgkmcnt(9)\n"
      "v_mfma_f32_16x16x32_bf16 v[184:187], a[200:203], a[80:83], v[184:187]\n"
      "s_waitcnt lgkmcnt(8)\n"
      "v_mfma_f32_16x16x32_bf16 v[188:191], a[200:203], a[84:87], v[188:191]\n"
      "global_load_dwordx4 a[200:203], %0, s[20:21] offset:-2048\n"
      "v_mfma_f32_16x16x32_bf16 v[192:195], a[204:207], v[112:115], v[192:195]\n"
      "v_mfma_f32_16x16x32_bf16 v[196:199], a[204:207], v[116:119], v[196:199]\n"
      "v_mfma_f32_16x16x32_bf16 v[200:203], a[204:207], a[80:83], v[200:203]\n"
      "v_mfma_f32_16x16x32_bf16 v[204:207], a[204:207], a[84:87], v[204:207]\n"
      "global_load_dwordx4 a[204:207], %0, s[20:21] offset:-1024\n"
      "s_waitcnt vmcnt(26)\n"
      "s_waitcnt lgkmcnt(7)\n"
      "v_mfma_f32_16x16x32_bf16 v[176:179], a[208:211], v[120:123], v[176:179]\n"
      "ds_read_b128 v[136:139], %2 offset:16384\n"
      "ds_read_b128 v[140:143], %2 offset:17408\n"
      "ds_read_b128 a[104:107], %2 offset:18432\n"
      "ds_read_b128 a[108:111], %2 offset:19456\n"
      "s_waitcnt lgkmcnt(10)\n"
      "v_mfma_f32_16x16x32_bf16 v[180:183], a[208:211], v[124:127], v[180:183]\n"
      "s_waitcnt lgkmcnt(9)\n"
      "v_mfma_f32_16x16x32_bf16 v[184:187], a[208:211], a[88:91], v[184:187]\n"
      "s_waitcnt lgkmcnt(8)\n"
      "v_mfma_f32_16x16x32_bf16 v[188:191], a[208:211], a[92:95], v[188:191]\n"
      "global_load_dwordx4 a[208:211], %0, s[20:21] offset:0\n"
      "v_mfma_f32_16x16x32_bf16 v[192:195], a[212:215], v[120:123], v[192:195]\n"
      "v_mfma_f32_16x16x32_bf16 v[196:199], a[212:215], v[124:127], v[196:199]\n"
      "v_mfma_f32_16x16x32_bf16 v[200:203], a[212:215], a[88:91], v[200:203]\n"
      "v_mfma_f32_16x16x32_bf16 v[204:207], a[212:215], a[92:95], v[204:207]\n"
      "global_load_dwordx4 a[212:215], %0, s[20:21] offset:1024\n"
      "s_waitcnt vmcnt(26)\n"
      "s_waitcnt lgkmcnt(7)\n"
      "v_mfma_f32_16x16x32_bf16 v[176:179], a[216:219], v[128:131], v[176:179]\n"
      "ds_read_b128 v[144:147], %2 offset:20480\n"
      "ds_read_b128 v[148:151], %2 offset:21504\n"
      "ds_read_b128 a[112:115], %2 offset:22528\n"
      "ds_read_b128 a[116:119], %2 offset:23552\n"
      "s_waitcnt lgkmcnt(10)\n"
      "v_mfma_f32_16x16x32_bf16 v[180:183], a[216:219], v[132:135], v[180:183]\n"
      "s_waitcnt lgkmcnt(9)\n"
      "v_mfma_f32_16x16x32_bf16 v[184:187], a[216:219], a[96:99], v[184:187]\n"
      "s_waitcnt lgkmcnt(8)\n"
      "v_mfma_f32_16x16x32_bf16 v[188:191], a[216:219], a[100:103], v[188:191]\n"
      "global_load_dwordx4 a[216:219], %0, s[20:21] offset:2048\n"
      "v_mfma_f32_16x16x32_bf16 v[192:195], a[220:223], v[128:131], v[192:195]\n"
      "v_mfma_f32_16x16x32_bf16 v[196:199], a[220:223], v[132:135], v[196:199]\n"
      "v_mfma_f32_16x16x32_bf16 v[200:203], a[220:223], a[96:99], v[200:203]\n"
      "v_mfma_f32_16x16x32_bf16 v[204:207], a[220:223], a[100:103], v[204:207]\n"
      "global_load_dwordx4 a[220:223], %0, s[20:21] offset:3072\n"
      "s_waitcnt vmcnt(26)\n"
      "s_waitcnt lgkmcnt(7)\n"
      "v_mfma_f32_16x16x32_bf16 v[176:179], a[224:227], v[136:139], v[176:179]\n"
      "ds_read_b128 v[152:155], %2 offset:24576\n"
      "ds_read_b128 v[156:159], %2 offset:25600\n"
      "ds_read_b128 a[120:123], %2 offset:26624\n"
      "ds_read_b128 a[124:127], %2 offset:27648\n"
      "s_waitcnt lgkmcnt(10)\n"
      "v_mfma_f32_16x16x32_bf16 v[180:183], a[224:227], v[140:143], v[180:183]\n"
      "s_waitcnt lgkmcnt(9)\n"
      "v_mfma_f32_16x16x32_bf16 v[184:187], a[224:227], a[104:107], v[184:187]\n"
      "s_waitcnt lgkmcnt(8)\n"
      "v_mfma_f32_16x16x32_bf16 v[188:191], a[224:227], a[108:111], v[188:191]\n"
      "ds_read_b128 a[224:227], %22 offset:0\n"
      "v_mfma_f32_16x16x32_bf16 v[192:195], a[228:231], v[136:139], v[192:195]\n"
      "v_mfma_f32_16x16x32_bf16 v[196:199], a[228:231], v[140:143], v[196:199]\n"
      "v_mfma_f32_16x16x32_bf16 v[200:203], a[228:231], a[104:107], v[200:203]\n"
      "v_mfma_f32_16x16x32_bf16 v[204:207], a[228:231], a[108:111], v[204:207]\n"
      "s_add_u32 s20, s20, 8192\n"
      "s_addc_u32 s21, s21, 0\n"
      "global_load_dwordx4 a[228:231], %0, s[20:21] offset:-3072\n"
      "s_waitcnt vmcnt(25)\n"
      "s_waitcnt lgkmcnt(8)\n"
      "v_mfma_f32_16x16x32_bf16 v[176:179], a[232:235], v[144:147], v[176:179]\n"
      "ds_read_b128 v[160:163], %2 offset:28672\n"
      "ds_read_b128 v[164:167], %2 offset:29696\n"
      "ds_read_b128 a[128:131], %2 offset:30720\n"
      "ds_read_b128 a[132:135], %2 offset:31744\n"
      "s_waitcnt lgkmcnt(11)\n"
      "v_mfma_f32_16x16x32_bf16 v[180:183], a[232:235], v[148:151], v[180:183]\n"
      "s_waitcnt lgkmcnt(10)\n"
      "v_mfma_f32_16x16x32_bf16 v[184:187], a[232:235], a[112:115], v[184:187]\n"
      "s_waitcnt lgkmcnt(9)\n"
      "v_mfma_f32_16x16x32_bf16 v[188:191], a[232:235], a[116:119], v[188:191]\n"
      "global_load_dwordx4 a[232:235], %0, s[20:21] offset:-2048\n"
      "v_mfma_f32_16x16x32_bf16 v[192:195], a[236:239], v[144:147], v[192:195]\n"
      "v_mfma_f32_16x16x32_bf16 v[196:199], a[236:239], v[148:151], v[196:199]\n"
      "v_mfma_f32_16x16x32_bf16 v[200:203], a[236:239], a[112:115], v[200:203]\n"
      "v_mfma_f32_16x16x32_bf16 v[204:207], a[236:239], a[116:119], v[204:207]\n"
      "global_load_dwordx4 a[236:239], %0, s[20:21] offset:-1024\n"
      "s_waitcnt vmcnt(25)\n"
      "s_waitcnt lgkmcnt(8)\n"
      "v_mfma_f32_16x16x32_bf16 v[176:179], a[240:243], v[152:155], v[176:179]\n"
      "ds_read_b128 v[168:171], %2 offset:32768\n"
      "ds_read_b128 v[172:175], %2 offset:33792\n"
      "ds_read_b128 a[136:139], %2 offset:34816\n"
      "ds_read_b128 a[140:143], %2 offset:35840\n"
      "s_waitcnt lgkmcnt(11)\n"
      "v_mfma_f32_16x16x32_bf16 v[180:183], a[240:243], v[156:159], v[180:183]\n"
      "s_waitcnt lgkmcnt(10)\n"
      "v_mfma_f32_16x16x32_bf16 v[184:187], a[240:243], a[120:123], v[184:187]\n"
      "s_waitcnt lgkmcnt(9)\n"
      "v_mfma_f32_16x16x32_bf16 v[188:191], a[240:243], a[124:127], v[188:191]\n"
      "global_load_dwordx4 a[240:243], %0, s[20:21] offset:0\n"
      "v_mfma_f32_16x16x32_bf16 v[192:195], a[244:247], v[152:155], v[192:195]\n"
      "v_mfma_f32_16x16x32_bf16 v[196:199], a[244:247], v[156:159], v[196:199]\n"
      "v_mfma_f32_16x16x32_bf16 v[200:203], a[244:247], a[120:123], v[200:203]\n"
      "v_mfma_f32_16x16x32_bf16 v[204:207], a[244:247], a[124:127], v[204:207]\n"
      "global_load_dwordx4 a[244:247], %0, s[20:21] offset:1024\n"
      "s_waitcnt vmcnt(25)\n"
      "s_waitcnt lgkmcnt(7)\n"
      "v_mfma_f32_16x16x32_bf16 v[176:179], a[248:251], v[160:163], v[176:179]\n"
      "s_waitcnt lgkmcnt(6)\n"
      "v_mfma_f32_16x16x32_bf16 v[180:183], a[248:251], v[164:167], v[180:183]\n"
      "s_waitcnt lgkmcnt(5)\n"
      "v_mfma_f32_16x16x32_bf16 v[184:187], a[248:251], a[128:131], v[184:187]\n"
      "s_waitcnt lgkmcnt(4)\n"
      "v_mfma_f32_16x16x32_bf16 v[188:191], a[248:251], a[132:135], v[188:191]\n"
      "global_load_dwordx4 a[248:251], %0, s[20:21] offset:2048\n"
      "v_mfma_f32_16x16x32_bf16 v[192:195], a[252:255], v[160:163], v[192:195]\n"
      "v_mfma_f32_16x16x32_bf16 v[196:199], a[252:255], v[164:167], v[196:199]\n"
      "v_mfma_f32_16x16x32_bf16 v[200:203], a[252:255], a[128:131], v[200:203]\n"
      "v_mfma_f32_16x16x32_bf16 v[204:207], a[252:255], a[132:135], v[204:207]\n"
      "global_load_dwordx4 a[252:255], %0, s[20:21] offset:3072\n"
      "s_waitcnt vmcnt(25)\n"
      "s_waitcnt lgkmcnt(3)\n"
      "v_mfma_f32_16x16x32_bf16 v[176:179], a[144:147], v[168:171], v[176:179]\n"
      "s_waitcnt lgkmcnt(2)\n"
      "v_mfma_f32_16x16x32_bf16 v[180:183], a[144:147], v[172:175], v[180:183]\n"
      "s_waitcnt lgkmcnt(1)\n"
      "v_mfma_f32_16x16x32_bf16 v[184:187], a[144:147], a[136:139], v[184:187]\n"
      "s_waitcnt lgkmcnt(0)\n"
      "v_mfma_f32_16x16x32_bf16 v[188:191], a[144:147], a[140:143], v[188:191]\n"
      "ds_read_b128 a[144:147], %22 offset:1024\n"
      "v_mfma_f32_16x16x32_bf16 v[192:195], a[148:151], v[168:171], v[192:195]\n"
      "v_mfma_f32_16x16x32_bf16 v[196:199], a[148:151], v[172:175], v[196:199]\n"
      "v_mfma_f32_16x16x32_bf16 v[200:203], a[148:151], a[136:139], v[200:203]\n"
      "v_mfma_f32_16x16x32_bf16 v[204:207], a[148:151], a[140:143], v[204:207]\n"
      "s_add_u32 s20, s20, 8192\n"
      "s_addc_u32 s21, s21, 0\n"
      "global_load_dwordx4 a[148:151], %0, s[20:21] offset:-3072\n"
      "s_nop 1\n"
      "s_waitcnt vmcnt(25)\n"
      "v_mfma_f32_16x16x32_bf16 v[208:211], a[152:155], v[32:35], 0\n"
      "v_mfma_f32_16x16x32_bf16 v[212:215], a[152:155], v[36:39], 0\n"
      "v_mfma_f32_16x16x32_bf16 v[216:219], a[152:155], a[0:3], 0\n"
      "v_mfma_f32_16x16x32_bf16 v[220:223], a[152:155], a[4:7], 0\n"
      "global_load_dwordx4 a[152:155], %0, s[20:21] offset:-2048\n"
      "s_waitcnt vmcnt(25)\n"
      "v_mfma_f32_16x16x32_bf16 v[208:211], a[156:159], v[40:43], v[208:211]\n"
      "v_mfma_f32_16x16x32_bf16 v[212:215], a[156:159], v[44:47], v[212:215]\n"
      "v_mfma_f32_16x16x32_bf16 v[216:219], a[156:159], a[8:11], v[216:219]\n"
      "v_mfma_f32_16x16x32_bf16 v[220:223], a[156:159], a[12:15], v[220:223]\n"
      "global_load_dwordx4 a[156:159], %0, s[20:21] offset:-1024\n"
      "s_waitcnt vmcnt(25)\n"
      "v_mfma_f32_16x16x32_bf16 v[208:211], a[160:163], v[48:51], v[208:211]\n"
      "v_mul_f32 v176, v25, v176\n"
      "v_mfma_f32_16x16x32_bf16 v[212:215], a[160:163], v[52:55], v[212:215]\n"
      "v_mul_f32 v177, v25, v177\n"
      "v_mfma_f32_16x16x32_bf16 v[216:219], a[160:163], a[16:19], v[216:219]\n"
      "v_mul_f32 v178, v25, v178\n"
      "v_mfma_f32_16x16x32_bf16 v[220:223], a[160:163], a[20:23], v[220:223]\n"
      "global_load_dwordx4 a[160:163], %0, s[20:21] offset:0\n"
      "v_mul_f32 v179, v25, v179\n"
      "s_waitcnt vmcnt(25)\n"
      "v_mfma_f32_16x16x32_bf16 v[208:211], a[164:167], v[56:59], v[208:211]\n"
      "v_mul_f32 v192, v25, v192\n"
      "v_mfma_f32_16x16x32_bf16 v[212:215], a[164:167], v[60:63], v[212:215]\n"
      "v_mul_f32 v193, v25, v193\n"
      "v_mfma_f32_16x16x32_bf16 v[216:219], a[164:167], a[24:27], v[216:219]\n"
      "v_mul_f32 v194, v25, v194\n"
      "v_mfma_f32_16x16x32_bf16 v[220:223], a[164:167], a[28:31], v[220:223]\n"
      "global_load_dwordx4 a[164:167], %0, s[20:21] offset:1024\n"
      "v_mul_f32 v195, v25, v195\n"
      "s_waitcnt vmcnt(25)\n"
      "v_mfma_f32_16x16x32_bf16 v[208:211], a[168:171], v[64:67], v[208:211]\n"
      "v_cvt_pk_bf16_f32 v240, v176, v177\n"
      "v_mfma_f32_16x16x32_bf16 v[212:215], a[168:171], v[68:71], v[212:215]\n"
      "v_cvt_pk_bf16_f32 v241, v178, v179\n"
      "v_mfma_f32_16x16x32_bf16 v[216:219], a[168:171], a[32:35], v[216:219]\n"
      "v_cvt_pk_bf16_f32 v242, v192, v193\n"
      "v_mfma_f32_16x16x32_bf16 v[220:223], a[168:171], a[36:39], v[220:223]\n"
      "global_load_dwordx4 a[168:171], %0, s[20:21] offset:2048\n"
      "v_cvt_pk_bf16_f32 v243, v194, v195\n"
      "s_waitcnt vmcnt(25)\n"
      "v_mfma_f32_16x16x32_bf16 v[208:211], a[172:175], v[72:75], v[208:211]\n"
      "s_cmp_eq_u32 s28, 0\n"
      "s_cbranch_scc1 .La1_%=\n"
      "global_store_dwordx4 %0, v[240:243], s[22:23] offset:0 sc1\n"
      ".Lb1_%=:\n"
      "v_mfma_f32_16x16x32_bf16 v[212:215], a[172:175], v[76:79], v[212:215]\n"
      "v_mul_f32 v180, v27, v180\n"
      "v_mfma_f32_16x16x32_bf16 v[216:219], a[172:175], a[40:43], v[216:219]\n"
      "v_mul_f32 v181, v27, v181\n"
      "v_mfma_f32_16x16x32_bf16 v[220:223], a[172:175], a[44:47], v[220:223]\n"
      "global_load_dwordx4 a[172:175], %0, s[20:21] offset:3072\n"
      "v_mul_f32 v182, v27, v182\n"
      "s_waitcnt vmcnt(25)\n"
      "v_mfma_f32_16x16x32_bf16 v[208:211], a[176:179], v[80:83], v[208:211]\n"
      "v_mul_f32 v183, v27, v183\n"
      "v_mfma_f32_16x16x32_bf16 v[212:215], a[176:179], v[84:87], v[212:215]\n"
      "v_mul_f32 v196, v27, v196\n"
      "v_mfma_f32_16x16x32_bf16 v[216:219], a[176:179], a[48:51], v[216:219]\n"
      "v_mul_f32 v197, v27, v197\n"
      "v_mfma_f32_16x16x32_bf16 v[220:223], a[176:179], a[52:55], v[220:223]\n"
      "ds_read_b128 a[176:179], %22 offset:2048\n"
      "v_mul_f32 v198, v27, v198\n"
      "s_waitcnt vmcnt(24)\n"
      "v_mfma_f32_16x16x32_bf16 v[208:211], a[180:183], v[88:91], v[208:211]\n"
      "v_mul_f32 v199, v27, v199\n"
      "v_mfma_f32_16x16x32_bf16 v[212:215], a[180:183], v[92:95], v[212:215]\n"
      "v_cvt_pk_bf16_f32 v244, v180, v181\n"
      "v_mfma_f32_16x16x32_bf16 v[216:219], a[180:183], a[56:59], v[216:219]\n"
      "v_cvt_pk_bf16_f32 v245, v182, v183\n"
      "v_mfma_f32_16x16x32_bf16 v[220:223], a[180:183], a[60:63], v[220:223]\n"
      "s_add_u32 s20, s20, 8192\n"
      "s_addc_u32 s21, s21, 0\n"
      "global_load_dwordx4 a[180:183], %0, s[20:21] offset:-3072\n"
      "v_cvt_pk_bf16_f32 v246, v196, v197\n"
      "s_waitcnt vmcnt(24)\n"
      "v_mfma_f32_16x16x32_bf16 v[208:211], a[184:187], v[96:99], v[208:211]\n"
      "v_cvt_pk_bf16_f32 v247, v198, v199\n"
      "v_mfma_f32_16x16x32_bf16 v[212:215], a[184:187], v[100:103], v[212:215]\n"
      "s_cmp_eq_u32 s28, 0\n"
      "s_cbranch_scc1 .La2_%=\n"
      "global_store_dwordx4 %0, v[244:247], s[22:23] offset:1024 sc1\n"
      ".Lb2_%=:\n"
      "v_mfma_f32_16x16x32_bf16 v[216:219], a[184:187], a[64:67], v[216:219]\n"
      "v_mul_f32 v184, v29, v184\n"
      "v_mfma_f32_16x16x32_bf16 v[220:223], a[184:187], a[68:71], v[220:223]\n"
      "global_load_dwordx4 a[184:187], %0, s[20:21] offset:-2048\n"
      "v_mul_f32 v185, v29, v185\n"
      "s_waitcnt vmcnt(24)\n"
      "v_mfma_f32_16x16x32_bf16 v[208:211], a[188:191], v[104:107], v[208:211]\n"
      "v_mul_f32 v186, v29, v186\n"
      "v_mfma_f32_16x16x32_bf16 v[212:215], a[188:191], v[108:111], v[212:215]\n"
      "v_mul_f32 v187, v29, v187\n"
      "v_mfma_f32_16x16x32_bf16 v[216:219], a[188:191], a[72:75], v[216:219]\n"
      "v_mul_f32 v200, v29, v200\n"
      "v_mfma_f32_16x16x32_bf16 v[220:223], a[188:191], a[76:79], v[220:223]\n"
      "global_load_dwordx4 a[188:191], %0, s[20:21] offset:-1024\n"
      "v_mul_f32 v201, v29, v201\n"
      "s_waitcnt vmcnt(24)\n"
      "v_mfma_f32_16x16x32_bf16 v[208:211], a[192:195], v[112:115], v[208:211]\n"
      "v_mul_f32 v202, v29, v202\n"
      "v_mfma_f32_16x16x32_bf16 v[212:215], a[192:195], v[116:119], v[212:215]\n"
      "v_mul_f32 v203, v29, v203\n"
      "v_mfma_f32_16x16x32_bf16 v[216:219], a[192:195], a[80:83], v[216:219]\n"
      "v_cvt_pk_bf16_f32 v248, v184, v185\n"
      "v_mfma_f32_16x16x32_bf16 v[220:223], a[192:195], a[84:87], v[220:223]\n"
      "global_load_dwordx4 a[192:195], %0, s[20:21] offset:0\n"
      "v_cvt_pk_bf16_f32 v249, v186, v187\n"
      "s_waitcnt vmcnt(24)\n"
      "v_mfma_f32_16x16x32_bf16 v[208:211], a[196:199], v[120:123], v[208:211]\n"
      "v_cvt_pk_bf16_f32 v250, v200, v201\n"
      "v_mfma_f32_16x16x32_bf16 v[212:215], a[196:199], v[124:127], v[212:215]\n"
      "v_cvt_pk_bf16_f32 v251, v202, v203\n"
      "v_mfma_f32_16x16x32_bf16 v[216:219], a[196:199], a[88:91], v[216:219]\n"
      "s_cmp_eq_u32 s28, 1\n"
      "s_cbranch_scc1 .La3_%=\n"
      "global_store_dwordx4 %0, v[248:251], s[22:23] offset:2048 sc1\n"
      ".Lb3_%=:\n"
      "v_mfma_f32_16x16x32_bf16 v[220:223], a[196:199], a[92:95], v[220:223]\n"
      "global_load_dwordx4 a[196:199], %0, s[20:21] offset:1024\n"
      "v_mul_f32 v188, v31, v188\n"
      "s_waitcnt vmcnt(24)\n"
      "v_mfma_f32_16x16x32_bf16 v[208:211], a[200:203], v[128:131], v[208:211]\n"
      "v_mul_f32 v189, v31, v189\n"
      "v_mfma_f32_16x16x32_bf16 v[212:215], a[200:203], v[132:135], v[212:215]\n"
      "v_mul_f32 v190, v31, v190\n"
      "v_mfma_f32_16x16x32_bf16 v[216:219], a[200:203], a[96:99], v[216:219]\n"
      "v_mul_f32 v191, v31, v191\n"
      "v_mfma_f32_16x16x32_bf16 v[220:223], a[200:203], a[100:103], v[220:223]\n"
      "global_load_dwordx4 a[200:203], %0, s[20:21] offset:2048\n"
      "v_mul_f32 v204, v31, v204\n"
      "s_waitcnt vmcnt(24)\n"
      "v_mfma_f32_16x16x32_bf16 v[208:211], a[204:207], v[136:139], v[208:211]\n"
      "v_mul_f32 v205, v31, v205\n"
      "v_mfma_f32_16x16x32_bf16 v[212:215], a[204:207], v[140:143], v[212:215]\n"
      "v_mul_f32 v206, v31, v206\n"
      "v_mfma_f32_16x16x32_bf16 v[216:219], a[204:207], a[104:107], v[216:219]\n"
      "v_mul_f32 v207, v31, v207\n"
      "v_mfma_f32_16x16x32_bf16 v[220:223], a[204:207], a[108:111], v[220:223]\n"
      "global_load_dwordx4 a[204:207], %0, s[20:21] offset:3072\n"
      "v_cvt_pk_bf16_f32 v252, v188, v189\n"
      "s_waitcnt vmcnt(24)\n"
      "v_mfma_f32_16x16x32_bf16 v[208:211], a[208:211], v[144:147], v[208:211]\n"
      "v_cvt_pk_bf16_f32 v253, v190, v191\n"
      "v_mfma_f32_16x16x32_bf16 v[212:215], a[208:211], v[148:151], v[212:215]\n"
      "v_cvt_pk_bf16_f32 v254, v204, v205\n"
      "v_mfma_f32_16x16x32_bf16 v[216:219], a[208:211], a[112:115], v[216:219]\n"
      "v_cvt_pk_bf16_f32 v255, v206, v207\n"
      "v_mfma_f32_16x16x32_bf16 v[220:223], a[208:211], a[116:119], v[220:223]\n"
      "ds_read_b128 a[208:211], %22 offset:3072\n"
      "s_cmp_eq_u32 s28, 1\n"
      "s_cbranch_scc1 .La4_%=\n"
      "global_store_dwordx4 %0, v[252:255], s[22:23] offset:3072 sc1\n"
      ".Lb4_%=:\n"
      "s_waitcnt vmcnt(23)\n"
      "v_mfma_f32_16x16x32_bf16 v[208:211], a[212:215], v[152:155], v[208:211]\n"
      "s_add_u32 s22, s22, 4096\n"
      "s_addc_u32 s23, s23, 0\n"
      "v_mfma_f32_16x16x32_bf16 v[212:215], a[212:215], v[156:159], v[212:215]\n"
      "v_mfma_f32_16x16x32_bf16 v[216:219], a[212:215], a[120:123], v[216:219]\n"
      "v_mfma_f32_16x16x32_bf16 v[220:223], a[212:215], a[124:127], v[220:223]\n"
      "s_add_u32 s20, s20, 8192\n"
      "s_addc_u32 s21, s21, 0\n"
      "global_load_dwordx4 a[212:215], %0, s[20:21] offset:-3072\n"
      "s_waitcnt vmcnt(23)\n"
      "v_mfma_f32_16x16x32_bf16 v[208:211], a[216:219], v[160:163], v[208:211]\n"
      "v_mfma_f32_16x16x32_bf16 v[212:215], a[216:219], v[164:167], v[212:215]\n"
      "v_mfma_f32_16x16x32_bf16 v[216:219], a[216:219], a[128:131], v[216:219]\n"
      "v_mfma_f32_16x16x32_bf16 v[220:223], a[216:219], a[132:135], v[220:223]\n"
      "global_load_dwordx4 a[216:219], %0, s[20:21] offset:-2048\n"
      "s_waitcnt vmcnt(23)\n"
      "v_mfma_f32_16x16x32_bf16 v[208:211], a[220:223], v[168:171], v[208:211]\n"
      "v_mfma_f32_16x16x32_bf16 v[212:215], a[220:223], v[172:175], v[212:215]\n"
      "v_mfma_f32_16x16x32_bf16 v[216:219], a[220:223], a[136:139], v[216:219]\n"
      "v_mfma_f32_16x16x32_bf16 v[220:223], a[220:223], a[140:143], v[220:223]\n"
      "global_load_dwordx4 a[220:223], %0, s[20:21] offset:-1024\n"
      "s_nop 1\n"
      "s_waitcnt vmcnt(23)\n"
      "v_mfma_f32_16x16x32_bf16 v[176:179], a[224:227], v[32:35], 0\n"
      "v_mfma_f32_16x16x32_bf16 v[180:183], a[224:227], v[36:39], 0\n"
      "v_mfma_f32_16x16x32_bf16 v[184:187], a[224:227], a[0:3], 0\n"
      "v_mfma_f32_16x16x32_bf16 v[188:191], a[224:227], a[4:7], 0\n"
      "global_load_dwordx4 a[224:227], %0, s[20:21] offset:0\n"
      "v_mfma_f32_16x16x32_bf16 v[192:195], a[228:231], v[32:35], 0\n"
      "v_mfma_f32_16x16x32_bf16 v[196:199], a[228:231], v[36:39], 0\n"
      "v_mfma_f32_16x16x32_bf16 v[200:203], a[228:231], a[0:3], 0\n"
      "v_mfma_f32_16x16x32_bf16 v[204:207], a[228:231], a[4:7], 0\n"
      "global_load_dwordx4 a[228:231], %0, s[20:21] offset:1024\n"
      "s_waitcnt vmcnt(23)\n"
      "v_mfma_f32_16x16x32_bf16 v[176:179], a[232:235], v[40:43], v[176:179]\n"
      "v_mul_f32 v208, v25, v208\n"
      "v_mfma_f32_16x16x32_bf16 v[180:183], a[232:235], v[44:47], v[180:183]\n"
      "v_mul_f32 v209, v25, v209\n"
      "v_mfma_f32_16x16x32_bf16 v[184:187], a[232:235], a[8:11], v[184:187]\n"
      "v_mul_f32 v210, v25, v210\n"
      "v_mfma_f32_16x16x32_bf16 v[188:191], a[232:235], a[12:15], v[188:191]\n"
      "global_load_dwordx4 a[232:235], %0, s[20:21] offset:2048\n"
      "v_mul_f32 v211, v25, v211\n"
      "v_mfma_f32_16x16x32_bf16 v[192:195], a[236:239], v[40:43], v[192:195]\n"
      "v_cvt_pk_bf16_f32 v240, v208, v209\n"
      "v_mfma_f32_16x16x32_bf16 v[196:199], a[236:239], v[44:47], v[196:199]\n"
      "v_cvt_pk_bf16_f32 v241, v210, v211\n"
      "v_mfma_f32_16x16x32_bf16 v[200:203], a[236:239], a[8:11], v[200:203]\n"
      "ds_write_b64 %5, v[240:241] offset:0\n"
      "v_mfma_f32_16x16x32_bf16 v[204:207], a[236:239], a[12:15], v[204:207]\n"
      "global_load_dwordx4 a[236:239], %0, s[20:21] offset:3072\n"
      "v_mul_f32 v212, v27, v212\n"
      "s_waitcnt vmcnt(23)\n"
      "v_mfma_f32_16x16x32_bf16 v[176:179], a[240:243], v[48:51], v[176:179]\n"
      "v_mul_f32 v213, v27, v213\n"
      "v_mfma_f32_16x16x32_bf16 v[180:183], a[240:243], v[52:55], v[180:183]\n"
      "v_mul_f32 v214, v27, v214\n"
      "v_mfma_f32_16x16x32_bf16 v[184:187], a[240:243], a[16:19], v[184:187]\n"
      "v_mul_f32 v215, v27, v215\n"
      "v_mfma_f32_16x16x32_bf16 v[188:191], a[240:243], a[20:23], v[188:191]\n"
      "ds_read_b128 a[240:243], %22 offset:4096\n"
      "v_cvt_pk_bf16_f32 v242, v212, v213\n"
      "v_mfma_f32_16x16x32_bf16 v[192:195], a[244:247], v[48:51], v[192:195]\n"
      "v_cvt_pk_bf16_f32 v243, v214, v215\n"
      "v_mfma_f32_16x16x32_bf16 v[196:199], a[244:247], v[52:55], v[196:199]\n"
      "ds_write_b64 %5, v[242:243] offset:1024\n"
      "v_mfma_f32_16x16x32_bf16 v[200:203], a[244:247], a[16:19], v[200:203]\n"
      "v_mul_f32 v216, v29, v216\n"
      "v_mfma_f32_16x16x32_bf16 v[204:207], a[244:247], a[20:23], v[204:207]\n"
      "s_add_u32 s20, s20, 8192\n"
      "s_addc_u32 s21, s21, 0\n"
      "global_load_dwordx4 a[244:247], %0, s[20:21] offset:-3072\n"
      "v_mul_f32 v217, v29, v217\n"
      "s_waitcnt vmcnt(22)\n"
      "v_mfma_f32_16x16x32_bf16 v[176:179], a[248:251], v[56:59], v[176:179]\n"
      "v_mul_f32 v218, v29, v218\n"
      "v_mfma_f32_16x16x32_bf16 v[180:183], a[248:251], v[60:63], v[180:183]\n"
      "v_mul_f32 v219, v29, v219\n"
      "v_mfma_f32_16x16x32_bf16 v[184:187], a[248:251], a[24:27], v[184:187]\n"
      "v_cvt_pk_bf16_f32 v244, v216, v217\n"
      "v_mfma_f32_16x16x32_bf16 v[188:191], a[248:251], a[28:31], v[188:191]\n"
      "global_load_dwordx4 a[248:251], %0, s[20:21] offset:-2048\n"
      "v_cvt_pk_bf16_f32 v245, v218, v219\n"
      "v_mfma_f32_16x16x32_bf16 v[192:195], a[252:255], v[56:59], v[192:195]\n"
      "ds_write_b64 %5, v[244:245] offset:2048\n"
      "v_mfma_f32_16x16x32_bf16 v[196:199], a[252:255], v[60:63], v[196:199]\n"
      "v_mul_f32 v220, v31, v220\n"
      "v_mfma_f32_16x16x32_bf16 v[200:203], a[252:255], a[24:27], v[200:203]\n"
      "v_mul_f32 v221, v31, v221\n"
      "v_mfma_f32_16x16x32_bf16 v[204:207], a[252:255], a[28:31], v[204:207]\n"
      "global_load_dwordx4 a[252:255], %0, s[20:21] offset:-1024\n"
      "v_mul_f32 v222, v31, v222\n"
      "s_waitcnt vmcnt(23)\n"
      "s_waitcnt lgkmcnt(6)\n"
      "v_mfma_f32_16x16x32_bf16 v[176:179], a[144:147], v[64:67], v[176:179]\n"
      "v_mul_f32 v223, v31, v223\n"
      "v_mfma_f32_16x16x32_bf16 v[180:183], a[144:147], v[68:71], v[180:183]\n"
      "v_cvt_pk_bf16_f32 v246, v220, v221\n"
      "v_mfma_f32_16x16x32_bf16 v[184:187], a[144:147], a[32:35], v[184:187]\n"
      "v_cvt_pk_bf16_f32 v247, v222, v223\n"
      "v_mfma_f32_16x16x32_bf16 v[188:191], a[144:147], a[36:39], v[188:191]\n"
      "global_load_dwordx4 a[144:147], %0, s[20:21] offset:0\n"
      "ds_write_b64 %5, v[246:247] offset:3072\n"
      "v_mfma_f32_16x16x32_bf16 v[192:195], a[148:151], v[64:67], v[192:195]\n"
      "v_mfma_f32_16x16x32_bf16 v[196:199], a[148:151], v[68:71], v[196:199]\n"
      "v_mfma_f32_16x16x32_bf16 v[200:203], a[148:151], a[32:35], v[200:203]\n"
      "v_mfma_f32_16x16x32_bf16 v[204:207], a[148:151], a[36:39], v[204:207]\n"
      "global_load_dwordx4 a[148:151], %0, s[20:21] offset:1024\n"
      "s_waitcnt vmcnt(23)\n"
      "v_mfma_f32_16x16x32_bf16 v[176:179], a[152:155], v[72:75], v[176:179]\n"
      "v_mfma_f32_16x16x32_bf16 v[180:183], a[152:155], v[76:79], v[180:183]\n"
      "v_mfma_f32_16x16x32_bf16 v[184:187], a[152:155], a[40:43], v[184:187]\n"
      "v_mfma_f32_16x16x32_bf16 v[188:191], a[152:155], a[44:47], v[188:191]\n"
      "global_load_dwordx4 a[152:155], %0, s[20:21] offset:2048\n"
      "v_mfma_f32_16x16x32_bf16 v[192:195], a[156:159], v[72:75], v[192:195]\n"
      "v_mfma_f32_16x16x32_bf16 v[196:199], a[156:159], v[76:79], v[196:199]\n"
      "v_mfma_f32_16x16x32_bf16 v[200:203], a[156:159], a[40:43], v[200:203]\n"
      "v_mfma_f32_16x16x32_bf16 v[204:207], a[156:159], a[44:47], v[204:207]\n"
      "global_load_dwordx4 a[156:159], %0, s[20:21] offset:3072\n"
      "s_waitcnt lgkmcnt(0)\n"
      "s_barrier\n"
      "s_waitcnt vmcnt(23)\n"
      "v_mfma_f32_16x16x32_bf16 v[176:179], a[160:163], v[80:83], v[176:179]\n"
      "ds_read_b128 v[248:251], %6 offset:0\n"
      "ds_read_b128 v[252:255], %6 offset:1024\n"
      "v_mfma_f32_16x16x32_bf16 v[180:183], a[160:163], v[84:87], v[180:183]\n"
      "v_mfma_f32_16x16x32_bf16 v[184:187], a[160:163], a[48:51], v[184:187]\n"
      "v_mfma_f32_16x16x32_bf16 v[188:191], a[160:163], a[52:55], v[188:191]\n"
      "ds_read_b128 a[160:163], %22 offset:5120\n"
      "v_mfma_f32_16x16x32_bf16 v[192:195], a[164:167], v[80:83], v[192:195]\n"
      "s_waitcnt lgkmcnt(1)\n"
      "v_mfma_f32_16x16x32_bf16 v[196:199], a[164:167], v[84:87], v[196:199]\n"
      "s_cmp_eq_u32 s28, 0\n"
      "s_cbranch_scc1 .La5_%=\n"
      "global_store_dwordx4 %0, v[248:251], %12 offset:0 sc1\n"
      ".Lb5_%=:\n"
      "s_cmp_eq_u32 s28, 1\n"
      "s_cbranch_scc1 .La6_%=\n"
      "global_store_dwordx4 %0, v[252:255], %12 offset:1024 sc1\n"
      ".Lb6_%=:\n"
      "v_mfma_f32_16x16x32_bf16 v[200:203], a[164:167], a[48:51], v[200:203]\n"
      "v_mfma_f32_16x16x32_bf16 v[204:207], a[164:167], a[52:55], v[204:207]\n"
      "s_add_u32 s20, s20, 8192\n"
      "s_addc_u32 s21, s21, 0\n"
      "global_load_dwordx4 a[164:167], %0, s[20:21] offset:-3072\n"
      "s_waitcnt vmcnt(22)\n"
      "v_mfma_f32_16x16x32_bf16 v[176:179], a[168:171], v[88:91], v[176:179]\n"
      "v_mfma_f32_16x16x32_bf16 v[180:183], a[168:171], v[92:95], v[180:183]\n"
      "v_mfma_f32_16x16x32_bf16 v[184:187], a[168:171], a[56:59], v[184:187]\n"
      "v_mfma_f32_16x16x32_bf16 v[188:191], a[168:171], a[60:63], v[188:191]\n"
      "global_load_dwordx4 a[168:171], %0, s[20:21] offset:-2048\n"
      "v_mfma_f32_16x16x32_bf16 v[192:195], a[172:175], v[88:91], v[192:195]\n"
      "v_mfma_f32_16x16x32_bf16 v[196:199], a[172:175], v[92:95], v[196:199]\n"
      "v_mfma_f32_16x16x32_bf16 v[200:203], a[172:175], a[56:59], v[200:203]\n"
      "v_mfma_f32_16x16x32_bf16 v[204:207], a[172:175], a[60:63], v[204:207]\n"
      "global_load_dwordx4 a[172:175], %0, s[20:21] offset:-1024\n"
      "s_waitcnt vmcnt(23)\n"
      "v_mfma_f32_16x16x32_bf16 v[176:179], a[176:179], v[96:99], v[176:179]\n"
      "v_mfma_f32_16x16x32_bf16 v[180:183], a[176:179], v[100:103], v[180:183]\n"
      "v_mfma_f32_16x16x32_bf16 v[184:187], a[176:179], a[64:67], v[184:187]\n"
      "v_mfma_f32_16x16x32_bf16 v[188:191], a[176:179], a[68:71], v[188:191]\n"
      "global_load_dwordx4 a[176:179], %0, s[20:21] offset:0\n"
      "v_mfma_f32_16x16x32_bf16 v[192:195], a[180:183], v[96:99], v[192:195]\n"
      "v_mfma_f32_16x16x32_bf16 v[196:199], a[180:183], v[100:103], v[196:199]\n"
      "v_mfma_f32_16x16x32_bf16 v[200:203], a[180:183], a[64:67], v[200:203]\n"
      "v_mfma_f32_16x16x32_bf16 v[204:207], a[180:183], a[68:71], v[204:207]\n"
      "global_load_dwordx4 a[180:183], %0, s[20:21] offset:1024\n"
      "s_waitcnt vmcnt(23)\n"
      "v_mfma_f32_16x16x32_bf16 v[176:179], a[184:187], v[104:107], v[176:179]\n"
      "v_mfma_f32_16x16x32_bf16 v[180:183], a[184:187], v[108:111], v[180:183]\n"
      "v_mfma_f32_16x16x32_bf16 v[184:187], a[184:187], a[72:75], v[184:187]\n"
      "v_mfma_f32_16x16x32_bf16 v[188:191], a[184:187], a[76:79], v[188:191]\n"
      "global_load_dwordx4 a[184:187], %0, s[20:21] offset:2048\n"
      "v_mfma_f32_16x16x32_bf16 v[192:195], a[188:191], v[104:107], v[192:195]\n"
      "v_mfma_f32_16x16x32_bf16 v[196:199], a[188:191], v[108:111], v[196:199]\n"
      "v_mfma_f32_16x16x32_bf16 v[200:203], a[188:191], a[72:75], v[200:203]\n"
      "v_mfma_f32_16x16x32_bf16 v[204:207], a[188:191], a[76:79], v[204:207]\n"
      "global_load_dwordx4 a[188:191], %0, s[20:21] offset:3072\n"
      "s_waitcnt vmcnt(23)\n"
      "v_mfma_f32_16x16x32_bf16 v[176:179], a[192:195], v[112:115], v[176:179]\n"
      "v_mfma_f32_16x16x32_bf16 v[180:183], a[192:195], v[116:119], v[180:183]\n"
      "v_mfma_f32_16x16x32_bf16 v[184:187], a[192:195], a[80:83], v[184:187]\n"
      "v_mfma_f32_16x16x32_bf16 v[188:191], a[192:195], a[84:87], v[188:191]\n"
      "ds_read_b128 a[192:195], %22 offset:6144\n"
      "v_mfma_f32_16x16x32_bf16 v[192:195], a[196:199], v[112:115], v[192:195]\n"
      "v_mfma_f32_16x16x32_bf16 v[196:199], a[196:199], v[116:119], v[196:199]\n"
      "v_mfma_f32_16x16x32_bf16 v[200:203], a[196:199], a[80:83], v[200:203]\n"
      "v_mfma_f32_16x16x32_bf16 v[204:207], a[196:199], a[84:87], v[204:207]\n"
      "s_add_u32 s20, s20, 8192\n"
      "s_addc_u32 s21, s21, 0\n"
      "global_load_dwordx4 a[196:199], %0, s[20:21] offset:-3072\n"
      "s_waitcnt vmcnt(22)\n"
      "v_mfma_f32_16x16x32_bf16 v[176:179], a[200:203], v[120:123], v[176:179]\n"
      "v_mfma_f32_16x16x32_bf16 v[180:183], a[200:203], v[124:127], v[180:183]\n"
      "v_mfma_f32_16x16x32_bf16 v[184:187], a[200:203], a[88:91], v[184:187]\n"
      "v_mfma_f32_16x16x32_bf16 v[188:191], a[200:203], a[92:95], v[188:191]\n"
      "global_load_dwordx4 a[200:203], %0, s[20:21] offset:-2048\n"
      "v_mfma_f32_16x16x32_bf16 v[192:195], a[204:207], v[120:123], v[192:195]\n"
      "v_mfma_f32_16x16x32_bf16 v[196:199], a[204:207], v[124:127], v[196:199]\n"
      "v_mfma_f32_16x16x32_bf16 v[200:203], a[204:207], a[88:91], v[200:203]\n"
      "v_mfma_f32_16x16x32_bf16 v[204:207], a[204:207], a[92:95], v[204:207]\n"
      "global_load_dwordx4 a[204:207], %0, s[20:21] offset:-1024\n"
      "s_waitcnt vmcnt(23)\n"
      "v_mfma_f32_16x16x32_bf16 v[176:179], a[208:211], v[128:131], v[176:179]\n"
      "v_mfma_f32_16x16x32_bf16 v[180:183], a[208:211], v[132:135], v[180:183]\n"
      "v_mfma_f32_16x16x32_bf16 v[184:187], a[208:211], a[96:99], v[184:187]\n"
      "v_mfma_f32_16x16x32_bf16 v[188:191], a[208:211], a[100:103], v[188:191]\n"
      "global_load_dwordx4 a[208:211], %0, s[20:21] offset:0\n"
      "v_mfma_f32_16x16x32_bf16 v[192:195], a[212:215], v[128:131], v[192:195]\n"
      "v_mfma_f32_16x16x32_bf16 v[196:199], a[212:215], v[132:135], v[196:199]\n"
      "v_mfma_f32_16x16x32_bf16 v[200:203], a[212:215], a[96:99], v[200:203]\n"
      "v_mfma_f32_16x16x32_bf16 v[204:207], a[212:215], a[100:103], v[204:207]\n"
      "global_load_dwordx4 a[212:215], %0, s[20:21] offset:1024\n"
      "s_waitcnt vmcnt(23)\n"
      "v_mfma_f32_16x16x32_bf16 v[176:179], a[216:219], v[136:139], v[176:179]\n"
      "v_mfma_f32_16x16x32_bf16 v[180:183], a[216:219], v[140:143], v[180:183]\n"
      "v_mfma_f32_16x16x32_bf16 v[184:187], a[216:219], a[104:107], v[184:187]\n"
      "v_mfma_f32_16x16x32_bf16 v[188:191], a[216:219], a[108:111], v[188:191]\n"
      "global_load_dwordx4 a[216:219], %0, s[20:21] offset:2048\n"
      "v_mfma_f32_16x16x32_bf16 v[192:195], a[220:223], v[136:139], v[192:195]\n"
      "v_mfma_f32_16x16x32_bf16 v[196:199], a[220:223], v[140:143], v[196:199]\n"
      "v_mfma_f32_16x16x32_bf16 v[200:203], a[220:223], a[104:107], v[200:203]\n"
      "v_mfma_f32_16x16x32_bf16 v[204:207], a[220:223], a[108:111], v[204:207]\n"
      "global_load_dwordx4 a[220:223], %0, s[20:21] offset:3072\n"
      "s_waitcnt vmcnt(23)\n"
      "v_mfma_f32_16x16x32_bf16 v[176:179], a[224:227], v[144:147], v[176:179]\n"
      "v_mfma_f32_16x16x32_bf16 v[180:183], a[224:227], v[148:151], v[180:183]\n"
      "v_mfma_f32_16x16x32_bf16 v[184:187], a[224:227], a[112:115], v[184:187]\n"
      "v_mfma_f32_16x16x32_bf16 v[188:191], a[224:227], a[116:119], v[188:191]\n"
      "ds_read_b128 a[224:227], %22 offset:7168\n"
      "v_mfma_f32_16x16x32_bf16 v[192:195], a[228:231], v[144:147], v[192:195]\n"
      "v_mfma_f32_16x16x32_bf16 v[196:199], a[228:231], v[148:151], v[196:199]\n"
      "v_mfma_f32_16x16x32_bf16 v[200:203], a[228:231], a[112:115], v[200:203]\n"
      "v_mfma_f32_16x16x32_bf16 v[204:207], a[228:231], a[116:119], v[204:207]\n"
      "s_add_u32 s20, s20, 8192\n"
      "s_addc_u32 s21, s21, 0\n"
      "global_load_dwordx4 a[228:231], %0, s[20:21] offset:-3072\n"
      "s_waitcnt vmcnt(22)\n"
      "v_mfma_f32_16x16x32_bf16 v[176:179], a[232:235], v[152:155], v[176:179]\n"
      "v_mfma_f32_16x16x32_bf16 v[180:183], a[232:235], v[156:159], v[180:183]\n"
      "v_mfma_f32_16x16x32_bf16 v[184:187], a[232:235], a[120:123], v[184:187]\n"
      "v_mfma_f32_16x16x32_bf16 v[188:191], a[232:235], a[124:127], v[188:191]\n"
      "global_load_dwordx4 a[232:235], %0, s[20:21] offset:-2048\n"
      "v_mfma_f32_16x16x32_bf16 v[192:195], a[236:239], v[152:155], v[192:195]\n"
      "v_mfma_f32_16x16x32_bf16 v[196:199], a[236:239], v[156:159], v[196:199]\n"
      "v_mfma_f32_16x16x32_bf16 v[200:203], a[236:239], a[120:123], v[200:203]\n"
      "v_mfma_f32_16x16x32_bf16 v[204:207], a[236:239], a[124:127], v[204:207]\n"
      "global_load_dwordx4 a[236:239], %0, s[20:21] offset:-1024\n"
      "s_waitcnt vmcnt(23)\n"
      "v_mfma_f32_16x16x32_bf16 v[176:179], a[240:243], v[160:163], v[176:179]\n"
      "v_mfma_f32_16x16x32_bf16 v[180:183], a[240:243], v[164:167], v[180:183]\n"
      "v_mfma_f32_16x16x32_bf16 v[184:187], a[240:243], a[128:131], v[184:187]\n"
      "v_mfma_f32_16x16x32_bf16 v[188:191], a[240:243], a[132:135], v[188:191]\n"
      "global_load_dwordx4 a[240:243], %0, s[20:21] offset:0\n"
      "v_mfma_f32_16x16x32_bf16 v[192:195], a[244:247], v[160:163], v[192:195]\n"
      "v_mfma_f32_16x16x32_bf16 v[196:199], a[244:247], v[164:167], v[196:199]\n"
      "v_mfma_f32_16x16x32_bf16 v[200:203], a[244:247], a[128:131], v[200:203]\n"
      "v_mfma_f32_16x16x32_bf16 v[204:207], a[244:247], a[132:135], v[204:207]\n"
      "global_load_dwordx4 a[244:247], %0, s[20:21] offset:1024\n"
      "s_waitcnt vmcnt(23)\n"
      "v_mfma_f32_16x16x32_bf16 v[176:179], a[248:251], v[168:171], v[176:179]\n"
      "v_mfma_f32_16x16x32_bf16 v[180:183], a[248:251], v[172:175], v[180:183]\n"
      "v_mfma_f32_16x16x32_bf16 v[184:187], a[248:251], a[136:139], v[184:187]\n"
      "v_mfma_f32_16x16x32_bf16 v[188:191], a[248:251], a[140:143], v[188:191]\n"
      "global_load_dwordx4 a[248:251], %0, s[20:21] offset:2048\n"
      "v_mfma_f32_16x16x32_bf16 v[192:195], a[252:255], v[168:171], v[192:195]\n"
      "v_mfma_f32_16x16x32_bf16 v[196:199], a[252:255], v[172:175], v[196:199]\n"
      "v_mfma_f32_16x16x32_bf16 v[200:203], a[252:255], a[136:139], v[200:203]\n"
      "v_mfma_f32_16x16x32_bf16 v[204:207], a[252:255], a[140:143], v[204:207]\n"
      "global_load_dwordx4 a[252:255], %0, s[20:21] offset:3072\n"
      "s_nop 1\n"
      "s_waitcnt vmcnt(23)\n"
      "v_mfma_f32_16x16x32_bf16 v[208:211], a[144:147], v[32:35], 0\n"
      "v_mfma_f32_16x16x32_bf16 v[212:215], a[144:147], v[36:39], 0\n"
      "v_mfma_f32_16x16x32_bf16 v[216:219], a[144:147], a[0:3], 0\n"
      "v_mfma_f32_16x16x32_bf16 v[220:223], a[144:147], a[4:7], 0\n"
      "ds_read_b128 a[144:147], %22 offset:8192\n"
      "v_mfma_f32_16x16x32_bf16 v[224:227], a[148:151], v[32:35], 0\n"
      "v_mfma_f32_16x16x32_bf16 v[228:231], a[148:151], v[36:39], 0\n"
      "v_mul_f32 v176, v25, v176\n"
      "v_mfma_f32_16x16x32_bf16 v[232:235], a[148:151], a[0:3], 0\n"
      "v_mul_f32 v177, v25, v177\n"
      "v_mul_f32 v178, v25, v178\n"
      "v_mfma_f32_16x16x32_bf16 v[236:239], a[148:151], a[4:7], 0\n"
      "s_add_u32 s20, s20, 8192\n"
      "s_addc_u32 s21, s21, 0\n"
      "global_load_dwordx4 a[148:151], %0, s[20:21] offset:-3072\n"
      "v_mul_f32 v179, v25, v179\n"
      "s_waitcnt vmcnt(22)\n"
      "v_mfma_f32_16x16x32_bf16 v[208:211], a[152:155], v[40:43], v[208:211]\n"
      "v_mul_f32 v192, v25, v192\n"
      "v_mul_f32 v193, v25, v193\n"
      "v_mfma_f32_16x16x32_bf16 v[212:215], a[152:155], v[44:47], v[212:215]\n"
      "v_mul_f32 v194, v25, v194\n"
      "v_mul_f32 v195, v25, v195\n"
      "v_mfma_f32_16x16x32_bf16 v[216:219], a[152:155], a[8:11], v[216:219]\n"
      "v_cvt_pk_bf16_f32 v240, v176, v177\n"
      "v_mfma_f32_16x16x32_bf16 v[220:223], a[152:155], a[12:15], v[220:223]\n"
      "global_load_dwordx4 a[152:155], %0, s[20:21] offset:-2048\n"
      "v_cvt_pk_bf16_f32 v241, v178, v179\n"
      "v_mfma_f32_16x16x32_bf16 v[224:227], a[156:159], v[40:43], v[224:227]\n"
      "v_cvt_pk_bf16_f32 v242, v192, v193\n"
      "v_cvt_pk_bf16_f32 v243, v194, v195\n"
      "v_mfma_f32_16x16x32_bf16 v[228:231], a[156:159], v[44:47], v[228:231]\n"
      "s_cmp_eq_u32 s28, 0\n"
      "s_cbranch_scc1 .La7_%=\n"
      "global_store_dwordx4 %0, v[240:243], s[22:23] offset:0 sc1\n"
      ".Lb7_%=:\n"
      "v_mul_f32 v180, v27, v180\n"
      "v_mfma_f32_16x16x32_bf16 v[232:235], a[156:159], a[8:11], v[232:235]\n"
      "v_mul_f32 v181, v27, v181\n"
      "v_mul_f32 v182, v27, v182\n"
      "v_mfma_f32_16x16x32_bf16 v[236:239], a[156:159], a[12:15], v[236:239]\n"
      "global_load_dwordx4 a[156:159], %0, s[20:21] offset:-1024\n"
      "v_mul_f32 v183, v27, v183\n"
      "s_waitcnt vmcnt(23)\n"
      "s_waitcnt lgkmcnt(3)\n"
      "v_mfma_f32_16x16x32_bf16 v[208:211], a[160:163], v[48:51], v[208:211]\n"
      "v_mul_f32 v196, v27, v196\n"
      "v_mul_f32 v197, v27, v197\n"
      "v_mfma_f32_16x16x32_bf16 v[212:215], a[160:163], v[52:55], v[212:215]\n"
      "v_mul_f32 v198, v27, v198\n"
      "v_mfma_f32_16x16x32_bf16 v[216:219], a[160:163], a[16:19], v[216:219]\n"
      "v_mul_f32 v199, v27, v199\n"
      "v_cvt_pk_bf16_f32 v244, v180, v181\n"
      "v_mfma_f32_16x16x32_bf16 v[220:223], a[160:163], a[20:23], v[220:223]\n"
      "global_load_dwordx4 a[160:163], %0, s[20:21] offset:0\n"
      "v_cvt_pk_bf16_f32 v245, v182, v183\n"
      "v_mfma_f32_16x16x32_bf16 v[224:227], a[164:167], v[48:51], v[224:227]\n"
      "v_cvt_pk_bf16_f32 v246, v196, v197\n"
      "v_cvt_pk_bf16_f32 v247, v198, v199\n"
      "v_mfma_f32_16x16x32_bf16 v[228:231], a[164:167], v[52:55], v[228:231]\n"
      "s_cmp_eq_u32 s28, 0\n"
      "s_cbranch_scc1 .La8_%=\n"
      "global_store_dwordx4 %0, v[244:247], s[22:23] offset:1024 sc1\n"
      ".Lb8_%=:\n"
      "v_mul_f32 v184, v29, v184\n"
      "v_mfma_f32_16x16x32_bf16 v[232:235], a[164:167], a[16:19], v[232:235]\n"
      "v_mul_f32 v185, v29, v185\n"
      "v_mfma_f32_16x16x32_bf16 v[236:239], a[164:167], a[20:23], v[236:239]\n"
      "global_load_dwordx4 a[164:167], %0, s[20:21] offset:1024\n"
      "v_mul_f32 v186, v29, v186\n"
      "s_waitcnt vmcnt(23)\n"
      "v_mfma_f32_16x16x32_bf16 v[208:211], a[168:171], v[56:59], v[208:211]\n"
      "v_mul_f32 v187, v29, v187\n"
      "v_mul_f32 v200, v29, v200\n"
      "v_mfma_f32_16x16x32_bf16 v[212:215], a[168:171], v[60:63], v[212:215]\n"
      "v_mul_f32 v201, v29, v201\n"
      "v_mul_f32 v202, v29, v202\n"
      "v_mfma_f32_16x16x32_bf16 v[216:219], a[168:171], a[24:27], v[216:219]\n"
      "v_mul_f32 v203, v29, v203\n"
      "v_cvt_pk_bf16_f32 v248, v184, v185\n"
      "v_mfma_f32_16x16x32_bf16 v[220:223], a[168:171], a[28:31], v[220:223]\n"
      "global_load_dwordx4 a[168:171], %0, s[20:21] offset:2048\n"
      "v_cvt_pk_bf16_f32 v249, v186, v187\n"
      "v_mfma_f32_16x16x32_bf16 v[224:227], a[172:175], v[56:59], v[224:227]\n"
      "v_cvt_pk_bf16_f32 v250, v200, v201\n"
      "v_cvt_pk_bf16_f32 v251, v202, v203\n"
      "v_mfma_f32_16x16x32_bf16 v[228:231], a[172:175], v[60:63], v[228:231]\n"
      "s_cmp_eq_u32 s28, 1\n"
      "s_cbranch_scc1 .La9_%=\n"
      "global_store_dwordx4 %0, v[248:251], s[22:23] offset:2048 sc1\n"
      ".Lb9_%=:\n"
      "v_mul_f32 v188, v31, v188\n"
      "v_mfma_f32_16x16x32_bf16 v[232:235], a[172:175], a[24:27], v[232:235]\n"
      "v_mul_f32 v189, v31, v189\n"
      "v_mfma_f32_16x16x32_bf16 v[236:239], a[172:175], a[28:31], v[236:239]\n"
      "global_load_dwordx4 a[172:175], %0, s[20:21] offset:3072\n"
      "v_mul_f32 v190, v31, v190\n"
      "s_waitcnt vmcnt(23)\n"
      "v_mfma_f32_16x16x32_bf16 v[208:211], a[176:179], v[64:67], v[208:211]\n"
      "v_mul_f32 v191, v31, v191\n"
      "v_mul_f32 v204, v31, v204\n"
      "v_mfma_f32_16x16x32_bf16 v[212:215], a[176:179], v[68:71], v[212:215]\n"
      "v_mul_f32 v205, v31, v205\n"
      "v_mul_f32 v206, v31, v206\n"
      "v_mfma_f32_16x16x32_bf16 v[216:219], a[176:179], a[32:35], v[216:219]\n"
      "v_mul_f32 v207, v31, v207\n"
      "v_cvt_pk_bf16_f32 v252, v188, v189\n"
      "v_mfma_f32_16x16x32_bf16 v[220:223], a[176:179], a[36:39], v[220:223]\n"
      "ds_read_b128 a[176:179], %22 offset:9216\n"
      "v_cvt_pk_bf16_f32 v253, v190, v191\n"
      "v_mfma_f32_16x16x32_bf16 v[224:227], a[180:183], v[64:67], v[224:227]\n"
      "v_cvt_pk_bf16_f32 v254, v204, v205\n"
      "v_cvt_pk_bf16_f32 v255, v206, v207\n"
      "v_mfma_f32_16x16x32_bf16 v[228:231], a[180:183], v[68:71], v[228:231]\n"
      "s_cmp_eq_u32 s28, 1\n"
      "s_cbranch_scc1 .La10_%=\n"
      "global_store_dwordx4 %0, v[252:255], s[22:23] offset:3072 sc1\n"
      ".Lb10_%=:\n"
      "v_mfma_f32_16x16x32_bf16 v[232:235], a[180:183], a[32:35], v[232:235]\n"
      "s_add_u32 s22, s22, 4096\n"
      "s_addc_u32 s23, s23, 0\n"
      "v_mfma_f32_16x16x32_bf16 v[236:239], a[180:183], a[36:39], v[236:239]\n"
      "s_add_u32 s20, s20, 8192\n"
      "s_addc_u32 s21, s21, 0\n"
      "global_load_dwordx4 a[180:183], %0, s[20:21] offset:-3072\n"
      "s_waitcnt vmcnt(22)\n"
      "v_mfma_f32_16x16x32_bf16 v[208:211], a[184:187], v[72:75], v[208:211]\n"
      "v_mfma_f32_16x16x32_bf16 v[212:215], a[184:187], v[76:79], v[212:215]\n"
      "v_mfma_f32_16x16x32_bf16 v[216:219], a[184:187], a[40:43], v[216:219]\n"
      "v_mfma_f32_16x16x32_bf16 v[220:223], a[184:187], a[44:47], v[220:223]\n"
      "global_load_dwordx4 a[184:187], %0, s[20:21] offset:-2048\n"
      "v_mfma_f32_16x16x32_bf16 v[224:227], a[188:191], v[72:75], v[224:227]\n"
      "v_mfma_f32_16x16x32_bf16 v[228:231], a[188:191], v[76:79], v[228:231]\n"
      "v_mfma_f32_16x16x32_bf16 v[232:235], a[188:191], a[40:43], v[232:235]\n"
      "v_mfma_f32_16x16x32_bf16 v[236:239], a[188:191], a[44:47], v[236:239]\n"
      "global_load_dwordx4 a[188:191], %0, s[20:21] offset:-1024\n"
      "s_waitcnt vmcnt(23)\n"
      "s_waitcnt lgkmcnt(3)\n"
      "v_mfma_f32_16x16x32_bf16 v[208:211], a[192:195], v[80:83], v[208:211]\n"
      "v_mfma_f32_16x16x32_bf16 v[212:215], a[192:195], v[84:87], v[212:215]\n"
      "v_mfma_f32_16x16x32_bf16 v[216:219], a[192:195], a[48:51], v[216:219]\n"
      "v_mfma_f32_16x16x32_bf16 v[220:223], a[192:195], a[52:55], v[220:223]\n"
      "global_load_dwordx4 a[192:195], %0, s[20:21] offset:0\n"
      "v_mfma_f32_16x16x32_bf16 v[224:227], a[196:199], v[80:83], v[224:227]\n"
      "v_mfma_f32_16x16x32_bf16 v[228:231], a[196:199], v[84:87], v[228:231]\n"
      "v_mfma_f32_16x16x32_bf16 v[232:235], a[196:199], a[48:51], v[232:235]\n"
      "v_mfma_f32_16x16x32_bf16 v[236:239], a[196:199], a[52:55], v[236:239]\n"
      "global_load_dwordx4 a[196:199], %0, s[20:21] offset:1024\n"
      "s_waitcnt vmcnt(23)\n"
      "v_mfma_f32_16x16x32_bf16 v[208:211], a[200:203], v[88:91], v[208:211]\n"
      "v_mfma_f32_16x16x32_bf16 v[212:215], a[200:203], v[92:95], v[212:215]\n"
      "v_mfma_f32_16x16x32_bf16 v[216:219], a[200:203], a[56:59], v[216:219]\n"
      "v_mfma_f32_16x16x32_bf16 v[220:223], a[200:203], a[60:63], v[220:223]\n"
      "global_load_dwordx4 a[200:203], %0, s[20:21] offset:2048\n"
      "v_mfma_f32_16x16x32_bf16 v[224:227], a[204:207], v[88:91], v[224:227]\n"
      "v_mfma_f32_16x16x32_bf16 v[228:231], a[204:207], v[92:95], v[228:231]\n"
      "v_mfma_f32_16x16x32_bf16 v[232:235], a[204:207], a[56:59], v[232:235]\n"
      "v_mfma_f32_16x16x32_bf16 v[236:239], a[204:207], a[60:63], v[236:239]\n"
      "global_load_dwordx4 a[204:207], %0, s[20:21] offset:3072\n"
      "s_waitcnt vmcnt(23)\n"
      "v_mfma_f32_16x16x32_bf16 v[208:211], a[208:211], v[96:99], v[208:211]\n"
      "v_mfma_f32_16x16x32_bf16 v[212:215], a[208:211], v[100:103], v[212:215]\n"
      "v_mfma_f32_16x16x32_bf16 v[216:219], a[208:211], a[64:67], v[216:219]\n"
      "v_mfma_f32_16x16x32_bf16 v[220:223], a[208:211], a[68:71], v[220:223]\n"
      "ds_read_b128 a[208:211], %22 offset:10240\n"
      "v_mfma_f32_16x16x32_bf16 v[224:227], a[212:215], v[96:99], v[224:227]\n"
      "v_mfma_f32_16x16x32_bf16 v[228:231], a[212:215], v[100:103], v[228:231]\n"
      "v_mfma_f32_16x16x32_bf16 v[232:235], a[212:215], a[64:67], v[232:235]\n"
      "v_mfma_f32_16x16x32_bf16 v[236:239], a[212:215], a[68:71], v[236:239]\n"
      "s_add_u32 s20, s20, 8192\n"
      "s_addc_u32 s21, s21, 0\n"
      "global_load_dwordx4 a[212:215], %0, s[20:21] offset:-3072\n"
      "s_waitcnt vmcnt(22)\n"
      "v_mfma_f32_16x16x32_bf16 v[208:211], a[216:219], v[104:107], v[208:211]\n"
      "v_mfma_f32_16x16x32_bf16 v[212:215], a[216:219], v[108:111], v[212:215]\n"
      "v_mfma_f32_16x16x32_bf16 v[216:219], a[216:219], a[72:75], v[216:219]\n"
      "v_mfma_f32_16x16x32_bf16 v[220:223], a[216:219], a[76:79], v[220:223]\n"
      "global_load_dwordx4 a[216:219], %0, s[20:21] offset:-2048\n"
      "v_mfma_f32_16x16x32_bf16 v[224:227], a[220:223], v[104:107], v[224:227]\n"
      "v_mfma_f32_16x16x32_bf16 v[228:231], a[220:223], v[108:111], v[228:231]\n"
      "v_mfma_f32_16x16x32_bf16 v[232:235], a[220:223], a[72:75], v[232:235]\n"
      "v_mfma_f32_16x16x32_bf16 v[236:239], a[220:223], a[76:79], v[236:239]\n"
      "global_load_dwordx4 a[220:223], %0, s[20:21] offset:-1024\n"
      "s_waitcnt vmcnt(10)\n"
      "s_barrier\n"
      "s_cmp_eq_u32 %14, 3\n"
      "s_cbranch_scc1 .Lsao%=\n"
      ".Lsar%=:\n"
      "s_waitcnt vmcnt(23)\n"
      "s_waitcnt lgkmcnt(3)\n"
      "v_mfma_f32_16x16x32_bf16 v[208:211], a[224:227], v[112:115], v[208:211]\n"
      "v_mfma_f32_16x16x32_bf16 v[212:215], a[224:227], v[116:119], v[212:215]\n"
      "v_mfma_f32_16x16x32_bf16 v[216:219], a[224:227], a[80:83], v[216:219]\n"
      "v_mfma_f32_16x16x32_bf16 v[220:223], a[224:227], a[84:87], v[220:223]\n"
      "global_load_dwordx4 a[224:227], %0, s[20:21] offset:0\n"
      "v_mfma_f32_16x16x32_bf16 v[224:227], a[228:231], v[112:115], v[224:227]\n"
      "v_mfma_f32_16x16x32_bf16 v[228:231], a[228:231], v[116:119], v[228:231]\n"
      "v_mfma_f32_16x16x32_bf16 v[232:235], a[228:231], a[80:83], v[232:235]\n"
      "v_mfma_f32_16x16x32_bf16 v[236:239], a[228:231], a[84:87], v[236:239]\n"
      "global_load_dwordx4 a[228:231], %0, s[20:21] offset:1024\n"
      "s_waitcnt vmcnt(23)\n"
      "v_mfma_f32_16x16x32_bf16 v[208:211], a[232:235], v[120:123], v[208:211]\n"
      "v_mfma_f32_16x16x32_bf16 v[212:215], a[232:235], v[124:127], v[212:215]\n"
      "v_mfma_f32_16x16x32_bf16 v[216:219], a[232:235], a[88:91], v[216:219]\n"
      "v_mfma_f32_16x16x32_bf16 v[220:223], a[232:235], a[92:95], v[220:223]\n"
      "global_load_dwordx4 a[232:235], %0, s[20:21] offset:2048\n"
      "v_mfma_f32_16x16x32_bf16 v[224:227], a[236:239], v[120:123], v[224:227]\n"
      "v_mfma_f32_16x16x32_bf16 v[228:231], a[236:239], v[124:127], v[228:231]\n"
      "v_mfma_f32_16x16x32_bf16 v[232:235], a[236:239], a[88:91], v[232:235]\n"
      "v_mfma_f32_16x16x32_bf16 v[236:239], a[236:239], a[92:95], v[236:239]\n"
      "global_load_dwordx4 a[236:239], %0, s[20:21] offset:3072\n"
      "s_waitcnt vmcnt(23)\n"
      "v_mfma_f32_16x16x32_bf16 v[208:211], a[240:243], v[128:131], v[208:211]\n"
      "v_mfma_f32_16x16x32_bf16 v[212:215], a[240:243], v[132:135], v[212:215]\n"
      "v_mfma_f32_16x16x32_bf16 v[216:219], a[240:243], a[96:99], v[216:219]\n"
      "v_mfma_f32_16x16x32_bf16 v[220:223], a[240:243], a[100:103], v[220:223]\n"
      "ds_read_b128 a[240:243], %22 offset:11264\n"
      "v_mfma_f32_16x16x32_bf16 v[224:227], a[244:247], v[128:131], v[224:227]\n"
      "v_mfma_f32_16x16x32_bf16 v[228:231], a[244:247], v[132:135], v[228:231]\n"
      "v_mfma_f32_16x16x32_bf16 v[232:235], a[244:247], a[96:99], v[232:235]\n"
      "v_mfma_f32_16x16x32_bf16 v[236:239], a[244:247], a[100:103], v[236:239]\n"
      "s_add_u32 s20, s20, 8192\n"
      "s_addc_u32 s21, s21, 0\n"
      "global_load_dwordx4 a[244:247], %0, s[20:21] offset:-3072\n"
      "s_waitcnt vmcnt(22)\n"
      "v_mfma_f32_16x16x32_bf16 v[208:211], a[248:251], v[136:139], v[208:211]\n"
      "v_mfma_f32_16x16x32_bf16 v[212:215], a[248:251], v[140:143], v[212:215]\n"
      "v_mfma_f32_16x16x32_bf16 v[216:219], a[248:251], a[104:107], v[216:219]\n"
      "v_mfma_f32_16x16x32_bf16 v[220:223], a[248:251], a[108:111], v[220:223]\n"
      "global_load_dwordx4 a[248:251], %0, s[20:21] offset:-2048\n"
      "v_mfma_f32_16x16x32_bf16 v[224:227], a[252:255], v[136:139], v[224:227]\n"
      "v_mfma_f32_16x16x32_bf16 v[228:231], a[252:255], v[140:143], v[228:231]\n"
      "v_mfma_f32_16x16x32_bf16 v[232:235], a[252:255], a[104:107], v[232:235]\n"
      "v_mfma_f32_16x16x32_bf16 v[236:239], a[252:255], a[108:111], v[236:239]\n"
      "global_load_dwordx4 a[252:255], %0, s[20:21] offset:-1024\n"
      "s_waitcnt vmcnt(23)\n"
      "s_waitcnt lgkmcnt(3)\n"
      "v_mfma_f32_16x16x32_bf16 v[208:211], a[144:147], v[144:147], v[208:211]\n"
      "v_mfma_f32_16x16x32_bf16 v[212:215], a[144:147], v[148:151], v[212:215]\n"
      "v_mfma_f32_16x16x32_bf16 v[216:219], a[144:147], a[112:115], v[216:219]\n"
      "v_mfma_f32_16x16x32_bf16 v[220:223], a[144:147], a[116:119], v[220:223]\n"
      "global_load_dwordx4 a[144:147], %0, s[20:21] offset:0\n"
      "v_mfma_f32_16x16x32_bf16 v[224:227], a[148:151], v[144:147], v[224:227]\n"
      "v_mfma_f32_16x16x32_bf16 v[228:231], a[148:151], v[148:151], v[228:231]\n"
      "v_mfma_f32_16x16x32_bf16 v[232:235], a[148:151], a[112:115], v[232:235]\n"
      "v_mfma_f32_16x16x32_bf16 v[236:239], a[148:151], a[116:119], v[236:239]\n"
      "global_load_dwordx4 a[148:151], %0, s[20:21] offset:1024\n"
      "s_waitcnt vmcnt(23)\n"
      "v_mfma_f32_16x16x32_bf16 v[208:211], a[152:155], v[152:155], v[208:211]\n"
      "v_mfma_f32_16x16x32_bf16 v[212:215], a[152:155], v[156:159], v[212:215]\n"
      "v_mfma_f32_16x16x32_bf16 v[216:219], a[152:155], a[120:123], v[216:219]\n"
      "v_mfma_f32_16x16x32_bf16 v[220:223], a[152:155], a[124:127], v[220:223]\n"
      "global_load_dwordx4 a[152:155], %0, s[20:21] offset:2048\n"
      "v_mfma_f32_16x16x32_bf16 v[224:227], a[156:159], v[152:155], v[224:227]\n"
      "v_mfma_f32_16x16x32_bf16 v[228:231], a[156:159], v[156:159], v[228:231]\n"
      "v_mfma_f32_16x16x32_bf16 v[232:235], a[156:159], a[120:123], v[232:235]\n"
      "v_mfma_f32_16x16x32_bf16 v[236:239], a[156:159], a[124:127], v[236:239]\n"
      "global_load_dwordx4 a[156:159], %0, s[20:21] offset:3072\n"
      "s_waitcnt vmcnt(23)\n"
      "v_mfma_f32_16x16x32_bf16 v[208:211], a[160:163], v[160:163], v[208:211]\n"
      "v_mfma_f32_16x16x32_bf16 v[212:215], a[160:163], v[164:167], v[212:215]\n"
      "v_mfma_f32_16x16x32_bf16 v[216:219], a[160:163], a[128:131], v[216:219]\n"
      "v_mfma_f32_16x16x32_bf16 v[220:223], a[160:163], a[132:135], v[220:223]\n"
      "s_add_u32 s20, s20, 8192\n"
      "s_addc_u32 s21, s21, 0\n"
      "global_load_dwordx4 a[160:163], %0, s[20:21] offset:-4096\n"
      "v_mfma_f32_16x16x32_bf16 v[224:227], a[164:167], v[160:163], v[224:227]\n"
      "v_mfma_f32_16x16x32_bf16 v[228:231], a[164:167], v[164:167], v[228:231]\n"
      "v_mfma_f32_16x16x32_bf16 v[232:235], a[164:167], a[128:131], v[232:235]\n"
      "v_mfma_f32_16x16x32_bf16 v[236:239], a[164:167], a[132:135], v[236:239]\n"
      "global_load_dwordx4 a[164:167], %0, s[20:21] offset:-3072\n"
      "s_waitcnt vmcnt(23)\n"
      "v_mfma_f32_16x16x32_bf16 v[208:211], a[168:171], v[168:171], v[208:211]\n"
      "v_mfma_f32_16x16x32_bf16 v[212:215], a[168:171], v[172:175], v[212:215]\n"
      "v_mfma_f32_16x16x32_bf16 v[216:219], a[168:171], a[136:139], v[216:219]\n"
      "v_mfma_f32_16x16x32_bf16 v[220:223], a[168:171], a[140:143], v[220:223]\n"
      "global_load_dwordx4 a[168:171], %0, s[20:21] offset:-2048\n"
      "v_mfma_f32_16x16x32_bf16 v[224:227], a[172:175], v[168:171], v[224:227]\n"
      "v_mfma_f32_16x16x32_bf16 v[228:231], a[172:175], v[172:175], v[228:231]\n"
      "v_mfma_f32_16x16x32_bf16 v[232:235], a[172:175], a[136:139], v[232:235]\n"
      "v_mfma_f32_16x16x32_bf16 v[236:239], a[172:175], a[140:143], v[236:239]\n"
      "global_load_dwordx4 a[172:175], %0, s[20:21] offset:-1024\n"
      "s_nop 1\n"
      "s_waitcnt vmcnt(24)\n"
      "s_waitcnt lgkmcnt(2)\n"
      "v_mfma_f32_16x16x32_bf16 v[176:179], a[176:179], v[32:35], 0\n"
      "v_mfma_f32_16x16x32_bf16 v[180:183], a[176:179], v[36:39], 0\n"
      "v_mfma_f32_16x16x32_bf16 v[184:187], a[176:179], a[0:3], 0\n"
      "v_mfma_f32_16x16x32_bf16 v[188:191], a[176:179], a[4:7], 0\n"
      "global_load_dwordx4 a[176:179], %0, s[20:21] offset:0\n"
      "v_mfma_f32_16x16x32_bf16 v[192:195], a[180:183], v[32:35], 0\n"
      "v_mfma_f32_16x16x32_bf16 v[196:199], a[180:183], v[36:39], 0\n"
      "v_mul_f32 v208, v25, v208\n"
      "v_mfma_f32_16x16x32_bf16 v[200:203], a[180:183], a[0:3], 0\n"
      "v_mul_f32 v209, v25, v209\n"
      "v_mul_f32 v210, v25, v210\n"
      "v_mfma_f32_16x16x32_bf16 v[204:207], a[180:183], a[4:7], 0\n"
      "global_load_dwordx4 a[180:183], %0, s[20:21] offset:1024\n"
      "v_mul_f32 v211, v25, v211\n"
      "s_waitcnt vmcnt(24)\n"
      "v_mfma_f32_16x16x32_bf16 v[176:179], a[184:187], v[40:43], v[176:179]\n"
      "v_mul_f32 v224, v25, v224\n"
      "v_mul_f32 v225, v25, v225\n"
      "v_mfma_f32_16x16x32_bf16 v[180:183], a[184:187], v[44:47], v[180:183]\n"
      "v_mul_f32 v226, v25, v226\n"
      "v_mul_f32 v227, v25, v227\n"
      "v_mfma_f32_16x16x32_bf16 v[184:187], a[184:187], a[8:11], v[184:187]\n"
      "v_cvt_pk_bf16_f32 v240, v208, v209\n"
      "v_mfma_f32_16x16x32_bf16 v[188:191], a[184:187], a[12:15], v[188:191]\n"
      "global_load_dwordx4 a[184:187], %0, s[20:21] offset:2048\n"
      "v_cvt_pk_bf16_f32 v241, v210, v211\n"
      "v_mfma_f32_16x16x32_bf16 v[192:195], a[188:191], v[40:43], v[192:195]\n"
      "v_cvt_pk_bf16_f32 v242, v224, v225\n"
      "v_cvt_pk_bf16_f32 v243, v226, v227\n"
      "v_mfma_f32_16x16x32_bf16 v[196:199], a[188:191], v[44:47], v[196:199]\n"
      "s_cmp_eq_u32 s28, 0\n"
      "s_cbranch_scc1 .La11_%=\n"
      "global_store_dwordx4 %0, v[240:243], s[22:23] offset:0 sc1\n"
      ".Lb11_%=:\n"
      "v_mul_f32 v212, v27, v212\n"
      "v_mfma_f32_16x16x32_bf16 v[200:203], a[188:191], a[8:11], v[200:203]\n"
      "v_mul_f32 v213, v27, v213\n"
      "v_mul_f32 v214, v27, v214\n"
      "v_mfma_f32_16x16x32_bf16 v[204:207], a[188:191], a[12:15], v[204:207]\n"
      "global_load_dwordx4 a[188:191], %0, s[20:21] offset:3072\n"
      "v_mul_f32 v215, v27, v215\n"
      "s_cmp_eq_u32 %14, 3\n"
      "s_cbranch_scc1 .Lpio%=\n"
      ".Lpir%=:\n"
      "s_waitcnt vmcnt(24)\n"
      "v_mfma_f32_16x16x32_bf16 v[176:179], a[192:195], v[48:51], v[176:179]\n"
      "v_mul_f32 v228, v27, v228\n"
      "v_mul_f32 v229, v27, v229\n"
      "v_mfma_f32_16x16x32_bf16 v[180:183], a[192:195], v[52:55], v[180:183]\n"
      "v_mul_f32 v230, v27, v230\n"
      "v_mfma_f32_16x16x32_bf16 v[184:187], a[192:195], a[16:19], v[184:187]\n"
      "v_mul_f32 v231, v27, v231\n"
      "v_cvt_pk_bf16_f32 v244, v212, v213\n"
      "v_mfma_f32_16x16x32_bf16 v[188:191], a[192:195], a[20:23], v[188:191]\n"
      "s_add_u32 s20, s20, 8192\n"
      "s_addc_u32 s21, s21, 0\n"
      "global_load_dwordx4 a[192:195], %0, s[20:21] offset:-4096\n"
      "v_cvt_pk_bf16_f32 v245, v214, v215\n"
      "v_mfma_f32_16x16x32_bf16 v[192:195], a[196:199], v[48:51], v[192:195]\n"
      "v_cvt_pk_bf16_f32 v246, v228, v229\n"
      "v_cvt_pk_bf16_f32 v247, v230, v231\n"
      "v_mfma_f32_16x16x32_bf16 v[196:199], a[196:199], v[52:55], v[196:199]\n"
      "s_cmp_eq_u32 s28, 0\n"
      "s_cbranch_scc1 .La12_%=\n"
      "global_store_dwordx4 %0, v[244:247], s[22:23] offset:1024 sc1\n"
      ".Lb12_%=:\n"
      "v_mul_f32 v216, v29, v216\n"
      "v_mfma_f32_16x16x32_bf16 v[200:203], a[196:199], a[16:19], v[200:203]\n"
      "v_mul_f32 v217, v29, v217\n"
      "v_mfma_f32_16x16x32_bf16 v[204:207], a[196:199], a[20:23], v[204:207]\n"
      "global_load_dwordx4 a[196:199], %0, s[20:21] offset:-3072\n"
      "v_mul_f32 v218, v29, v218\n"
      "s_waitcnt vmcnt(24)\n"
      "v_mfma_f32_16x16x32_bf16 v[176:179], a[200:203], v[56:59], v[176:179]\n"
      "v_mul_f32 v219, v29, v219\n"
      "v_mul_f32 v232, v29, v232\n"
      "v_mfma_f32_16x16x32_bf16 v[180:183], a[200:203], v[60:63], v[180:183]\n"
      "v_mul_f32 v233, v29, v233\n"
      "v_mul_f32 v234, v29, v234\n"
      "v_mfma_f32_16x16x32_bf16 v[184:187], a[200:203], a[24:27], v[184:187]\n"
      "v_mul_f32 v235, v29, v235\n"
      "v_cvt_pk_bf16_f32 v248, v216, v217\n"
      "v_mfma_f32_16x16x32_bf16 v[188:191], a[200:203], a[28:31], v[188:191]\n"
      "global_load_dwordx4 a[200:203], %0, s[20:21] offset:-2048\n"
      "v_cvt_pk_bf16_f32 v249, v218, v219\n"
      "v_mfma_f32_16x16x32_bf16 v[192:195], a[204:207], v[56:59], v[192:195]\n"
      "v_cvt_pk_bf16_f32 v250, v232, v233\n"
      "v_cvt_pk_bf16_f32 v251, v234, v235\n"
      "v_mfma_f32_16x16x32_bf16 v[196:199], a[204:207], v[60:63], v[196:199]\n"
      "s_cmp_eq_u32 s28, 1\n"
      "s_cbranch_scc1 .La13_%=\n"
      "global_store_dwordx4 %0, v[248:251], s[22:23] offset:2048 sc1\n"
      ".Lb13_%=:\n"
      "v_mul_f32 v220, v31, v220\n"
      "v_mfma_f32_16x16x32_bf16 v[200:203], a[204:207], a[24:27], v[200:203]\n"
      "v_mul_f32 v221, v31, v221\n"
      "v_mfma_f32_16x16x32_bf16 v[204:207], a[204:207], a[28:31], v[204:207]\n"
      "global_load_dwordx4 a[204:207], %0, s[20:21] offset:-1024\n"
      "v_mul_f32 v222, v31, v222\n"
      "s_waitcnt vmcnt(25)\n"
      "s_waitcnt lgkmcnt(1)\n"
      "v_mfma_f32_16x16x32_bf16 v[176:179], a[208:211], v[64:67], v[176:179]\n"
      "v_mul_f32 v223, v31, v223\n"
      "v_mul_f32 v236, v31, v236\n"
      "v_mfma_f32_16x16x32_bf16 v[180:183], a[208:211], v[68:71], v[180:183]\n"
      "v_mul_f32 v237, v31, v237\n"
      "v_mul_f32 v238, v31, v238\n"
      "v_mfma_f32_16x16x32_bf16 v[184:187], a[208:211], a[32:35], v[184:187]\n"
      "v_mul_f32 v239, v31, v239\n"
      "v_cvt_pk_bf16_f32 v252, v220, v221\n"
      "v_mfma_f32_16x16x32_bf16 v[188:191], a[208:211], a[36:39], v[188:191]\n"
      "v_cvt_pk_bf16_f32 v253, v222, v223\n"
      "v_mfma_f32_16x16x32_bf16 v[192:195], a[212:215], v[64:67], v[192:195]\n"
      "v_cvt_pk_bf16_f32 v254, v236, v237\n"
      "v_cvt_pk_bf16_f32 v255, v238, v239\n"
      "v_mfma_f32_16x16x32_bf16 v[196:199], a[212:215], v[68:71], v[196:199]\n"
      "s_cmp_eq_u32 s28, 1\n"
      "s_cbranch_scc1 .La14_%=\n"
      "global_store_dwordx4 %0, v[252:255], s[22:23] offset:3072 sc1\n"
      ".Lb14_%=:\n"
      "v_mfma_f32_16x16x32_bf16 v[200:203], a[212:215], a[32:35], v[200:203]\n"
      "s_add_u32 s22, s22, 4096\n"
      "s_addc_u32 s23, s23, 0\n"
      "v_mfma_f32_16x16x32_bf16 v[204:207], a[212:215], a[36:39], v[204:207]\n"
      "s_waitcnt vmcnt(23)\n"
      "v_mfma_f32_16x16x32_bf16 v[176:179], a[216:219], v[72:75], v[176:179]\n"
      "v_mfma_f32_16x16x32_bf16 v[180:183], a[216:219], v[76:79], v[180:183]\n"
      "v_mfma_f32_16x16x32_bf16 v[184:187], a[216:219], a[40:43], v[184:187]\n"
      "v_mfma_f32_16x16x32_bf16 v[188:191], a[216:219], a[44:47], v[188:191]\n"
      "v_mfma_f32_16x16x32_bf16 v[192:195], a[220:223], v[72:75], v[192:195]\n"
      "v_mfma_f32_16x16x32_bf16 v[196:199], a[220:223], v[76:79], v[196:199]\n"
      "v_mfma_f32_16x16x32_bf16 v[200:203], a[220:223], a[40:43], v[200:203]\n"
      "v_mfma_f32_16x16x32_bf16 v[204:207], a[220:223], a[44:47], v[204:207]\n"
      "s_waitcnt vmcnt(21)\n"
      "v_mfma_f32_16x16x32_bf16 v[176:179], a[224:227], v[80:83], v[176:179]\n"
      "v_mfma_f32_16x16x32_bf16 v[180:183], a[224:227], v[84:87], v[180:183]\n"
      "v_mfma_f32_16x16x32_bf16 v[184:187], a[224:227], a[48:51], v[184:187]\n"
      "v_mfma_f32_16x16x32_bf16 v[188:191], a[224:227], a[52:55], v[188:191]\n"
      "v_mfma_f32_16x16x32_bf16 v[192:195], a[228:231], v[80:83], v[192:195]\n"
      "v_mfma_f32_16x16x32_bf16 v[196:199], a[228:231], v[84:87], v[196:199]\n"
      "v_mfma_f32_16x16x32_bf16 v[200:203], a[228:231], a[48:51], v[200:203]\n"
      "v_mfma_f32_16x16x32_bf16 v[204:207], a[228:231], a[52:55], v[204:207]\n"
      "s_cmp_eq_u32 %14, 3\n"
      "s_cbranch_scc1 .Lpco%=\n"
      ".Lpcr%=:\n"
      "s_barrier\n"
      "s_waitcnt vmcnt(19)\n"
      "v_mfma_f32_16x16x32_bf16 v[176:179], a[232:235], v[88:91], v[176:179]\n"
      "s_lshl_b32 s25, %16, 10\n"
      "v_add_u32 v34, s25, %0\n"
      "s_mov_b32 s30, %20\n"
      "s_add_u32 s30, s30, 1\n"
      "s_and_b32 s30, s30, 7\n"
      "s_mul_i32 s25, s30, 2359296\n"
      "s_mov_b64 s[26:27], %15\n"
      "s_add_u32 s26, s26, s25\n"
      "s_addc_u32 s27, s27, 0\n"
      "global_load_dwordx4 v[40:43], v34, s[26:27] sc1\n"
      "v_mfma_f32_16x16x32_bf16 v[180:183], a[232:235], v[92:95], v[180:183]\n"
      "s_add_u32 s30, s30, 1\n"
      "s_and_b32 s30, s30, 7\n"
      "s_mul_i32 s25, s30, 2359296\n"
      "s_mov_b64 s[26:27], %15\n"
      "s_add_u32 s26, s26, s25\n"
      "s_addc_u32 s27, s27, 0\n"
      "global_load_dwordx4 v[44:47], v34, s[26:27] sc1\n"
      "s_add_u32 s30, s30, 1\n"
      "s_and_b32 s30, s30, 7\n"
      "s_mul_i32 s25, s30, 2359296\n"
      "s_mov_b64 s[26:27], %15\n"
      "s_add_u32 s26, s26, s25\n"
      "s_addc_u32 s27, s27, 0\n"
      "global_load_dwordx4 v[48:51], v34, s[26:27] sc1\n"
      "v_mfma_f32_16x16x32_bf16 v[184:187], a[232:235], a[56:59], v[184:187]\n"
      "s_add_u32 s30, s30, 1\n"
      "s_and_b32 s30, s30, 7\n"
      "s_mul_i32 s25, s30, 2359296\n"
      "s_mov_b64 s[26:27], %15\n"
      "s_add_u32 s26, s26, s25\n"
      "s_addc_u32 s27, s27, 0\n"
      "global_load_dwordx4 v[52:55], v34, s[26:27] sc1\n"
      "s_add_u32 s30, s30, 1\n"
      "s_and_b32 s30, s30, 7\n"
      "s_mul_i32 s25, s30, 2359296\n"
      "s_mov_b64 s[26:27], %15\n"
      "s_add_u32 s26, s26, s25\n"
      "s_addc_u32 s27, s27, 0\n"
      "global_load_dwordx4 v[56:59], v34, s[26:27] sc1\n"
      "v_mfma_f32_16x16x32_bf16 v[188:191], a[232:235], a[60:63], v[188:191]\n"
      "s_add_u32 s30, s30, 1\n"
      "s_and_b32 s30, s30, 7\n"
      "s_mul_i32 s25, s30, 2359296\n"
      "s_mov_b64 s[26:27], %15\n"
      "s_add_u32 s26, s26, s25\n"
      "s_addc_u32 s27, s27, 0\n"
      "global_load_dwordx4 v[60:63], v34, s[26:27] sc1\n"
      "v_mfma_f32_16x16x32_bf16 v[192:195], a[236:239], v[88:91], v[192:195]\n"
      "s_add_u32 s30, s30, 1\n"
      "s_and_b32 s30, s30, 7\n"
      "s_mul_i32 s25, s30, 2359296\n"
      "s_mov_b64 s[26:27], %15\n"
      "s_add_u32 s26, s26, s25\n"
      "s_addc_u32 s27, s27, 0\n"
      "global_load_dwordx4 v[64:67], v34, s[26:27] sc1\n"
      "v_mfma_f32_16x16x32_bf16 v[196:199], a[236:239], v[92:95], v[196:199]\n"
      "v_mfma_f32_16x16x32_bf16 v[200:203], a[236:239], a[56:59], v[200:203]\n"
      "v_mfma_f32_16x16x32_bf16 v[204:207], a[236:239], a[60:63], v[204:207]\n"
      "s_waitcnt vmcnt(25)\n"
      "s_waitcnt lgkmcnt(0)\n"
      "v_mfma_f32_16x16x32_bf16 v[176:179], a[240:243], v[96:99], v[176:179]\n"
      "v_mfma_f32_16x16x32_bf16 v[180:183], a[240:243], v[100:103], v[180:183]\n"
      "v_mfma_f32_16x16x32_bf16 v[184:187], a[240:243], a[64:67], v[184:187]\n"
      "v_mfma_f32_16x16x32_bf16 v[188:191], a[240:243], a[68:71], v[188:191]\n"
      "v_mfma_f32_16x16x32_bf16 v[192:195], a[244:247], v[96:99], v[192:195]\n"
      "v_mfma_f32_16x16x32_bf16 v[196:199], a[244:247], v[100:103], v[196:199]\n"
      "v_mfma_f32_16x16x32_bf16 v[200:203], a[244:247], a[64:67], v[200:203]\n"
      "v_mfma_f32_16x16x32_bf16 v[204:207], a[244:247], a[68:71], v[204:207]\n"
      "s_waitcnt vmcnt(23)\n"
      "v_mfma_f32_16x16x32_bf16 v[176:179], a[248:251], v[104:107], v[176:179]\n"
      "v_mfma_f32_16x16x32_bf16 v[180:183], a[248:251], v[108:111], v[180:183]\n"
      "v_mfma_f32_16x16x32_bf16 v[184:187], a[248:251], a[72:75], v[184:187]\n"
      "v_mfma_f32_16x16x32_bf16 v[188:191], a[248:251], a[76:79], v[188:191]\n"
      "v_mfma_f32_16x16x32_bf16 v[192:195], a[252:255], v[104:107], v[192:195]\n"
      "v_mfma_f32_16x16x32_bf16 v[196:199], a[252:255], v[108:111], v[196:199]\n"
      "v_mfma_f32_16x16x32_bf16 v[200:203], a[252:255], a[72:75], v[200:203]\n"
      "v_mfma_f32_16x16x32_bf16 v[204:207], a[252:255], a[76:79], v[204:207]\n"
      "s_waitcnt vmcnt(21)\n"
      "v_mfma_f32_16x16x32_bf16 v[176:179], a[144:147], v[112:115], v[176:179]\n"
      "v_mfma_f32_16x16x32_bf16 v[180:183], a[144:147], v[116:119], v[180:183]\n"
      "v_mfma_f32_16x16x32_bf16 v[184:187], a[144:147], a[80:83], v[184:187]\n"
      "v_mfma_f32_16x16x32_bf16 v[188:191], a[144:147], a[84:87], v[188:191]\n"
      "v_mfma_f32_16x16x32_bf16 v[192:195], a[148:151], v[112:115], v[192:195]\n"
      "v_mfma_f32_16x16x32_bf16 v[196:199], a[148:151], v[116:119], v[196:199]\n"
      "v_mfma_f32_16x16x32_bf16 v[200:203], a[148:151], a[80:83], v[200:203]\n"
      "v_mfma_f32_16x16x32_bf16 v[204:207], a[148:151], a[84:87], v[204:207]\n"
      "s_waitcnt vmcnt(19)\n"
      "v_mfma_f32_16x16x32_bf16 v[176:179], a[152:155], v[120:123], v[176:179]\n"
      "v_mfma_f32_16x16x32_bf16 v[180:183], a[152:155], v[124:127], v[180:183]\n"
      "v_mfma_f32_16x16x32_bf16 v[184:187], a[152:155], a[88:91], v[184:187]\n"
      "v_mfma_f32_16x16x32_bf16 v[188:191], a[152:155], a[92:95], v[188:191]\n"
      "v_mfma_f32_16x16x32_bf16 v[192:195], a[156:159], v[120:123], v[192:195]\n"
      "v_mfma_f32_16x16x32_bf16 v[196:199], a[156:159], v[124:127], v[196:199]\n"
      "v_mfma_f32_16x16x32_bf16 v[200:203], a[156:159], a[88:91], v[200:203]\n"
      "v_mfma_f32_16x16x32_bf16 v[204:207], a[156:159], a[92:95], v[204:207]\n"
      "s_waitcnt vmcnt(17)\n"
      "v_mfma_f32_16x16x32_bf16 v[176:179], a[160:163], v[128:131], v[176:179]\n"
      "v_mfma_f32_16x16x32_bf16 v[180:183], a[160:163], v[132:135], v[180:183]\n"
      "v_mfma_f32_16x16x32_bf16 v[184:187], a[160:163], a[96:99], v[184:187]\n"
      "v_mfma_f32_16x16x32_bf16 v[188:191], a[160:163], a[100:103], v[188:191]\n"
      "v_mfma_f32_16x16x32_bf16 v[192:195], a[164:167], v[128:131], v[192:195]\n"
      "v_mfma_f32_16x16x32_bf16 v[196:199], a[164:167], v[132:135], v[196:199]\n"
      "v_mfma_f32_16x16x32_bf16 v[200:203], a[164:167], a[96:99], v[200:203]\n"
      "v_mfma_f32_16x16x32_bf16 v[204:207], a[164:167], a[100:103], v[204:207]\n"
      "s_waitcnt vmcnt(15)\n"
      "v_mfma_f32_16x16x32_bf16 v[176:179], a[168:171], v[136:139], v[176:179]\n"
      "v_mfma_f32_16x16x32_bf16 v[180:183], a[168:171], v[140:143], v[180:183]\n"
      "v_mfma_f32_16x16x32_bf16 v[184:187], a[168:171], a[104:107], v[184:187]\n"
      "v_mfma_f32_16x16x32_bf16 v[188:191], a[168:171], a[108:111], v[188:191]\n"
      "v_mfma_f32_16x16x32_bf16 v[192:195], a[172:175], v[136:139], v[192:195]\n"
      "v_mfma_f32_16x16x32_bf16 v[196:199], a[172:175], v[140:143], v[196:199]\n"
      "v_mfma_f32_16x16x32_bf16 v[200:203], a[172:175], a[104:107], v[200:203]\n"
      "v_mfma_f32_16x16x32_bf16 v[204:207], a[172:175], a[108:111], v[204:207]\n"
      "s_waitcnt vmcnt(13)\n"
      "v_mfma_f32_16x16x32_bf16 v[176:179], a[176:179], v[144:147], v[176:179]\n"
      "v_mfma_f32_16x16x32_bf16 v[180:183], a[176:179], v[148:151], v[180:183]\n"
      "v_mfma_f32_16x16x32_bf16 v[184:187], a[176:179], a[112:115], v[184:187]\n"
      "v_mfma_f32_16x16x32_bf16 v[188:191], a[176:179], a[116:119], v[188:191]\n"
      "v_mfma_f32_16x16x32_bf16 v[192:195], a[180:183], v[144:147], v[192:195]\n"
      "v_mfma_f32_16x16x32_bf16 v[196:199], a[180:183], v[148:151], v[196:199]\n"
      "v_mfma_f32_16x16x32_bf16 v[200:203], a[180:183], a[112:115], v[200:203]\n"
      "v_mfma_f32_16x16x32_bf16 v[204:207], a[180:183], a[116:119], v[204:207]\n"
      "s_waitcnt vmcnt(11)\n"
      "v_mfma_f32_16x16x32_bf16 v[176:179], a[184:187], v[152:155], v[176:179]\n"
      "v_mfma_f32_16x16x32_bf16 v[180:183], a[184:187], v[156:159], v[180:183]\n"
      "v_mfma_f32_16x16x32_bf16 v[184:187], a[184:187], a[120:123], v[184:187]\n"
      "v_mfma_f32_16x16x32_bf16 v[188:191], a[184:187], a[124:127], v[188:191]\n"
      "v_mfma_f32_16x16x32_bf16 v[192:195], a[188:191], v[152:155], v[192:195]\n"
      "v_mfma_f32_16x16x32_bf16 v[196:199], a[188:191], v[156:159], v[196:199]\n"
      "v_mfma_f32_16x16x32_bf16 v[200:203], a[188:191], a[120:123], v[200:203]\n"
      "v_mfma_f32_16x16x32_bf16 v[204:207], a[188:191], a[124:127], v[204:207]\n"
      "s_waitcnt vmcnt(9)\n"
      "v_mfma_f32_16x16x32_bf16 v[176:179], a[192:195], v[160:163], v[176:179]\n"
      "v_mfma_f32_16x16x32_bf16 v[180:183], a[192:195], v[164:167], v[180:183]\n"
      "v_mfma_f32_16x16x32_bf16 v[184:187], a[192:195], a[128:131], v[184:187]\n"
      "v_mfma_f32_16x16x32_bf16 v[188:191], a[192:195], a[132:135], v[188:191]\n"
      "v_mfma_f32_16x16x32_bf16 v[192:195], a[196:199], v[160:163], v[192:195]\n"
      "v_mfma_f32_16x16x32_bf16 v[196:199], a[196:199], v[164:167], v[196:199]\n"
      "v_mfma_f32_16x16x32_bf16 v[200:203], a[196:199], a[128:131], v[200:203]\n"
      "v_mfma_f32_16x16x32_bf16 v[204:207], a[196:199], a[132:135], v[204:207]\n"
      "s_waitcnt vmcnt(7)\n"
      "v_mfma_f32_16x16x32_bf16 v[176:179], a[200:203], v[168:171], v[176:179]\n"
      "v_mfma_f32_16x16x32_bf16 v[180:183], a[200:203], v[172:175], v[180:183]\n"
      "v_mfma_f32_16x16x32_bf16 v[184:187], a[200:203], a[136:139], v[184:187]\n"
      "v_mfma_f32_16x16x32_bf16 v[188:191], a[200:203], a[140:143], v[188:191]\n"
      "v_mfma_f32_16x16x32_bf16 v[192:195], a[204:207], v[168:171], v[192:195]\n"
      "v_mfma_f32_16x16x32_bf16 v[196:199], a[204:207], v[172:175], v[196:199]\n"
      "v_mfma_f32_16x16x32_bf16 v[200:203], a[204:207], a[136:139], v[200:203]\n"
      "v_mfma_f32_16x16x32_bf16 v[204:207], a[204:207], a[140:143], v[204:207]\n"
      "s_nop 1\n"
      "s_cmp_eq_u32 %14, 1\n"
      "s_cbranch_scc1 .Lea%=\n"
      "s_waitcnt vmcnt(7)\n"
      ".Leb%=:\n"
      "s_barrier\n"
      "s_cmp_eq_u32 %14, 3\n"
      "s_cbranch_scc1 .Lsbo%=\n"
      ".Lsbr%=:\n"
      "s_nop 7\n"
      "v_mul_f32 v176, v25, v176\n"
      "v_mul_f32 v177, v25, v177\n"
      "v_mul_f32 v178, v25, v178\n"
      "v_mul_f32 v179, v25, v179\n"
      "v_mul_f32 v192, v25, v192\n"
      "v_mul_f32 v193, v25, v193\n"
      "v_mul_f32 v194, v25, v194\n"
      "v_mul_f32 v195, v25, v195\n"
      "v_cvt_pk_bf16_f32 v240, v176, v177\n"
      "v_cvt_pk_bf16_f32 v241, v178, v179\n"
      "v_cvt_pk_bf16_f32 v242, v192, v193\n"
      "v_cvt_pk_bf16_f32 v243, v194, v195\n"
      "s_cmp_eq_u32 s28, 0\n"
      "s_cbranch_scc1 .La15_%=\n"
      "global_store_dwordx4 %0, v[240:243], s[22:23] offset:0 sc1\n"
      ".Lb15_%=:\n"
      "v_mul_f32 v180, v27, v180\n"
      "v_mul_f32 v181, v27, v181\n"
      "v_mul_f32 v182, v27, v182\n"
      "v_mul_f32 v183, v27, v183\n"
      "v_mul_f32 v196, v27, v196\n"
      "v_mul_f32 v197, v27, v197\n"
      "v_mul_f32 v198, v27, v198\n"
      "v_mul_f32 v199, v27, v199\n"
      "v_cvt_pk_bf16_f32 v244, v180, v181\n"
      "v_cvt_pk_bf16_f32 v245, v182, v183\n"
      "v_cvt_pk_bf16_f32 v246, v196, v197\n"
      "v_cvt_pk_bf16_f32 v247, v198, v199\n"
      "s_cmp_eq_u32 s28, 0\n"
      "s_cbranch_scc1 .La16_%=\n"
      "global_store_dwordx4 %0, v[244:247], s[22:23] offset:1024 sc1\n"
      ".Lb16_%=:\n"
      "v_mul_f32 v184, v29, v184\n"
      "v_mul_f32 v185, v29, v185\n"
      "v_mul_f32 v186, v29, v186\n"
      "v_mul_f32 v187, v29, v187\n"
      "v_mul_f32 v200, v29, v200\n"
      "v_mul_f32 v201, v29, v201\n"
      "v_mul_f32 v202, v29, v202\n"
      "v_mul_f32 v203, v29, v203\n"
      "v_cvt_pk_bf16_f32 v248, v184, v185\n"
      "v_cvt_pk_bf16_f32 v249, v186, v187\n"
      "v_cvt_pk_bf16_f32 v250, v200, v201\n"
      "v_cvt_pk_bf16_f32 v251, v202, v203\n"
      "s_cmp_eq_u32 s28, 1\n"
      "s_cbranch_scc1 .La17_%=\n"
      "global_store_dwordx4 %0, v[248:251], s[22:23] offset:2048 sc1\n"
      ".Lb17_%=:\n"
      "v_mul_f32 v188, v31, v188\n"
      "v_mul_f32 v189, v31, v189\n"
      "v_mul_f32 v190, v31, v190\n"
      "v_mul_f32 v191, v31, v191\n"
      "v_mul_f32 v204, v31, v204\n"
      "v_mul_f32 v205, v31, v205\n"
      "v_mul_f32 v206, v31, v206\n"
      "v_mul_f32 v207, v31, v207\n"
      "v_cvt_pk_bf16_f32 v252, v188, v189\n"
      "v_cvt_pk_bf16_f32 v253, v190, v191\n"
      "v_cvt_pk_bf16_f32 v254, v204, v205\n"
      "v_cvt_pk_bf16_f32 v255, v206, v207\n"
      "s_cmp_eq_u32 s28, 1\n"
      "s_cbranch_scc1 .La18_%=\n"
      "global_store_dwordx4 %0, v[252:255], s[22:23] offset:3072 sc1\n"
      ".Lb18_%=:\n"
      "s_add_u32 s22, s22, 4096\n"
      "s_addc_u32 s23, s23, 0\n"
      "v_lshrrev_b32 v112, 4, %0\n"
      "v_lshrrev_b32 v113, 4, v112\n"
      "v_bfe_u32 v114, v112, 2, 2\n"
      "v_and_b32 v115, 3, v112\n"
      "v_and_b32 v116, 1, v114\n"
      "v_lshl_add_u32 v115, v116, 2, v115\n"
      "v_and_b32 v116, 1, v115\n"
      "v_lshlrev_b32 v116, 4, v116\n"
      "v_mov_b32 v117, 0x3f80\n"
      "v_lshlrev_b32 v116, v116, v117\n"
      "v_lshrrev_b32 v115, 1, v115\n"
      "v_and_b32 v114, 2, v114\n"
      "v_sub_u32 v114, v113, v114\n"
      "v_lshl_add_u32 v114, v114, 2, v115\n"
      "v_and_b32 v117, 15, v112\n"
      "v_mul_u32_u24 v128, 1152, v117\n"
      "v_lshl_add_u32 v128, v113, 4, v128\n"
      "v_cmp_eq_u32 vcc, 0, v114\n"
      "s_nop 1\n"
      "v_cndmask_b32 v120, 0, v116, vcc\n"
      "v_cmp_eq_u32 vcc, 1, v114\n"
      "s_nop 1\n"
      "v_cndmask_b32 v121, 0, v116, vcc\n"
      "v_cmp_eq_u32 vcc, 2, v114\n"
      "s_nop 1\n"
      "v_cndmask_b32 v122, 0, v116, vcc\n"
      "v_cmp_eq_u32 vcc, 3, v114\n"
      "s_nop 1\n"
      "v_cndmask_b32 v123, 0, v116, vcc\n"
      "v_cmp_eq_u32 vcc, 4, v114\n"
      "s_nop 1\n"
      "v_cndmask_b32 v124, 0, v116, vcc\n"
      "v_cmp_eq_u32 vcc, 5, v114\n"
      "s_nop 1\n"
      "v_cndmask_b32 v125, 0, v116, vcc\n"
      "v_cmp_eq_u32 vcc, 6, v114\n"
      "s_nop 1\n"
      "v_cndmask_b32 v126, 0, v116, vcc\n"
      "v_cmp_eq_u32 vcc, 7, v114\n"
      "s_nop 1\n"
      "v_cndmask_b32 v127, 0, v116, vcc\n"
      "s_add_u32 s24, %16, 8\n"
      "v_mov_b32 v38, %19\n"
      "v_mov_b32 v39, 1\n"
      "s_lshl_b32 s25, %14, 10\n"
      "v_add_u32 v130, s25, %9\n"
      "s_mul_i32 s25, %14, 3072\n"
      "s_add_u32 s25, s25, 9216\n"
      "v_add_u32 v131, s25, %9\n"
      "s_waitcnt vmcnt(0) lgkmcnt(0)\n"
      "s_barrier\n"
      "s_cmp_eq_u32 %14, 3\n"
      "s_cbranch_scc1 .Ltao%=\n"
      ".Ltar%=:\n"
      "ds_read_b128 v[68:71], v130 offset:0\n"
      "ds_read_b128 v[72:75], v131 offset:0\n"
      "s_waitcnt lgkmcnt(0)\n"
      "v_mfma_f32_16x16x32_bf16 v[160:163], v[120:123], v[68:71], 0\n"
      "v_mfma_f32_16x16x32_bf16 v[164:167], v[124:127], v[68:71], 0\n"
      "v_mfma_f32_16x16x32_bf16 v[160:163], v[120:123], v[72:75], v[160:163]\n"
      "v_mfma_f32_16x16x32_bf16 v[164:167], v[124:127], v[72:75], v[164:167]\n"
      "v_mfma_f32_16x16x32_bf16 v[160:163], v[120:123], v[40:43], v[160:163]\n"
      "v_mfma_f32_16x16x32_bf16 v[164:167], v[124:127], v[40:43], v[164:167]\n"
      "v_mfma_f32_16x16x32_bf16 v[160:163], v[120:123], v[44:47], v[160:163]\n"
      "v_mfma_f32_16x16x32_bf16 v[164:167], v[124:127], v[44:47], v[164:167]\n"
      "v_mfma_f32_16x16x32_bf16 v[160:163], v[120:123], v[48:51], v[160:163]\n"
      "v_mfma_f32_16x16x32_bf16 v[164:167], v[124:127], v[48:51], v[164:167]\n"
      "v_mfma_f32_16x16x32_bf16 v[160:163], v[120:123], v[52:55], v[160:163]\n"
      "v_mfma_f32_16x16x32_bf16 v[164:167], v[124:127], v[52:55], v[164:167]\n"
      "v_mfma_f32_16x16x32_bf16 v[160:163], v[120:123], v[56:59], v[160:163]\n"
      "v_mfma_f32_16x16x32_bf16 v[164:167], v[124:127], v[56:59], v[164:167]\n"
      "v_mfma_f32_16x16x32_bf16 v[160:163], v[120:123], v[60:63], v[160:163]\n"
      "v_mfma_f32_16x16x32_bf16 v[164:167], v[124:127], v[60:63], v[164:167]\n"
      "v_mfma_f32_16x16x32_bf16 v[160:163], v[120:123], v[64:67], v[160:163]\n"
      "v_mfma_f32_16x16x32_bf16 v[164:167], v[124:127], v[64:67], v[164:167]\n"
      "s_and_b32 s25, %16, 3\n"
      "s_mul_i32 s25, s25, 18432\n"
      "s_lshr_b32 s26, %16, 2\n"
      "s_lshl_b32 s26, s26, 6\n"
      "s_add_u32 s25, s25, s26\n"
      "v_add_u32 v129, s25, v128\n"
      "s_nop 1\n"
      "v_cvt_pk_bf16_f32 v192, v160, v161\n"
      "v_cvt_pk_bf16_f32 v193, v162, v163\n"
      "v_cvt_pk_bf16_f32 v194, v164, v165\n"
      "v_cvt_pk_bf16_f32 v195, v166, v167\n"
      "global_store_dwordx4 v129, v[192:195], %18\n"
      "s_cmp_lg_u32 %14, 1\n"
      "s_cbranch_scc1 .Ltb%=\n"
      "ds_read_b128 a[28:31], %9 offset:8192\n"
      "ds_read_b128 a[32:35], v131 offset:2048\n"
      "s_waitcnt lgkmcnt(0)\n"
      "v_mfma_f32_16x16x32_bf16 v[176:179], v[120:123], a[28:31], 0\n"
      "v_mfma_f32_16x16x32_bf16 v[180:183], v[124:127], a[28:31], 0\n"
      "v_mfma_f32_16x16x32_bf16 v[176:179], v[120:123], a[32:35], v[176:179]\n"
      "v_mfma_f32_16x16x32_bf16 v[180:183], v[124:127], a[32:35], v[180:183]\n"
      "v_mfma_f32_16x16x32_bf16 v[176:179], v[120:123], a[0:3], v[176:179]\n"
      "v_mfma_f32_16x16x32_bf16 v[180:183], v[124:127], a[0:3], v[180:183]\n"
      "v_mfma_f32_16x16x32_bf16 v[176:179], v[120:123], a[4:7], v[176:179]\n"
      "v_mfma_f32_16x16x32_bf16 v[180:183], v[124:127], a[4:7], v[180:183]\n"
      "v_mfma_f32_16x16x32_bf16 v[176:179], v[120:123], a[8:11], v[176:179]\n"
      "v_mfma_f32_16x16x32_bf16 v[180:183], v[124:127], a[8:11], v[180:183]\n"
      "v_mfma_f32_16x16x32_bf16 v[176:179], v[120:123], a[12:15], v[176:179]\n"
      "v_mfma_f32_16x16x32_bf16 v[180:183], v[124:127], a[12:15], v[180:183]\n"
      "v_mfma_f32_16x16x32_bf16 v[176:179], v[120:123], a[16:19], v[176:179]\n"
      "v_mfma_f32_16x16x32_bf16 v[180:183], v[124:127], a[16:19], v[180:183]\n"
      "v_mfma_f32_16x16x32_bf16 v[176:179], v[120:123], a[20:23], v[176:179]\n"
      "v_mfma_f32_16x16x32_bf16 v[180:183], v[124:127], a[20:23], v[180:183]\n"
      "v_mfma_f32_16x16x32_bf16 v[176:179], v[120:123], a[24:27], v[176:179]\n"
      "v_mfma_f32_16x16x32_bf16 v[180:183], v[124:127], a[24:27], v[180:183]\n"
      "s_and_b32 s25, %17, 3\n"
      "s_mul_i32 s25, s25, 18432\n"
      "s_lshr_b32 s26, %17, 2\n"
      "s_lshl_b32 s26, s26, 6\n"
      "s_add_u32 s25, s25, s26\n"
      "v_add_u32 v129, s25, v128\n"
      "s_nop 1\n"
      "v_cvt_pk_bf16_f32 v200, v176, v177\n"
      "v_cvt_pk_bf16_f32 v201, v178, v179\n"
      "v_cvt_pk_bf16_f32 v202, v180, v181\n"
      "v_cvt_pk_bf16_f32 v203, v182, v183\n"
      "global_store_dwordx4 v129, v[200:203], %18\n"
      ".Ltb%=:\n"
      "s_cmp_lg_u32 %14, 3\n"
      "s_cbranch_scc1 .Ltc%=\n"
      "s_mov_b32 s22, 1048576\n"
      "v_lshlrev_b32 v32, 3, %0\n"
      "v_and_b32 v32, 128, v32\n"
      ".Lp%=:\n"
      "global_load_dword v33, v32, %13 offset:256 sc1\n"
      "s_waitcnt vmcnt(0)\n"
      "v_readlane_b32 s23, v33, 1\n"
      "v_readlane_b32 s21, v33, 0\n"
      "s_sub_u32 s22, s22, 1\n"
      "s_cbranch_scc1 .Lq%=\n"
      "s_cmp_lt_u32 s23, 8\n"
      "s_cbranch_scc1 .Lr%=\n"
      "ds_write_b32 v38, v39\n"
      "s_cmp_ge_u32 s21, 8\n"
      "s_cbranch_scc1 .Lq%=\n"
      ".Lr%=:\n"
      "s_sleep 2\n"
      "s_branch .Lp%=\n"
      ".Lq%=:\n"
      "ds_write_b32 v38, v39\n"
      "ds_write_b32 v38, v39 offset:4\n"
      "s_branch .Lx%=\n"
      ".Ltc%=:\n"
      "s_and_b32 s20, %14, 2\n"
      "s_lshl_b32 s20, s20, 1\n"
      "v_add_u32 v38, s20, v38\n"
      "s_mov_b32 s22, 67108864\n"
      ".Ly%=:\n"
      "ds_read_b32 v39, v38\n"
      "s_waitcnt lgkmcnt(0)\n"
      "v_readfirstlane_b32 s23, v39\n"
      "s_sub_u32 s22, s22, 1\n"
      "s_cbranch_scc1 .Lx%=\n"
      "s_cmp_lg_u32 s23, 0\n"
      "s_cbranch_scc1 .Lx%=\n"
      "s_sleep 1\n"
      "s_branch .Ly%=\n"
      ".Lx%=:\n"
      "s_lshl_b32 s25, s24, 10\n"
      "v_add_u32 v35, s25, %0\n"
      "s_mov_b32 s30, %20\n"
      "s_add_u32 s30, s30, 1\n"
      "s_and_b32 s30, s30, 7\n"
      "s_mul_i32 s25, s30, 2359296\n"
      "s_mov_b64 s[26:27], %15\n"
      "s_add_u32 s26, s26, s25\n"
      "s_addc_u32 s27, s27, 0\n"
      "global_load_dwordx4 v[76:79], v35, s[26:27] sc1\n"
      "s_add_u32 s30, s30, 1\n"
      "s_and_b32 s30, s30, 7\n"
      "s_mul_i32 s25, s30, 2359296\n"
      "s_mov_b64 s[26:27], %15\n"
      "s_add_u32 s26, s26, s25\n"
      "s_addc_u32 s27, s27, 0\n"
      "global_load_dwordx4 v[80:83], v35, s[26:27] sc1\n"
      "s_add_u32 s30, s30, 1\n"
      "s_and_b32 s30, s30, 7\n"
      "s_mul_i32 s25, s30, 2359296\n"
      "s_mov_b64 s[26:27], %15\n"
      "s_add_u32 s26, s26, s25\n"
      "s_addc_u32 s27, s27, 0\n"
      "global_load_dwordx4 v[84:87], v35, s[26:27] sc1\n"
      "s_add_u32 s30, s30, 1\n"
      "s_and_b32 s30, s30, 7\n"
      "s_mul_i32 s25, s30, 2359296\n"
      "s_mov_b64 s[26:27], %15\n"
      "s_add_u32 s26, s26, s25\n"
      "s_addc_u32 s27, s27, 0\n"
      "global_load_dwordx4 v[88:91], v35, s[26:27] sc1\n"
      "s_add_u32 s30, s30, 1\n"
      "s_and_b32 s30, s30, 7\n"
      "s_mul_i32 s25, s30, 2359296\n"
      "s_mov_b64 s[26:27], %15\n"
      "s_add_u32 s26, s26, s25\n"
      "s_addc_u32 s27, s27, 0\n"
      "global_load_dwordx4 v[92:95], v35, s[26:27] sc1\n"
      "s_add_u32 s30, s30, 1\n"
      "s_and_b32 s30, s30, 7\n"
      "s_mul_i32 s25, s30, 2359296\n"
      "s_mov_b64 s[26:27], %15\n"
      "s_add_u32 s26, s26, s25\n"
      "s_addc_u32 s27, s27, 0\n"
      "global_load_dwordx4 v[96:99], v35, s[26:27] sc1\n"
      "s_add_u32 s30, s30, 1\n"
      "s_and_b32 s30, s30, 7\n"
      "s_mul_i32 s25, s30, 2359296\n"
      "s_mov_b64 s[26:27], %15\n"
      "s_add_u32 s26, s26, s25\n"
      "s_addc_u32 s27, s27, 0\n"
      "global_load_dwordx4 v[100:103], v35, s[26:27] sc1\n"
      "ds_read_b128 v[104:107], v130 offset:4096\n"
      "ds_read_b128 v[108:111], v131 offset:1024\n"
      "s_waitcnt lgkmcnt(0)\n"
      "v_mfma_f32_16x16x32_bf16 v[168:171], v[120:123], v[104:107], 0\n"
      "v_mfma_f32_16x16x32_bf16 v[172:175], v[124:127], v[104:107], 0\n"
      "v_mfma_f32_16x16x32_bf16 v[168:171], v[120:123], v[108:111], v[168:171]\n"
      "v_mfma_f32_16x16x32_bf16 v[172:175], v[124:127], v[108:111], v[172:175]\n"
      "s_waitcnt vmcnt(6)\n"
      "v_mfma_f32_16x16x32_bf16 v[168:171], v[120:123], v[76:79], v[168:171]\n"
      "v_mfma_f32_16x16x32_bf16 v[172:175], v[124:127], v[76:79], v[172:175]\n"
      "s_waitcnt vmcnt(5)\n"
      "v_mfma_f32_16x16x32_bf16 v[168:171], v[120:123], v[80:83], v[168:171]\n"
      "v_mfma_f32_16x16x32_bf16 v[172:175], v[124:127], v[80:83], v[172:175]\n"
      "s_waitcnt vmcnt(4)\n"
      "v_mfma_f32_16x16x32_bf16 v[168:171], v[120:123], v[84:87], v[168:171]\n"
      "v_mfma_f32_16x16x32_bf16 v[172:175], v[124:127], v[84:87], v[172:175]\n"
      "s_waitcnt vmcnt(3)\n"
      "v_mfma_f32_16x16x32_bf16 v[168:171], v[120:123], v[88:91], v[168:171]\n"
      "v_mfma_f32_16x16x32_bf16 v[172:175], v[124:127], v[88:91], v[172:175]\n"
      "s_waitcnt vmcnt(2)\n"
      "v_mfma_f32_16x16x32_bf16 v[168:171], v[120:123], v[92:95], v[168:171]\n"
      "v_mfma_f32_16x16x32_bf16 v[172:175], v[124:127], v[92:95], v[172:175]\n"
      "s_waitcnt vmcnt(1)\n"
      "v_mfma_f32_16x16x32_bf16 v[168:171], v[120:123], v[96:99], v[168:171]\n"
      "v_mfma_f32_16x16x32_bf16 v[172:175], v[124:127], v[96:99], v[172:175]\n"
      "s_waitcnt vmcnt(0)\n"
      "v_mfma_f32_16x16x32_bf16 v[168:171], v[120:123], v[100:103], v[168:171]\n"
      "v_mfma_f32_16x16x32_bf16 v[172:175], v[124:127], v[100:103], v[172:175]\n"
      "s_and_b32 s25, s24, 3\n"
      "s_mul_i32 s25, s25, 18432\n"
      "s_lshr_b32 s26, s24, 2\n"
      "s_lshl_b32 s26, s26, 6\n"
      "s_add_u32 s25, s25, s26\n"
      "v_add_u32 v129, s25, v128\n"
      "s_nop 1\n"
      "v_cvt_pk_bf16_f32 v196, v168, v169\n"
      "v_cvt_pk_bf16_f32 v197, v170, v171\n"
      "v_cvt_pk_bf16_f32 v198, v172, v173\n"
      "v_cvt_pk_bf16_f32 v199, v174, v175\n"
      "global_store_dwordx4 v129, v[196:199], %18\n"
      "s_branch .Lzz%=\n"
      ".La1_%=:\n"
      "ds_write_b128 %9, v[240:243] offset:0\n"
      "s_branch .Lb1_%=\n"
      ".La2_%=:\n"
      "ds_write_b128 %9, v[244:247] offset:1024\n"
      "s_branch .Lb2_%=\n"
      ".La3_%=:\n"
      "ds_write_b128 %9, v[248:251] offset:0\n"
      "s_branch .Lb3_%=\n"
      ".La4_%=:\n"
      "ds_write_b128 %9, v[252:255] offset:1024\n"
      "s_branch .Lb4_%=\n"
      ".La5_%=:\n"
      "ds_write_b128 %9, v[248:251] offset:8192\n"
      "s_branch .Lb5_%=\n"
      ".La6_%=:\n"
      "ds_write_b128 %9, v[252:255] offset:8192\n"
      "s_branch .Lb6_%=\n"
      ".La7_%=:\n"
      "ds_write_b128 %9, v[240:243] offset:2048\n"
      "s_branch .Lb7_%=\n"
      ".La8_%=:\n"
      "ds_write_b128 %9, v[244:247] offset:3072\n"
      "s_branch .Lb8_%=\n"
      ".La9_%=:\n"
      "ds_write_b128 %9, v[248:251] offset:2048\n"
      "s_branch .Lb9_%=\n"
      ".La10_%=:\n"
      "ds_write_b128 %9, v[252:255] offset:3072\n"
      "s_branch .Lb10_%=\n"
      ".Lsao%=:\n"
      "s_mov_b64 exec, 1\n"
      "v_mov_b32 v240, 0\n"
      "v_mov_b32 v241, 1\n"
      "global_atomic_add v240, v241, %13 offset:128 sc1\n"
      "s_mov_b64 exec, -1\n"
      "s_branch .Lsar%=\n"
      ".La11_%=:\n"
      "ds_write_b128 %9, v[240:243] offset:4096\n"
      "s_branch .Lb11_%=\n"
      ".Lpio%=:\n"
      "v_mov_b32 v32, 0\n"
      "global_load_dword v33, v32, %13 offset:128 sc1\n"
      "s_branch .Lpir%=\n"
      ".La12_%=:\n"
      "ds_write_b128 %9, v[244:247] offset:5120\n"
      "s_branch .Lb12_%=\n"
      ".La13_%=:\n"
      "ds_write_b128 %9, v[248:251] offset:4096\n"
      "s_branch .Lb13_%=\n"
      ".La14_%=:\n"
      "ds_write_b128 %9, v[252:255] offset:5120\n"
      "s_branch .Lb14_%=\n"
      ".Lpco%=:\n"
      "s_waitcnt vmcnt(4)\n"
      "v_readfirstlane_b32 s25, v33\n"
      "s_nop 0\n"
      "s_cmp_ge_u32 s25, 8\n"
      "s_cbranch_scc1 .Lpag%=\n"
      "s_mov_b32 s24, 1048576\n"
      "v_mov_b32 v32, 0\n"
      ".Lpap%=:\n"
      "global_load_dword v33, v32, %13 offset:128 sc1\n"
      "s_waitcnt vmcnt(0)\n"
      "v_readfirstlane_b32 s25, v33\n"
      "s_sub_u32 s24, s24, 1\n"
      "s_cbranch_scc1 .Lpag%=\n"
      "s_cmp_ge_u32 s25, 8\n"
      "s_cbranch_scc1 .Lpag%=\n"
      "s_sleep 2\n"
      "s_branch .Lpap%=\n"
      ".Lpag%=:\n"
      "s_branch .Lpcr%=\n"
      ".Lea%=:\n"
      "s_lshl_b32 s25, %17, 10\n"
      "v_add_u32 v36, s25, %0\n"
      "s_mov_b32 s30, %20\n"
      "s_add_u32 s30, s30, 1\n"
      "s_and_b32 s30, s30, 7\n"
      "s_mul_i32 s25, s30, 2359296\n"
      "s_mov_b64 s[26:27], %15\n"
      "s_add_u32 s26, s26, s25\n"
      "s_addc_u32 s27, s27, 0\n"
      "global_load_dwordx4 a[0:3], v36, s[26:27] sc1\n"
      "s_add_u32 s30, s30, 1\n"
      "s_and_b32 s30, s30, 7\n"
      "s_mul_i32 s25, s30, 2359296\n"
      "s_mov_b64 s[26:27], %15\n"
      "s_add_u32 s26, s26, s25\n"
      "s_addc_u32 s27, s27, 0\n"
      "global_load_dwordx4 a[4:7], v36, s[26:27] sc1\n"
      "s_add_u32 s30, s30, 1\n"
      "s_and_b32 s30, s30, 7\n"
      "s_mul_i32 s25, s30, 2359296\n"
      "s_mov_b64 s[26:27], %15\n"
      "s_add_u32 s26, s26, s25\n"
      "s_addc_u32 s27, s27, 0\n"
      "global_load_dwordx4 a[8:11], v36, s[26:27] sc1\n"
      "s_add_u32 s30, s30, 1\n"
      "s_and_b32 s30, s30, 7\n"
      "s_mul_i32 s25, s30, 2359296\n"
      "s_mov_b64 s[26:27], %15\n"
      "s_add_u32 s26, s26, s25\n"
      "s_addc_u32 s27, s27, 0\n"
      "global_load_dwordx4 a[12:15], v36, s[26:27] sc1\n"
      "s_add_u32 s30, s30, 1\n"
      "s_and_b32 s30, s30, 7\n"
      "s_mul_i32 s25, s30, 2359296\n"
      "s_mov_b64 s[26:27], %15\n"
      "s_add_u32 s26, s26, s25\n"
      "s_addc_u32 s27, s27, 0\n"
      "global_load_dwordx4 a[16:19], v36, s[26:27] sc1\n"
      "s_add_u32 s30, s30, 1\n"
      "s_and_b32 s30, s30, 7\n"
      "s_mul_i32 s25, s30, 2359296\n"
      "s_mov_b64 s[26:27], %15\n"
      "s_add_u32 s26, s26, s25\n"
      "s_addc_u32 s27, s27, 0\n"
      "global_load_dwordx4 a[20:23], v36, s[26:27] sc1\n"
      "s_add_u32 s30, s30, 1\n"
      "s_and_b32 s30, s30, 7\n"
      "s_mul_i32 s25, s30, 2359296\n"
      "s_mov_b64 s[26:27], %15\n"
      "s_add_u32 s26, s26, s25\n"
      "s_addc_u32 s27, s27, 0\n"
      "global_load_dwordx4 a[24:27], v36, s[26:27] sc1\n"
      "s_waitcnt vmcnt(14)\n"
      "s_branch .Leb%=\n"
      ".Lsbo%=:\n"
      "s_mov_b64 exec, 1\n"
      "v_mov_b32 v32, 0\n"
      "v_mov_b32 v33, 1\n"
      "global_atomic_add v32, v33, %13 offset:384 sc1\n"
      "s_mov_b64 exec, -1\n"
      "s_branch .Lsbr%=\n"
      ".La15_%=:\n"
      "ds_write_b128 %9, v[240:243] offset:6144\n"
      "s_branch .Lb15_%=\n"
      ".La16_%=:\n"
      "ds_write_b128 %9, v[244:247] offset:7168\n"
      "s_branch .Lb16_%=\n"
      ".La17_%=:\n"
      "ds_write_b128 %9, v[248:251] offset:6144\n"
      "s_branch .Lb17_%=\n"
      ".La18_%=:\n"
      "ds_write_b128 %9, v[252:255] offset:7168\n"
      "s_branch .Lb18_%=\n"
      ".Ltao%=:\n"
      "s_mov_b64 exec, 1\n"
      "v_mov_b32 v32, 0\n"
      "v_mov_b32 v33, 1\n"
      "global_atomic_add v32, v33, %13 offset:256 sc1\n"
      "s_mov_b64 exec, -1\n"
      "s_branch .Ltar%=\n"
      ".Lzz%=:\n"
      :
      : "v"(lane16), "v"(la), "v"(la + 36864), "v"(la + w * 16384), "v"(la + (16 + (w >> 1)) * 4096 + par * 8),
        "v"(la + K4_EX + (w >> 1) * 4096 + par * 8), "v"(la + K4_EX + (w >> 1) * 4096 + par * 2048), "v"(lds0 + K4_CS + w * 640 + g * 32),
        "v"(lds0 + K4_MS + t16 * 8), "v"(la + K4_OWN),
        "s"(wstream), "s"(part + w * 16384), "s"(part + (16 + (w >> 1)) * 4096 + par * 2048), "s"(cnt), "s"(w),
        "s"(ws + PART_OFF + (size_t)mg * 73728), "s"(qa), "s"(64 + fs), "s"((char*)out + (size_t)mg * 64 * (D * 2)), "s"(lds0 + K4_GO), "s"(fs), "s"(hf), "v"(la + K4_PF + w * 13312)
      : "memory", "scc", "vcc", "s20", "s21", "s22", "s23", "s24", "s25", "s26", "s27", "s28", "s29", "s30", "s31", "v24","v25","v26","v27","v28","v29","v30","v31","v32","v33","v34","v35","v36","v37","v38","v39","v40","v41","v42","v43","v44","v45","v46","v47","v48","v49","v50","v51","v52","v53","v54","v55","v56","v57","v58","v59","v60","v61","v62","v63","v64","v65","v66","v67","v68","v69","v70","v71","v72","v73","v74","v75","v76","v77","v78","v79","v80","v81","v82","v83","v84","v85","v86","v87","v88","v89","v90","v91","v92","v93","v94","v95","v96","v97","v98","v99","v100","v101","v102","v103","v104","v105","v106","v107","v108","v109","v110","v111","v112","v113","v114","v115","v116","v117","v118","v119","v120","v121","v122","v123","v124","v125","v126","v127","v128","v129","v130","v131","v132","v133","v134","v135","v136","v137","v138","v139","v140","v141","v142","v143","v144","v145","v146","v147","v148","v149","v150","v151","v152","v153","v154","v155","v156","v157","v158","v159","v160","v161","v162","v163","v164","v165","v166","v167","v168","v169","v170","v171","v172","v173","v174","v175","v176","v177","v178","v179","v180","v181","v182","v183","v184","v185","v186","v187","v188","v189","v190","v191","v192","v193","v194","v195","v196","v197","v198","v199","v200","v201","v202","v203","v204","v205","v206","v207","v208","v209","v210","v211","v212","v213","v214","v215","v216","v217","v218","v219","v220","v221","v222","v223","v224","v225","v226","v227","v228","v229","v230","v231","v232","v233","v234","v235","v236","v237","v238","v239","v240","v241","v242","v243","v244","v245","v246","v247","v248","v249","v250","v251","v252","v253","v254","v255", "a0","a1","a2","a3","a4","a5","a6","a7","a8","a9","a10","a11","a12","a13","a14","a15","a16","a17","a18","a19","a20","a21","a22","a23","a24","a25","a26","a27","a28","a29","a30","a31","a32","a33","a34","a35","a36","a37","a38","a39","a40","a41","a42","a43","a44","a45","a46","a47","a48","a49","a50","a51","a52","a53","a54","a55","a56","a57","a58","a59","a60","a61","a62","a63","a64","a65","a66","a67","a68","a69","a70","a71","a72","a73","a74","a75","a76","a77","a78","a79","a80","a81","a82","a83","a84","a85","a86","a87","a88","a89","a90","a91","a92","a93","a94","a95","a96","a97","a98","a99","a100","a101","a102","a103","a104","a105","a106","a107","a108","a109","a110","a111","a112","a113","a114","a115","a116","a117","a118","a119","a120","a121","a122","a123","a124","a125","a126","a127","a128","a129","a130","a131","a132","a133","a134","a135","a136","a137","a138","a139","a140","a141","a142","a143","a144","a145","a146","a147","a148","a149","a150","a151","a152","a153","a154","a155","a156","a157","a158","a159","a160","a161","a162","a163","a164","a165","a166","a167","a168","a169","a170","a171","a172","a173","a174","a175","a176","a177","a178","a179","a180","a181","a182","a183","a184","a185","a186","a187","a188","a189","a190","a191","a192","a193","a194","a195","a196","a197","a198","a199","a200","a201","a202","a203","a204","a205","a206","a207","a208","a209","a210","a211","a212","a213","a214","a215","a216","a217","a218","a219","a220","a221","a222","a223","a224","a225","a226","a227","a228","a229","a230","a231","a232","a233","a234","a235","a236","a237","a238","a239","a240","a241","a242","a243","a244","a245","a246","a247","a248","a249","a250","a251","a252","a253","a254","a255");
}

extern "C" void kernel_launch(void* const* d_in, const int* in_sizes, int n_in, void* d_out, int out_size, void* d_ws, size_t ws_size,
                              hipStream_t stream) {
  const u16* x = (const u16*)d_in[0]; const u16* wq = (const u16*)d_in[1]; const u16* wk = (const u16*)d_in[2];
  const u16* wv = (const u16*)d_in[3]; const u16* wo = (const u16*)d_in[4];
  const u16* w1 = (const u16*)d_in[6]; const u16* w2 = (const u16*)d_in[7];
  char* ws = (char*)d_ws;
  (void)hipFuncSetAttribute((const void*)ka_attn, hipFuncAttributeMaxDynamicSharedMemorySize, KA_LDS);
  (void)hipFuncSetAttribute((const void*)kb_ffn, hipFuncAttributeMaxDynamicSharedMemorySize, K4_LDS);
  ka_attn<<<256, 768, KA_LDS, stream>>>(x, wq, wk, wv, wo, w1, w2, ws);
  kb_ffn<<<256, 256, K4_LDS, stream>>>(x, ws, (u16*)d_out);
}
```
